```python
import torch

def _softmax(x, axis=-1):
    return torch.softmax(x, dim=axis)
def _gelu(x):
    return torch.nn.functional.gelu(x, approximate="tanh")
def _silu(x):
    return torch.nn.functional.silu(x)
def _sigmoid(x):
    return torch.sigmoid(x)
def _layernorm(x, axis=-1, eps=1e-5):
    m = x.mean(dim=axis, keepdim=True)
    v = x.var(dim=axis, keepdim=True, unbiased=False)
    return (x - m) / torch.sqrt(v + eps)
def _scatter_add(tgt, idx, src, axis=0):
    return tgt.clone().index_add_(axis, idx, src)
def _segment_sum(src, seg, n):
    out = torch.zeros((n,) + src.shape[1:], dtype=src.dtype)
    return out.index_add_(0, seg, src)
def _pad(x, pw):
    flat = tuple(v for pair in reversed(pw) for v in pair)
    return torch.nn.functional.pad(x, flat)

kernel_name = 'interleave_layers__linear_attn_block__ssm_conv_s4__prompt'

def setup_inputs():

    _w = (lambda a: (lambda t: t.to(torch.bfloat16) if t.is_floating_point() else t)(torch.as_tensor(a)))
    def _A_setup():
        B, S, D = 1, 2048, 512
        return {
            "x": _w((lambda *s: torch.randn(*s, dtype=torch.bfloat16) * 0.05)(B, S, D)),
            "wqkv": _w((lambda *s: torch.randn(*s, dtype=torch.bfloat16) * 0.05)(D, 3 * D)),
            "wo": _w((lambda *s: torch.randn(*s, dtype=torch.bfloat16) * 0.05)(D, D)),
        }
    _A_in = _A_setup()

    def _B_setup():
        B, L, D, N = 1, 2048, 512, 128
        A_re = -(lambda _x: torch.exp(torch.as_tensor(_x)))((lambda *s: torch.randn(*s, dtype=torch.bfloat16) * 1.6)(D, N))
        A_im = torch.pi * (lambda *s: torch.randn(*s, dtype=torch.bfloat16) * 0.1)(D, N)
        B_re = (lambda *s: torch.randn(*s, dtype=torch.bfloat16) * 0.1)(D, N)
        B_im = (lambda *s: torch.randn(*s, dtype=torch.bfloat16) * 0.1)(D, N)
        C_re = (lambda *s: torch.randn(*s, dtype=torch.bfloat16) * 0.1)(D, N)
        C_im = (lambda *s: torch.randn(*s, dtype=torch.bfloat16) * 0.1)(D, N)
        log_dt = (lambda *s: torch.randn(*s, dtype=torch.bfloat16) * 0.1)(D) * 0.5 - 4.0
        Dskip = (lambda *s: torch.randn(*s, dtype=torch.bfloat16) * 0.1)(D)
        return {
            "u": _w((lambda *s: torch.randn(*s, dtype=torch.bfloat16) * 0.05)(B, L, D)),
            "A_re": _w(A_re),
            "A_im": _w(A_im),
            "B_re": _w(B_re),
            "B_im": _w(B_im),
            "C_re": _w(C_re),
            "C_im": _w(C_im),
            "log_dt": _w(log_dt),
            "Dskip": _w(Dskip),
        }
    _B_in = _B_setup()

    return {
        "A_x": _A_in["x"],
        "A_wqkv": _A_in["wqkv"],
        "A_wo": _A_in["wo"],
        "B_u": _B_in["u"],
        "B_A_re": _B_in["A_re"],
        "B_A_im": _B_in["A_im"],
        "B_B_re": _B_in["B_re"],
        "B_B_im": _B_in["B_im"],
        "B_C_re": _B_in["C_re"],
        "B_C_im": _B_in["C_im"],
        "B_log_dt": _B_in["log_dt"],
        "B_Dskip": _B_in["Dskip"],
    }

def reference(A_x, A_wqkv, A_wo, B_u, B_A_re, B_A_im, B_B_re, B_B_im, B_C_re, B_C_im, B_log_dt, B_Dskip):
    def _A_ref(x, wqkv, wo):
        B, S, D = x.shape
        H = 32; Dh = D // H
        qkv = (lambda x, *d: x.permute(*d))((x @ wqkv).reshape(B, S, 3, H, Dh), 2, 0, 3, 1, 4)
        q, k, v = qkv[0], qkv[1], qkv[2]
        phi = lambda t: torch.where(t > 0, t + 1, torch.exp(t)) + 1e-6
        pq, pk = phi(q), phi(k)
        kv = torch.einsum("bhsd,bhse->bhde", pk, v)
        z = torch.einsum("bhsd->bhd", pk)
        num = torch.einsum("bhqd,bhde->bhqe", pq, kv)
        den = torch.einsum("bhqd,bhd->bhq", pq, z)[..., None] + 1e-6
        o = num / den
        return (lambda x, *d: x.permute(*d))(o, 0, 2, 1, 3).reshape(B, S, D) @ wo

    def _B_ref(u, A_re, A_im, B_re, B_im, C_re, C_im, log_dt, Dskip):
        Bsz, L, D = u.shape
        dt = torch.exp(log_dt)[:, None]
        A = A_re + 1j * A_im
        Bc = B_re + 1j * B_im
        Cc = C_re + 1j * C_im
        logdA = dt * A
        dB = dt * Bc
        l = (lambda x, dt: x.to(dt))(torch.arange(L), A_re.dtype)
        P = torch.exp(logdA[:, :, None] * l[None, None, :])
        K = torch.einsum("dn,dnl,dn->dl", Cc, P, dB).real
        L2 = 2 * L
        Kp = _pad(K, ((0, 0), (0, L)))
        up = (lambda x, *d: x.permute(*d))(u, 0, 2, 1)
        up = _pad(up, ((0, 0), (0, 0), (0, L)))
        Kf = (lambda x, axes=(-2,-1): torch.fft.rfft2(x.float(), dim=axes))(Kp[:, None, :], axes=(-2, -1))
        Uf = (lambda x, axes=(-2,-1): torch.fft.rfft2(x.float(), dim=axes))(up[:, :, None, :], axes=(-2, -1))
        Yf = Uf * Kf[None, :, :, :]
        y = (lambda x, s, axes=(-2,-1): torch.fft.irfft2(x, s=s, dim=axes).to(torch.bfloat16))(Yf, (1, L2), axes=(-2, -1))[:, :, 0, :L]
        y = (lambda x, *d: x.permute(*d))(y, 0, 2, 1) + u * Dskip[None, None, :]
        return y

    x = A_x
    x = x + _A_ref(_layernorm(x, axis=-1), A_wqkv, A_wo)
    x = x + _B_ref(_layernorm(x, axis=-1), B_A_re, B_A_im, B_B_re, B_B_im, B_C_re, B_C_im, B_log_dt, B_Dskip)
    x = x + _A_ref(_layernorm(x, axis=-1), A_wqkv, A_wo)
    x = x + _B_ref(_layernorm(x, axis=-1), B_A_re, B_A_im, B_B_re, B_B_im, B_C_re, B_C_im, B_log_dt, B_Dskip)
    return x
```

```cpp
#include <hip/hip_runtime.h>
#include <hip/hip_bf16.h>
#include <stdint.h>
typedef unsigned short u16;
#define S_ 2048
#define D_ 512
#define H_ 32
#define N_ 128
__device__ __forceinline__ float bf2f(u16 v){ return __uint_as_float(((unsigned)v)<<16); }
__device__ __forceinline__ u16 f2bf(float f){ unsigned u=__float_as_uint(f); u += 0x7FFF + ((u>>16)&1); return (u16)(u>>16);}
__device__ __forceinline__ float rb(float f){ return bf2f(f2bf(f)); }

__global__ void k_ln(const u16* x, u16* h){
    int r = blockIdx.x; int t = threadIdx.x;
    __shared__ float red[256];
    float a = bf2f(x[r*D_+t]), b = bf2f(x[r*D_+t+256]);
    red[t]=a+b; __syncthreads();
    for(int s=128;s>0;s>>=1){ if(t<s) red[t]+=red[t+s]; __syncthreads(); }
    float mean = red[0]/D_; __syncthreads();
    float da=a-mean, db=b-mean;
    red[t]=da*da+db*db; __syncthreads();
    for(int s=128;s>0;s>>=1){ if(t<s) red[t]+=red[t+s]; __syncthreads(); }
    float var = red[0]/D_;
    float m = rb(mean), v = rb(var);
    float sd = rb(sqrtf(rb(v+1e-5f)));
    h[r*D_+t] = f2bf(rb(a-m)/sd);
    h[r*D_+t+256] = f2bf(rb(b-m)/sd);
}
__global__ void k_gemm(const u16* A, const u16* B, u16* C, const u16* R, int M, int N, int K){
    int n = blockIdx.x*blockDim.x+threadIdx.x; int m = blockIdx.y;
    float acc=0;
    for(int k=0;k<K;++k) acc += bf2f(A[m*K+k])*bf2f(B[k*N+n]);
    float o = rb(acc);
    if(R) o = rb(o + bf2f(R[m*N+n]));
    C[m*N+n]=f2bf(o);
}
__device__ __forceinline__ float phi(float t){
    float r = t>0.f ? rb(t+1.f) : rb(expf(t));
    return rb(r+1e-6f);
}
__global__ void k_kv(const u16* qkv, float* kv, float* z){
    int h = blockIdx.x; int d = threadIdx.x>>4, e = threadIdx.x&15;
    float acc=0, zz=0;
    for(int s=0;s<S_;++s){
        float pk = phi(bf2f(qkv[s*1536+512+h*16+d]));
        float v = bf2f(qkv[s*1536+1024+h*16+e]);
        acc += pk*v; zz += pk;
    }
    kv[h*256+d*16+e]=rb(acc);
    if(e==0) z[h*16+d]=rb(zz);
}
__global__ void k_o(const u16* qkv, const float* kv, const float* z, u16* o){
    int s = blockIdx.x; int c = threadIdx.x;
    int h=c>>4, e=c&15;
    float num=0, den=0;
    for(int d=0;d<16;++d){
        float pq = phi(bf2f(qkv[s*1536+h*16+d]));
        num += pq*kv[h*256+d*16+e]; den += pq*z[h*16+d];
    }
    num = rb(num); den = rb(rb(den)+1e-6f);
    o[s*D_+c]=f2bf(num/den);
}
__global__ void k_kgen(const u16* Are,const u16* Aim,const u16* Bre,const u16* Bim,const u16* Cre,const u16* Cim,const u16* logdt,float* K){
    int d = blockIdx.x;
    __shared__ float ar[N_], ai[N_], wr[N_], wi[N_];
    float dt = rb(expf(bf2f(logdt[d])));
    if(threadIdx.x<N_){
        int n=threadIdx.x;
        ar[n]=dt*bf2f(Are[d*N_+n]); ai[n]=dt*bf2f(Aim[d*N_+n]);
        float br=dt*bf2f(Bre[d*N_+n]), bi=dt*bf2f(Bim[d*N_+n]);
        float cr=bf2f(Cre[d*N_+n]), ci=bf2f(Cim[d*N_+n]);
        wr[n]=cr*br-ci*bi; wi[n]=cr*bi+ci*br;
    }
    __syncthreads();
    for(int l=threadIdx.x;l<S_;l+=blockDim.x){
        float lf = rb((float)l);
        float acc=0;
        for(int n=0;n<N_;++n){
            float e = expf(ar[n]*lf); float s,c; sincosf(ai[n]*lf,&s,&c);
            acc += e*(wr[n]*c - wi[n]*s);
        }
        K[d*S_+l]=acc;
    }
}
__global__ void k_conv(const u16* u, const u16* x, const float* K, const u16* Dskip, u16* xo){
    int d = blockIdx.x;
    __shared__ float su[S_], sk[S_];
    for(int t=threadIdx.x;t<S_;t+=blockDim.x){ su[t]=bf2f(u[t*D_+d]); sk[t]=K[d*S_+t]; }
    __syncthreads();
    float Dd = bf2f(Dskip[d]);
    for(int t=threadIdx.x;t<S_;t+=blockDim.x){
        float acc=0;
        for(int l=0;l<=t;++l) acc += sk[l]*su[t-l];
        float y = rb(rb(acc) + rb(su[t]*Dd));
        xo[t*D_+d]=f2bf(bf2f(x[t*D_+d])+y);
    }
}
extern "C" void kernel_launch(void* const* d_in, const int* in_sizes, int n_in,
                              void* d_out, int out_size, void* d_ws, size_t ws_size,
                              hipStream_t stream) {
    const u16* x0=(const u16*)d_in[0]; const u16* wqkv=(const u16*)d_in[1]; const u16* wo=(const u16*)d_in[2];
    const u16* Are=(const u16*)d_in[4]; const u16* Aim=(const u16*)d_in[5];
    const u16* Bre=(const u16*)d_in[6]; const u16* Bim=(const u16*)d_in[7];
    const u16* Cre=(const u16*)d_in[8]; const u16* Cim=(const u16*)d_in[9];
    const u16* logdt=(const u16*)d_in[10]; const u16* Dskip=(const u16*)d_in[11];
    char* ws=(char*)d_ws;
    u16* h=(u16*)(ws); u16* qkv=(u16*)(ws+(2<<20)); float* kv=(float*)(ws+(10<<20)); float* z=(float*)(ws+(11<<20));
    u16* o=(u16*)(ws+(12<<20)); u16* x1=(u16*)(ws+(14<<20)); u16* x2=(u16*)(ws+(16<<20)); u16* x3=(u16*)(ws+(18<<20));
    float* K=(float*)(ws+(20<<20));
    u16* out=(u16*)d_out;
    k_kgen<<<D_,256,0,stream>>>(Are,Aim,Bre,Bim,Cre,Cim,logdt,K);
    const u16* xin=x0;
    for(int layer=0;layer<2;++layer){
        u16* xa = layer==0? x1: x3; u16* xb = layer==0? x2: out;
        k_ln<<<S_,256,0,stream>>>(xin,h);
        k_gemm<<<dim3(1536/256,S_),256,0,stream>>>(h,wqkv,qkv,nullptr,S_,1536,512);
        k_kv<<<H_,256,0,stream>>>(qkv,kv,z);
        k_o<<<S_,512,0,stream>>>(qkv,kv,z,o);
        k_gemm<<<dim3(512/256,S_),256,0,stream>>>(o,wo,xa,xin,S_,512,512);
        k_ln<<<S_,256,0,stream>>>(xa,h);
        k_conv<<<D_,256,0,stream>>>(h,xa,K,Dskip,xb);
        xin=xb;
    }
}
```

```cpp
#include <hip/hip_runtime.h>
#include <stdint.h>

typedef unsigned v4u __attribute__((ext_vector_type(4)));
typedef unsigned v2u __attribute__((ext_vector_type(2)));
typedef float f32x2 __attribute__((ext_vector_type(2)));
typedef float f32x4 __attribute__((ext_vector_type(4)));
typedef float f32x16 __attribute__((ext_vector_type(16)));
typedef __bf16 bf16x8 __attribute__((ext_vector_type(8)));
typedef __bf16 bf16x2 __attribute__((ext_vector_type(2)));
typedef short s16x4 __attribute__((ext_vector_type(4)));
typedef __amdgpu_buffer_rsrc_t rsrc_t;
#define LDSP __attribute__((address_space(3)))
#define DI __device__ __forceinline__
typedef LDSP unsigned char* ldsp_t;

constexpr unsigned WS_EPOCH = 0;
constexpr unsigned WS_RDY = 4096;
constexpr unsigned WS_KVR = 65536;
constexpr unsigned WS_ZP = 131072;
constexpr unsigned WS_STX = 262144;
constexpr unsigned WS_WOF = 1u << 20;
constexpr unsigned WS_KVP = 2u << 20;
constexpr unsigned WS_Q = 4u << 20;
constexpr unsigned WS_X1 = 8u << 20;
constexpr unsigned WS_U = 12u << 20;
constexpr unsigned WS_XT = 16u << 20;
constexpr unsigned WS_DBG = 20u << 20;
constexpr unsigned WS_SIZE = 24u << 20;
constexpr unsigned L_XS = 0;
constexpr unsigned L_RING = 65536;
constexpr unsigned L_KC = 65536;
constexpr unsigned L_KG = 98816;
constexpr unsigned KG_CH = 53 * 528;
constexpr unsigned L_KD = 154784;
constexpr unsigned L_OF = 0;
constexpr unsigned L_UL = 32768;
constexpr unsigned L_EX = 41984;
constexpr unsigned SPIN_CAP = 1u << 15;
constexpr unsigned POIS = 0xFFFFFFFFu;
constexpr float LOG2E = 1.4426950408889634f;

DI f32x16 mfma32(v4u a, v4u b, f32x16 c) { return __builtin_amdgcn_mfma_f32_32x32x16_bf16(__builtin_bit_cast(bf16x8, a), __builtin_bit_cast(bf16x8, b), c, 0, 0, 0); }
DI f32x4 mfma16(v4u a, v4u b, f32x4 c) { return __builtin_amdgcn_mfma_f32_16x16x32_bf16(__builtin_bit_cast(bf16x8, a), __builtin_bit_cast(bf16x8, b), c, 0, 0, 0); }
DI unsigned pk2(float lo, float hi) { f32x2 v = {lo, hi}; bf16x2 r = __builtin_convertvector(v, bf16x2); return __builtin_bit_cast(unsigned, r); }
DI float bflo(unsigned x) { return __uint_as_float(x << 16); }
DI float bfhi(unsigned x) { return __uint_as_float(x & 0xFFFF0000u); }
DI float rbf(float x) { return bflo(pk2(x, x)); }
DI unsigned guard(unsigned x) { return x < 0xFFFFFFFEu ? x : 0xFFFFFFFEu; }
DI float dot2(unsigned a, unsigned b, float c) { return __builtin_amdgcn_fdot2_f32_bf16(__builtin_bit_cast(bf16x2, a), __builtin_bit_cast(bf16x2, b), c, false); }
DI v4u lds128(ldsp_t p, unsigned off) { return *(LDSP v4u*)(p + off); }
DI v2u lds64(ldsp_t p, unsigned off) { return *(LDSP v2u*)(p + off); }
DI v2u tr64(ldsp_t p, unsigned off) { return __builtin_bit_cast(v2u, __builtin_amdgcn_ds_read_tr16_b64_v4i16((LDSP s16x4*)(p + off))); }
DI v4u tr2(ldsp_t p, unsigned o0, unsigned o1) { v2u a = tr64(p, o0), b = tr64(p, o1); return v4u{a.x, a.y, b.x, b.y}; }
DI void dma16(unsigned ldsaddr, unsigned voff, rsrc_t rs, unsigned soff) {
    asm volatile("s_mov_b32 m0, %0\n\ts_nop 0\n\tbuffer_load_dwordx4 %1, %2, %3 offen lds" ::"s"(ldsaddr), "v"(voff), "s"(rs), "s"(soff) : "m0", "memory");
}
#define WAITVM_BAR(N) asm volatile("s_waitcnt vmcnt(" #N ") lgkmcnt(0)\n\ts_barrier" ::: "memory")
#define BAR() asm volatile("s_waitcnt lgkmcnt(0)\n\ts_barrier" ::: "memory")
#define WAITVM(N) asm volatile("s_waitcnt vmcnt(" #N ")" ::: "memory")
DI v4u ld128(rsrc_t rs, unsigned voff, unsigned soff) { return __builtin_amdgcn_raw_buffer_load_b128(rs, voff, soff, 16); }
DI v2u ld64(rsrc_t rs, unsigned voff, unsigned soff) { return __builtin_amdgcn_raw_buffer_load_b64(rs, voff, soff, 16); }
DI unsigned ld32(rsrc_t rs, unsigned voff, unsigned soff) { return __builtin_amdgcn_raw_buffer_load_b32(rs, voff, soff, 16); }
DI void st128(v4u v, rsrc_t rs, unsigned voff, unsigned soff) { __builtin_amdgcn_raw_buffer_store_b128(v, rs, voff, soff, 16); }
DI void st64(v2u v, rsrc_t rs, unsigned voff, unsigned soff) { __builtin_amdgcn_raw_buffer_store_b64(v, rs, voff, soff, 16); }
DI void st32(unsigned v, rsrc_t rs, unsigned voff, unsigned soff) { __builtin_amdgcn_raw_buffer_store_b32(v, rs, voff, soff, 16); }
DI bool ok4(v4u v) { return (v.x != POIS) & (v.y != POIS) & (v.z != POIS) & (v.w != POIS); }
DI bool ok2(v2u v) { return (v.x != POIS) & (v.y != POIS); }
DI bool wall(bool p) { return __builtin_amdgcn_ballot_w64(!p) == 0ull; }
DI float phi_u(float u) {
    float e = __builtin_amdgcn_fmed3f(__builtin_amdgcn_exp2f(u), 0.f, 1.f);
    return __builtin_fmaf(__builtin_fmaxf(u, 1e-6f * LOG2E), 1.0f / LOG2E, e);
}
DI void ln_stats(float sx, float sxx, float& m, float& rs) {
    float mean = sx * (1.0f / 512.0f);
    float var = __builtin_fmaxf(__builtin_fmaf(-mean, mean, sxx * (1.0f / 512.0f)), 0.f);
    m = rbf(mean);
    float sd = rbf(__builtin_sqrtf(rbf(rbf(var) + 1e-5f)));
    rs = 1.0f / sd;
}
template <int CTRL> DI float dpp_f(float x) { return __uint_as_float(__builtin_amdgcn_update_dpp(0u, __float_as_uint(x), CTRL, 0xF, 0xF, true)); }
template <int CTRL> DI unsigned dpp_u(unsigned x) { return __builtin_amdgcn_update_dpp(0u, x, CTRL, 0xF, 0xF, true); }
DI float xsum16(float x) { auto s = __builtin_amdgcn_permlane16_swap(__float_as_uint(x), __float_as_uint(x), false, false); return __uint_as_float(s[0]) + __uint_as_float(s[1]); }
DI float xsum32(float x) { auto s = __builtin_amdgcn_permlane32_swap(__float_as_uint(x), __float_as_uint(x), false, false); return __uint_as_float(s[0]) + __uint_as_float(s[1]); }

#define TS(i)

struct Cx { float re, im; };
DI Cx cmul(Cx a, Cx b) { return Cx{__builtin_fmaf(a.re, b.re, -(a.im * b.im)), __builtin_fmaf(a.re, b.im, a.im * b.re)}; }
DI Cx cexp_at(float ar2, float ai_rev, float x) {
    float mag = __builtin_amdgcn_exp2f(ar2 * x);
    float r = __builtin_amdgcn_fractf(ai_rev * x);
    return Cx{mag * __builtin_amdgcn_cosf(r), mag * __builtin_amdgcn_sinf(r)};
}

DI void poison_kvq(rsrc_t ws, int L, int tt, int g, int w, int lane, int b) {
    const v4u P = {POIS, POIS, POIS, POIS};
    const int h = 4 * g + w, gq = lane >> 4, e = lane & 15;
    st128(P, ws, (unsigned)((4 * (gq >> 1) + (e >> 2)) * 4096 + 32 * (e & 3) + 16 * (gq & 1)), WS_KVP + L * (1u << 20) + (unsigned)(h * 8 * 32 + tt) * 128);
    if (e < 2) st64(v2u{POIS, POIS}, ws, (unsigned)((2 * gq + e) * 256), WS_ZP + L * 65536 + (unsigned)(h * 8 * 32 + tt) * 8);
    st128(P, ws, (unsigned)lane * 16, WS_Q + L * (2u << 20) + (unsigned)((2 * tt) * 32 + h) * 1024);
    st128(P, ws, (unsigned)lane * 16, WS_Q + L * (2u << 20) + (unsigned)((2 * tt + 1) * 32 + h) * 1024);
    if (w == 0 && lane < 9) {
        if (lane < 8) st64(v2u{POIS, POIS}, ws, (unsigned)lane * 8, WS_KVR + L * 20480 + (unsigned)(b >> 3) * 640 + (unsigned)(b & 7) * 64);
        else st32(POIS, ws, 0, WS_KVR + L * 20480 + (unsigned)(b >> 3) * 640 + 512 + (unsigned)(b & 7) * 4);
    }
}
DI void poison_ux(rsrc_t ws, int L, int rt, int cb, int w, int lane, int tid, int b) {
    const v4u P = {POIS, POIS, POIS, POIS};
    st32(POIS, ws, (unsigned)lane * 4, WS_STX + L * 262144 + (unsigned)(rt * 16 + cb * 4 + w) * 256);
#pragma unroll
    for (int x = 0; x < 2; ++x)
        st128(P, ws, (unsigned)(((lane >> 3) + 8 * x) * 8192 + (lane & 7) * 16), WS_U + L * (2u << 20) + (unsigned)(cb * 64 + w * 16) * 8192 + (unsigned)rt * 128);
#pragma unroll
    for (int x = 0; x < 2; ++x)
        st128(P, ws, (unsigned)((tid >> 3) * 65536 + (tid & 7) * 16), WS_XT + L * (2u << 20) + (unsigned)(2 * b + x) * 128);
}

template <int L> struct LC { static constexpr int v = L; };

__global__ void __launch_bounds__(256, 1) fused_kernel(
    const uint16_t* __restrict__ x, const uint16_t* __restrict__ wqkv, const uint16_t* __restrict__ wo,
    const uint16_t* __restrict__ pAre, const uint16_t* __restrict__ pAim, const uint16_t* __restrict__ pBre, const uint16_t* __restrict__ pBim,
    const uint16_t* __restrict__ pCre, const uint16_t* __restrict__ pCim, const uint16_t* __restrict__ pLdt, const uint16_t* __restrict__ pDsk,
    uint16_t* __restrict__ out, unsigned char* __restrict__ wsp) {
    __shared__ __attribute__((aligned(128))) unsigned char lds_raw[163840];
    const ldsp_t lds = (ldsp_t)lds_raw;
    const int tid = threadIdx.x, lane = tid & 63;
    const int w = __builtin_amdgcn_readfirstlane(tid >> 6);
    const int b = blockIdx.x, xcd = b & 7, ix = b >> 3;
    const int tt = (xcd >> 1) * 8 + (ix >> 2), g = (xcd & 1) * 4 + (ix & 3);
    const int rt = 2 * tt + (xcd & 1), cb = ix & 3;
    const int rot = ((ix >> 2) + 2 * (ix & 3)) & 7;
    const int gq = lane >> 4, l15 = lane & 15, l31 = lane & 31, hf = lane >> 5;
    TS(0);
    const rsrc_t rsX = __builtin_amdgcn_make_buffer_rsrc((void*)x, 0, 2048 * 512 * 2, 0x00020000);
    const rsrc_t rsW = __builtin_amdgcn_make_buffer_rsrc((void*)wqkv, 0, 512 * 1536 * 2, 0x00020000);
    const rsrc_t rsWo = __builtin_amdgcn_make_buffer_rsrc((void*)wo, 0, 512 * 512 * 2, 0x00020000);
    const rsrc_t ws = __builtin_amdgcn_make_buffer_rsrc((void*)wsp, 0, WS_SIZE, 0x00020000);
    const rsrc_t rsO = __builtin_amdgcn_make_buffer_rsrc((void*)out, 0, 2048 * 512 * 2, 0x00020000);

    unsigned tag = ld32(ws, 0, WS_EPOCH);
    const int kch = 2 * b + (w >> 1), kn = (w & 1) * 64 + lane;
    unsigned rAre = pAre[kch * 128 + kn], rAim = pAim[kch * 128 + kn], rBre = pBre[kch * 128 + kn], rBim = pBim[kch * 128 + kn],
             rCre = pCre[kch * 128 + kn], rCim = pCim[kch * 128 + kn], rLdt = pLdt[kch + (lane & 0)], rDsk = pDsk[kch + (lane & 0)];
    asm volatile("" ::: "memory");
    dma16(L_XS + 7 * 8192 + w * 1024, (unsigned)((lane >> 2) * 1024 + (lane & 3) * 16), rsWo,
          (unsigned)((32 * (b & 15) + 16 * (w & 1)) * 1024 + (b >> 4) * 64));
    const unsigned vx0 = (unsigned)((lane >> 3) * 1024 + (((lane & 7) ^ ((lane >> 4) & 7)) * 16));
    const unsigned vx1 = (unsigned)((lane >> 3) * 1024 + (((lane & 7) ^ ((4 + (lane >> 4)) & 7)) * 16));
    unsigned vkv, vq0, vq1;
    {
        const int up = l15 >> 1, half = lane & 1, unit = up ^ gq;
        vkv = (unsigned)(gq * 3072 + ((unit & 4) ? 2048 : 1024) + 128 * g + (unit & 3) * 32 + half * 16);
        const int uq = (lane & 7) >> 1, f0 = (lane >> 4) & 1;
        vq0 = (unsigned)((lane >> 3) * 3072 + 128 * g + (uq ^ f0) * 32 + half * 16);
        vq1 = (unsigned)((lane >> 3) * 3072 + 128 * g + (uq ^ (f0 | 2)) * 32 + half * 16);
    }
    auto dma_x = [&](int c) {
        const int kc = (c + rot) & 7;
        dma16(L_XS + c * 8192 + (2 * w) * 1024, vx0, rsX, (unsigned)((64 * tt + 16 * w) * 1024 + kc * 128));
        dma16(L_XS + c * 8192 + (2 * w + 1) * 1024, vx1, rsX, (unsigned)((64 * tt + 16 * w + 8) * 1024 + kc * 128));
    };
    auto dma_kv = [&](int c, int slot) {
        const int kc = (c + rot) & 7;
#pragma unroll
        for (int y = 0; y < 4; ++y) dma16(L_RING + slot * 16384 + (4 * w + y) * 1024, vkv, rsW, (unsigned)((64 * kc + 4 * (4 * w + y)) * 3072));
    };
    auto dma_q = [&](int n, int slot) {
        const int kc = (2 * n + (w >> 1) + rot) & 7;
#pragma unroll
        for (int y = 0; y < 4; ++y) dma16(L_RING + slot * 16384 + (4 * w + y) * 1024, (y & 1) ? vq1 : vq0, rsW, (unsigned)((64 * kc + 8 * ((4 * w + y) & 7)) * 3072));
    };
#pragma unroll
    for (int c = 0; c < 6; ++c) { dma_x(c); dma_kv(c, c); }
    dma_x(6);
    poison_kvq(ws, 0, tt, g, w, lane, b);
    TS(1);

    v4u Wkv[32], Wq[16], WO[32];
    float csk, csv, csq[4];
    const v4u ONES = {0x3F803F80u, 0x3F803F80u, 0x3F803F80u, 0x3F803F80u};

    const unsigned a_row0 = (unsigned)(l31 * 128 + 0), swz0 = (unsigned)((l31 >> 1) & 7);
    const unsigned q4 = (unsigned)(l15 >> 2), p4 = (unsigned)(lane & 3);

    auto kv_epilogue = [&](auto Lc, f32x16 acc0, f32x16 acc1, float s1a, float s2a, float s1b, float s2b, unsigned SA) {
        constexpr int L = decltype(Lc)::v;
        *(LDSP f32x2*)(lds + SA + ((w * 2 + hf) * 64 + l31) * 8) = f32x2{s1a, s2a};
        *(LDSP f32x2*)(lds + SA + ((w * 2 + hf) * 64 + 32 + l31) * 8) = f32x2{s1b, s2b};
        BAR();
        float sx = 0.f, sxx = 0.f;
#pragma unroll
        for (int i = 0; i < 8; ++i) { f32x2 v = *(LDSP f32x2*)(lds + SA + (i * 64 + lane) * 8); sx += v.x; sxx += v.y; }
        float m, rs; ln_stats(sx, sxx, m, rs);
        const unsigned MS = SA + 4096 + w * 512;
        *(LDSP float*)(lds + MS + lane * 4) = m;
        *(LDSP float*)(lds + MS + 256 + lane * 4) = rs;
        asm volatile("s_waitcnt lgkmcnt(0)" ::: "memory");
        const unsigned tb = (unsigned)(32 * (gq & 1) + 4 * (gq >> 1));
        unsigned pk[8], vv[8];
#pragma unroll
        for (int q = 0; q < 4; ++q) {
            f32x4 m4 = *(LDSP f32x4*)(lds + MS + (tb + 8 * q) * 4), r4 = *(LDSP f32x4*)(lds + MS + 256 + (tb + 8 * q) * 4);
            float kk[4], vx[4];
#pragma unroll
            for (int r = 0; r < 4; ++r) {
                auto s = __builtin_amdgcn_permlane16_swap(__float_as_uint(acc0[4 * q + r]), __float_as_uint(acc1[4 * q + r]), false, false);
                float kraw = __uint_as_float(s[0]), vraw = __uint_as_float(s[1]);
                kk[r] = phi_u(__builtin_fmaf(-m4[r], csk, kraw) * (r4[r] * LOG2E));
                vx[r] = __builtin_fmaf(-m4[r], csv, vraw) * r4[r];
            }
            pk[2 * q] = pk2(kk[0], kk[1]); pk[2 * q + 1] = pk2(kk[2], kk[3]);
            vv[2 * q] = pk2(vx[0], vx[1]); vv[2 * q + 1] = pk2(vx[2], vx[3]);
        }
        f32x4 kv = {0, 0, 0, 0}, zz = {0, 0, 0, 0};
        kv = mfma16(v4u{pk[0], pk[1], pk[2], pk[3]}, v4u{vv[0], vv[1], vv[2], vv[3]}, kv);
        zz = mfma16(v4u{pk[0], pk[1], pk[2], pk[3]}, ONES, zz);
        kv = mfma16(v4u{pk[4], pk[5], pk[6], pk[7]}, v4u{vv[4], vv[5], vv[6], vv[7]}, kv);
        zz = mfma16(v4u{pk[4], pk[5], pk[6], pk[7]}, ONES, zz);
        const int h = 4 * g + w;
        WAITVM(0);
        st128(v4u{guard(__float_as_uint(kv[0])), guard(__float_as_uint(kv[1])), guard(__float_as_uint(kv[2])), guard(__float_as_uint(kv[3]))}, ws,
              (unsigned)((4 * (gq >> 1) + (l15 >> 2)) * 4096 + 32 * (l15 & 3) + 16 * (gq & 1)), WS_KVP + L * (1u << 20) + (unsigned)(h * 8 * 32 + tt) * 128);
        if (l15 < 2) {
            float z0 = (l15 & 1) ? zz[2] : zz[0], z1 = (l15 & 1) ? zz[3] : zz[1];
            st64(v2u{guard(__float_as_uint(z0)), guard(__float_as_uint(z1))}, ws, (unsigned)((2 * gq + l15) * 256), WS_ZP + L * 65536 + (unsigned)(h * 8 * 32 + tt) * 8);
        }
    };
    auto q_epilogue = [&](auto Lc, f32x4 (&aq)[4], unsigned SA) {
        constexpr int L = decltype(Lc)::v;
        const unsigned MS = SA + 4096 + w * 512;
        unsigned a[4][2];
#pragma unroll
        for (int tq = 0; tq < 4; ++tq) {
            const float m = *(LDSP float*)(lds + MS + (16 * tq + l15) * 4), rs = *(LDSP float*)(lds + MS + 256 + (16 * tq + l15) * 4) * LOG2E;
            float v[4];
#pragma unroll
            for (int i = 0; i < 4; ++i) v[i] = phi_u(__builtin_fmaf(-m, csq[i], aq[tq][i]) * rs);
            a[tq][0] = pk2(v[0], v[1]); a[tq][1] = pk2(v[2], v[3]);
        }
#pragma unroll
        for (int xh = 0; xh < 2; ++xh) {
            auto s0 = __builtin_amdgcn_permlane16_swap(a[2 * xh][0], a[2 * xh + 1][0], false, false);
            auto s1 = __builtin_amdgcn_permlane16_swap(a[2 * xh][1], a[2 * xh + 1][1], false, false);
            st128(v4u{guard(s0[0]), guard(s1[0]), guard(s0[1]), guard(s1[1])}, ws, (unsigned)lane * 16,
                  WS_Q + L * (2u << 20) + (unsigned)((2 * tt + xh) * 32 + 4 * g + w) * 1024);
        }
    };

    v2u resid[4];
    {
        constexpr unsigned SA = L_RING;
        f32x16 acc0, acc1;
#pragma unroll
        for (int i = 0; i < 16; ++i) { acc0[i] = 0.f; acc1[i] = 0.f; }
        f32x4 accs = {0, 0, 0, 0};
        float s1a = 0, s2a = 0, s1b = 0, s2b = 0;
        const unsigned selv = ((gq & 1) == (lane & 1)) ? 0x3F803F80u : 0u;
        const v4u SEL = {selv, selv, selv, selv};
        const unsigned wkv_lane = (unsigned)((8 * (gq >> 1) + q4) * 256 + ((((gq & 1) * 4 + w) ^ q4) * 32) + p4 * 8);
        auto chunk = [&](auto Cc) {
            constexpr int c = decltype(Cc)::v;
            const unsigned slot = L_RING + (c % 6) * 16384;
#pragma unroll
            for (int ks = 0; ks < 4; ++ks) {
                Wkv[4 * c + ks] = tr2(lds, slot + ks * 4096 + wkv_lane, slot + ks * 4096 + 1024 + wkv_lane);
                const unsigned u = (unsigned)(2 * ks + hf);
                v4u a0 = lds128(lds, L_XS + c * 8192 + a_row0 + ((u ^ swz0) * 16));
                v4u a1 = lds128(lds, L_XS + c * 8192 + 4096 + a_row0 + ((u ^ swz0) * 16));
                acc0 = mfma32(a0, Wkv[4 * c + ks], acc0);
                acc1 = mfma32(a1, Wkv[4 * c + ks], acc1);
                accs = mfma16(SEL, Wkv[4 * c + ks], accs);
            }
            const unsigned u = (unsigned)(2 * w + hf);
            v4u f0 = lds128(lds, L_XS + c * 8192 + a_row0 + ((u ^ swz0) * 16));
            v4u f1 = lds128(lds, L_XS + c * 8192 + 4096 + a_row0 + ((u ^ swz0) * 16));
#pragma unroll
            for (int i = 0; i < 4; ++i) {
                s1a = dot2(f0[i], 0x3F803F80u, s1a); s2a = dot2(f0[i], f0[i], s2a);
                s1b = dot2(f1[i], 0x3F803F80u, s1b); s2b = dot2(f1[i], f1[i], s2b);
            }
        };
        WAITVM_BAR(36);
        {
            const unsigned base = L_XS + 7 * 8192 + w * 1024 + (4 * (gq >> 1) + q4) * 64 + ((gq & 1) * 16 + p4 * 4) * 2;
            v4u f = tr2(lds, base, base + 8 * 64);
            st128(f, ws, (unsigned)lane * 16, WS_WOF + (unsigned)(((b >> 4) * 32 + 2 * (b & 15) + (w & 1)) * 1024));
        }
        chunk(LC<0>{});
        WAITVM_BAR(31);
        dma_x(7); dma_kv(6, 0);
        chunk(LC<1>{});
        WAITVM_BAR(31);
        dma_kv(7, 1);
        chunk(LC<2>{});
        WAITVM_BAR(29);
        dma_q(0, 2);
        chunk(LC<3>{});
        WAITVM_BAR(27);
        dma_q(1, 3);
        chunk(LC<4>{});
        WAITVM_BAR(18);
        asm volatile("" : "+v"(tag));
        tag += 1u;
        if (tid == 0) st32(tag, ws, 0, WS_RDY + (unsigned)b * 4);
        dma_q(2, 4);
        chunk(LC<5>{});
        WAITVM_BAR(16);
        dma_q(3, 5);
        chunk(LC<6>{});
        WAITVM_BAR(16);
        poison_ux(ws, 0, rt, cb, w, lane, tid, b);
        chunk(LC<7>{});
        csk = accs[0]; csv = accs[1];
        TS(2);
        kv_epilogue(LC<0>{}, acc0, acc1, s1a, s2a, s1b, s2b, SA);
        TS(3);
        v4u rdy = ld128(ws, (unsigned)lane * 16, WS_RDY);
        BAR();
        f32x4 aq[4], aqs = {0, 0, 0, 0};
#pragma unroll
        for (int i = 0; i < 4; ++i) aq[i] = f32x4{0, 0, 0, 0};
        const unsigned fq = (q4 >> 1) | ((gq & 1) << 1);
        const unsigned wq_lane = (unsigned)((8 * gq + q4) * 128 + ((w ^ fq) * 32) + p4 * 8);
        const unsigned xq_row = (unsigned)(l15 * 128), xq_swz = (unsigned)((l15 >> 1) & 7);
#pragma unroll
        for (int k2 = 0; k2 < 16; ++k2) {
            const unsigned slot = L_RING + (2 + (k2 >> 2)) * 16384 + (k2 & 3) * 4096;
            Wq[k2] = tr2(lds, slot + wq_lane, slot + 512 + wq_lane);
            aqs = mfma16(Wq[k2], ONES, aqs);
            const unsigned u = (unsigned)(4 * (k2 & 1) + gq);
#pragma unroll
            for (int tq = 0; tq < 4; ++tq) {
                v4u xf = lds128(lds, L_XS + (k2 >> 1) * 8192 + tq * 2048 + xq_row + ((u ^ xq_swz) * 16));
                aq[tq] = mfma16(Wq[k2], xf, aq[tq]);
            }
        }
#pragma unroll
        for (int i = 0; i < 4; ++i) csq[i] = aqs[i];
        q_epilogue(LC<0>{}, aq, SA);
        {
            const int tl = 32 * (rt & 1) + l31;
#pragma unroll
            for (int q = 0; q < 4; ++q) {
                const int kc = 2 * cb + (w >> 1), pos = (kc - rot) & 7, u = 4 * (w & 1) + q;
                resid[q] = lds64(lds, (unsigned)(L_XS + pos * 8192 + tl * 128 + ((u ^ ((tl >> 1) & 7)) * 16) + 8 * hf));
            }
        }
        if (w == 0) {
            bool ok = (rdy.x == tag) & (rdy.y == tag) & (rdy.z == tag) & (rdy.w == tag);
            for (unsigned s = 0; !wall(ok) && s < SPIN_CAP; ++s) {
            asm volatile("" ::: "memory");
                __builtin_amdgcn_s_sleep(2);
                rdy = ld128(ws, (unsigned)lane * 16, WS_RDY);
                ok = (rdy.x == tag) & (rdy.y == tag) & (rdy.z == tag) & (rdy.w == tag);
            }
        }
        BAR();
        TS(4);
    }

    auto red_issue = [&](auto Lc, v4u (&kp)[4], unsigned& zp) {
        constexpr int L = decltype(Lc)::v;
#pragma unroll
        for (int r = 0; r < 4; ++r) kp[r] = ld128(ws, (unsigned)lane * 16, WS_KVP + L * (1u << 20) + (unsigned)b * 4096 + r * 1024);
        zp = ld32(ws, (unsigned)lane * 4, WS_ZP + L * 65536 + (unsigned)b * 256);
    };
    auto red_finish = [&](auto Lc, v4u (&kp)[4], unsigned& zp) {
        constexpr int L = decltype(Lc)::v;
        bool ok = ok4(kp[0]) & ok4(kp[1]) & ok4(kp[2]) & ok4(kp[3]) & (zp != POIS);
        for (unsigned s = 0; !wall(ok) && s < SPIN_CAP; ++s) {
            asm volatile("" ::: "memory");
            red_issue(Lc, kp, zp);
            ok = ok4(kp[0]) & ok4(kp[1]) & ok4(kp[2]) & ok4(kp[3]) & (zp != POIS);
        }
        float v[4];
#pragma unroll
        for (int i = 0; i < 4; ++i) {
            float a = (__uint_as_float(kp[0][i]) + __uint_as_float(kp[1][i])) + (__uint_as_float(kp[2][i]) + __uint_as_float(kp[3][i]));
            a += dpp_f<0x128>(a);
            a = xsum16(a); a = xsum32(a);
            v[i] = a;
        }
        float z = __uint_as_float(zp);
        z += dpp_f<0x4E>(z);
        z += dpp_f<0x124>(z);
        z += dpp_f<0x128>(z);
        z = xsum16(z); z = xsum32(z);
        const float zo = dpp_f<0xB1>(z);
        const unsigned base = WS_KVR + L * 20480 + (unsigned)(b >> 3) * 640;
        if (lane < 8) st64(v2u{guard(pk2(v[0], v[1])), guard(pk2(v[2], v[3]))}, ws, (unsigned)lane * 8, base + (unsigned)(b & 7) * 64);
        if (lane == 0) st32(guard(pk2(z, zo)), ws, 0, base + 512 + (unsigned)(b & 7) * 4);
    };

    asm volatile("" : "+v"(rAre), "+v"(rAim), "+v"(rBre), "+v"(rBim), "+v"(rCre), "+v"(rCim), "+v"(rLdt), "+v"(rDsk));
    const float dsk = bflo(rDsk);
    auto kgen1 = [&](int part, Cx& wv, float& ar2, float& air) {
        const unsigned XA = L_KG + (w >> 1) * KG_CH + kn * 4, YB = XA + 21 * 528;
        auto putx = [&](int ci, Cx v) { *(LDSP unsigned*)(lds + XA + ci * 528) = pk2(v.re, -v.im); };
        if (part == 0) {
            const float dt = rbf(expf(bflo(rLdt)));
            const float ar = dt * bflo(rAre), ai = dt * bflo(rAim);
            ar2 = ar * LOG2E; air = ai * 0.15915494309189535f;
            const Cx Bd = {dt * bflo(rBre), dt * bflo(rBim)}, Cc = {bflo(rCre), bflo(rCim)};
            wv = cmul(Cc, Bd);
            const Cx e1 = cexp_at(ar2, air, 1.f), e2 = cmul(e1, e1), e4 = cmul(e2, e2), E = cexp_at(ar2, air, 256.f);
            Cx v = wv; putx(0, v);
#pragma unroll
            for (int c = 1; c < 8; ++c) { v = cmul(v, e1); putx(c, v); }
            v = cmul(wv, E); putx(8, v);
#pragma unroll
            for (int c = 9; c < 12; ++c) { v = cmul(v, e2); putx(c, v); }
            const Cx E2 = cmul(E, E), E3 = cmul(E2, E), E4 = cmul(E2, E2);
            v = cmul(wv, E2); putx(12, v); v = cmul(v, e4); putx(13, v);
            v = cmul(wv, E3); putx(14, v); v = cmul(v, e4); putx(15, v);
            v = cmul(wv, E4); putx(16, v);
#pragma unroll
            for (int c = 17; c < 21; ++c) { v = cmul(v, E); putx(c, v); }
        } else {
            const Cx e8 = cexp_at(ar2, air, 8.f), e16 = cmul(e8, e8);
            Cx ya = {1.f, 0.f}, yb = e8;
#pragma unroll
            for (int j = 0; j < 32; j += 2) {
                *(LDSP unsigned*)(lds + YB + j * 528) = pk2(ya.re, ya.im);
                *(LDSP unsigned*)(lds + YB + (j + 1) * 528) = pk2(yb.re, yb.im);
                if (j < 30) { ya = cmul(ya, e16); yb = cmul(yb, e16); }
            }
        }
    };
    auto kgen2 = [&]() {
        const unsigned XA = L_KG + (w >> 1) * KG_CH, YB = XA + 21 * 528;
        const int fh = w & 1;
        f32x4 d0 = {0, 0, 0, 0}, d1 = {0, 0, 0, 0};
#pragma unroll
        for (int ks = 0; ks < 8; ++ks) {
            v4u bf = lds128(lds, YB + (16 * fh + l15) * 528 + ks * 64 + gq * 16);
            v4u a0 = lds128(lds, XA + l15 * 528 + ks * 64 + gq * 16);
            v4u a1 = lds128(lds, XA + (16 + l15) * 528 + ks * 64 + gq * 16);
            d0 = mfma16(a0, bf, d0); d1 = mfma16(a1, bf, d1);
        }
        const int f = 16 * fh + l15;
        const unsigned KD = L_KD + (w >> 1) * 1296;
#pragma unroll
        for (int r = 0; r < 4; ++r) {
            const int i0 = gq < 2 ? 4 * gq + r + 8 * f : (gq == 2 ? 256 + r + 4 * f : 384 + 64 * (r >> 1) + (r & 1) + 2 * f);
            *(LDSP uint16_t*)(lds + KD + i0 * 2) = (uint16_t)pk2(d0[r], d0[r]);
            if (gq == 0) *(LDSP uint16_t*)(lds + KD + (512 + 32 * r + f) * 2) = (uint16_t)pk2(d1[r], d1[r]);
        }
        if (gq == 1 && f == 0) { *(LDSP uint16_t*)(lds + KD + 640 * 2) = (uint16_t)pk2(d1[0], d1[0]); *(LDSP uint16_t*)(lds + KD + 641 * 2) = 0; }
    };
    auto kgen3 = [&]() {
        for (int gi = tid; gi < 520; gi += 256) {
            const int ch = gi >= 260, i0 = (gi - 260 * ch) * 8;
            unsigned v[11];
#pragma unroll
            for (int j = 0; j < 11; ++j) {
                const int l = i0 + j - 32;
                const int lc = l > 2047 ? 2047 : l;
                const int ib = (int)(pk2((float)lc, 0.f) & 0xFFFFu) - 0x4380 + 256;
                const int idx = l < 0 ? 641 : (lc < 256 ? lc : ib);
                v[j] = *(LDSP uint16_t*)(lds + L_KD + ch * 1296 + idx * 2);
            }
#pragma unroll
            for (int s = 0; s < 4; ++s)
                *(LDSP v4u*)(lds + L_KC + ch * 16640 + s * 4160 + i0 * 2) =
                    v4u{v[s] | (v[s + 1] << 16), v[s + 2] | (v[s + 3] << 16), v[s + 4] | (v[s + 5] << 16), v[s + 6] | (v[s + 7] << 16)};
        }
    };

    auto phaseA2 = [&](auto Lc) {
        constexpr int L = decltype(Lc)::v;
        v4u qf[8], kf[8];
        const unsigned qbase = WS_Q + L * (2u << 20) + (unsigned)(rt * 32 + 8 * w) * 1024;
        const unsigned kvoff = (l31 < 16) ? (unsigned)((hf * 16 + l31) * 16) : (unsigned)(512 + hf * 16);
        const unsigned kbase = WS_KVR + L * 20480 + (unsigned)(8 * w) * 640;
#pragma unroll
        for (int j = 0; j < 8; ++j) qf[j] = ld128(ws, (unsigned)lane * 16, qbase + j * 1024);
#pragma unroll
        for (int j = 0; j < 8; ++j) kf[j] = ld128(ws, kvoff, kbase + j * 640);
        {
            bool ok = true;
#pragma unroll
            for (int j = 0; j < 8; ++j) ok &= ok4(qf[j]);
            for (unsigned s = 0; !wall(ok) && s < SPIN_CAP; ++s) {
            asm volatile("" ::: "memory");
                ok = true;
#pragma unroll
                for (int j = 0; j < 8; ++j) qf[j] = ld128(ws, (unsigned)lane * 16, qbase + j * 1024);
#pragma unroll
                for (int j = 0; j < 8; ++j) ok &= ok4(qf[j]);
            }
            ok = true;
#pragma unroll
            for (int j = 0; j < 8; ++j) ok &= ok4(kf[j]);
            for (unsigned s = 0; !wall(ok) && s < SPIN_CAP; ++s) {
            asm volatile("" ::: "memory");
                ok = true;
#pragma unroll
                for (int j = 0; j < 8; ++j) kf[j] = ld128(ws, kvoff, kbase + j * 640);
#pragma unroll
                for (int j = 0; j < 8; ++j) ok &= ok4(kf[j]);
            }
        }
        TS(6 + 10 * L);
#pragma unroll
        for (int j = 0; j < 8; ++j) {
            f32x16 d;
#pragma unroll
            for (int i = 0; i < 16; ++i) d[i] = 0.f;
            d = mfma32(kf[j], qf[j], d);
            const float rc = __builtin_amdgcn_rcpf(d[8] + 1e-6f);
            *(LDSP v4u*)(lds + L_OF + (8 * w + j) * 1024 + lane * 16) =
                v4u{pk2(d[0] * rc, d[1] * rc), pk2(d[2] * rc, d[3] * rc), pk2(d[4] * rc, d[5] * rc), pk2(d[6] * rc, d[7] * rc)};
        }
        BAR();
        f32x16 acc;
#pragma unroll
        for (int i = 0; i < 16; ++i) acc[i] = 0.f;
#pragma unroll
        for (int h = 0; h < 32; ++h) acc = mfma32(WO[h], lds128(lds, L_OF + h * 1024 + lane * 16), acc);
        TS(7 + 10 * L);
        unsigned xp[8];
        float sx = 0.f, sxx = 0.f;
#pragma unroll
        for (int q = 0; q < 4; ++q) {
            const unsigned o01 = pk2(acc[4 * q], acc[4 * q + 1]), o23 = pk2(acc[4 * q + 2], acc[4 * q + 3]);
            xp[2 * q] = pk2(bflo(resid[q].x) + bflo(o01), bfhi(resid[q].x) + bfhi(o01));
            xp[2 * q + 1] = pk2(bflo(resid[q].y) + bflo(o23), bfhi(resid[q].y) + bfhi(o23));
            sx = dot2(xp[2 * q], 0x3F803F80u, sx); sxx = dot2(xp[2 * q], xp[2 * q], sxx);
            sx = dot2(xp[2 * q + 1], 0x3F803F80u, sx); sxx = dot2(xp[2 * q + 1], xp[2 * q + 1], sxx);
        }
        sx = xsum32(sx); sxx = xsum32(sxx);
        const unsigned stb = WS_STX + L * 262144 + (unsigned)rt * 4096;
        if (lane < 32) st64(v2u{guard(__float_as_uint(sx)), guard(__float_as_uint(sxx))}, ws, (unsigned)lane * 8, stb + (unsigned)(cb * 4 + w) * 256);
        const bool even = (lane & 1) == 0;
        const unsigned pairoff = (unsigned)((2 * hf + (lane & 1)) * 8192 + (l31 & ~1) * 4);
        const unsigned pairbase = (unsigned)(cb * 64 + w * 16) * 8192 + (unsigned)rt * 128;
        auto put_pairs = [&](const unsigned (&p)[8], unsigned wsbase, bool gd) {
#pragma unroll
            for (int q = 0; q < 4; ++q) {
                const unsigned send = even ? p[2 * q + 1] : p[2 * q];
                const unsigned recv = dpp_u<0xB1>(send);
                v2u v = even ? v2u{p[2 * q], recv} : v2u{recv, p[2 * q + 1]};
                if (gd) { v.x = guard(v.x); v.y = guard(v.y); }
                st64(v, ws, pairoff + q * 4 * 8192, wsbase + pairbase);
            }
        };
        put_pairs(xp, WS_X1 + L * (2u << 20), false);
        if (L == 0) { kgen2(); }
        v2u sp[8];
        auto st_issue = [&]() {
#pragma unroll
            for (int j = 0; j < 8; ++j) sp[j] = ld64(ws, (unsigned)(hf * 2048 + l31 * 8), stb + j * 256);
        };
        st_issue();
        {
            bool ok = true;
#pragma unroll
            for (int j = 0; j < 8; ++j) ok &= ok2(sp[j]);
            for (unsigned s = 0; !wall(ok) && s < SPIN_CAP; ++s) {
            asm volatile("" ::: "memory");
                st_issue(); ok = true;
#pragma unroll
                for (int j = 0; j < 8; ++j) ok &= ok2(sp[j]);
            }
        }
        float tx = 0.f, txx = 0.f;
#pragma unroll
        for (int j = 0; j < 8; ++j) { tx += __uint_as_float(sp[j].x); txx += __uint_as_float(sp[j].y); }
        tx = xsum32(tx); txx = xsum32(txx);
        float m, rs; ln_stats(tx, txx, m, rs);
        unsigned up[8];
#pragma unroll
        for (int i = 0; i < 8; ++i) {
            const unsigned dd = pk2(bflo(xp[i]) - m, bfhi(xp[i]) - m);
            up[i] = pk2(bflo(dd) * rs, bfhi(dd) * rs);
        }
        put_pairs(up, WS_U + L * (2u << 20), true);
        TS(8 + 10 * L);
    };

    auto phaseB = [&](auto Lc) {
        constexpr int L = decltype(Lc)::v;
        const unsigned ub = WS_U + L * (2u << 20) + (unsigned)b * 8192;
        if (tid < 64) *(LDSP v4u*)(lds + L_UL + (tid >> 5) * 4608 + (tid & 31) * 16) = v4u{0, 0, 0, 0};
        v4u uu[2];
        uu[0] = ld128(ws, (unsigned)tid * 16, ub); uu[1] = ld128(ws, (unsigned)tid * 16, ub + 4096);
        {
            bool ok = ok4(uu[0]) & ok4(uu[1]);
            for (unsigned s = 0; !wall(ok) && s < SPIN_CAP; ++s) {
            asm volatile("" ::: "memory");
                uu[0] = ld128(ws, (unsigned)tid * 16, ub); uu[1] = ld128(ws, (unsigned)tid * 16, ub + 4096);
                ok = ok4(uu[0]) & ok4(uu[1]);
            }
        }
        TS(9 + 10 * L);
        asm volatile("" ::: "memory");
        const int ch = w >> 1, par = w & 1;
        v4u xr[4];
#pragma unroll
        for (int i = 0; i < 4; ++i) xr[i] = ld128(ws, (unsigned)((16 * l15 + 4 * gq) * 4), WS_X1 + L * (2u << 20) + (unsigned)b * 8192 + (unsigned)(4 * par + i) * 1024);
#pragma unroll
        for (int j = 0; j < 2; ++j) {
            const unsigned t0 = (unsigned)(j * 256 + tid) * 4;
            *(LDSP v2u*)(lds + L_UL + (256 + t0) * 2) = v2u{__builtin_amdgcn_perm(uu[j].y, uu[j].x, 0x05040100u), __builtin_amdgcn_perm(uu[j].w, uu[j].z, 0x05040100u)};
            *(LDSP v2u*)(lds + L_UL + 4608 + (256 + t0) * 2) = v2u{__builtin_amdgcn_perm(uu[j].y, uu[j].x, 0x07060302u), __builtin_amdgcn_perm(uu[j].w, uu[j].z, 0x07060302u)};
        }
        BAR();
        f32x4 acc[8];
#pragma unroll
        for (int i = 0; i < 8; ++i) acc[i] = f32x4{0, 0, 0, 0};
        const unsigned sc = (4 - q4) & 3;
        const unsigned tbase = L_KC + ch * 16640 + sc * 4160 + 2 * (32 - 8 * gq - (q4 + sc) + 4 * p4) + 32 + 256 * par;
        const unsigned vbase = L_UL + ch * 4608 + 2 * (256 - 16 + 16 * l15 + 8 * gq) - 256 * par;
#pragma unroll
        for (int r4 = 0; r4 < 4; ++r4) {
            v4u T[8], V[8];
#pragma unroll
            for (int a = 0; a < 8; ++a) {
                V[a] = lds128(lds, vbase + 512 * a - 64 * r4);
                T[a] = tr2(lds, tbase + 64 * r4 + 512 * a, tbase + 64 * r4 + 512 * a - 8);
            }
#pragma unroll
            for (int a = 0; a < 8; ++a)
#pragma unroll
                for (int bb = 0; bb + a < 8; ++bb) acc[a + bb] = mfma16(T[a], V[bb], acc[a + bb]);
        }
#pragma unroll
        for (int i = 0; i < 4; ++i) {
            const f32x4 lo = acc[i], hi = acc[4 + i];
            const f32x4 s = par ? lo : hi;
            *(LDSP f32x4*)(lds + L_EX + w * 4096 + i * 1024 + lane * 16) = s;
        }
        BAR();
        TS(10 + 10 * L);
        v2u o[4];
#pragma unroll
        for (int i = 0; i < 4; ++i) {
            const f32x4 lo = acc[i], hi = acc[4 + i];
            const f32x4 y = (par ? hi : lo) + *(LDSP f32x4*)(lds + L_EX + (w ^ 1) * 4096 + i * 1024 + lane * 16);
            const unsigned t = (unsigned)(256 * (4 * par + i) + 16 * l15 + 4 * gq);
            const v2u u = lds64(lds, L_UL + ch * 4608 + (256 + t) * 2);
            const unsigned y01 = pk2(y[0], y[1]), y23 = pk2(y[2], y[3]);
            const unsigned s01 = pk2(bflo(u.x) * dsk, bfhi(u.x) * dsk), s23 = pk2(bflo(u.y) * dsk, bfhi(u.y) * dsk);
            const unsigned r01 = pk2(bflo(y01) + bflo(s01), bfhi(y01) + bfhi(s01)), r23 = pk2(bflo(y23) + bflo(s23), bfhi(y23) + bfhi(s23));
            const float x0 = ch ? bfhi(xr[i].x) : bflo(xr[i].x), x1 = ch ? bfhi(xr[i].y) : bflo(xr[i].y);
            const float x2 = ch ? bfhi(xr[i].z) : bflo(xr[i].z), x3 = ch ? bfhi(xr[i].w) : bflo(xr[i].w);
            o[i] = v2u{guard(pk2(x0 + bflo(r01), x1 + bfhi(r01))), guard(pk2(x2 + bflo(r23), x3 + bfhi(r23)))};
        }
        WAITVM(0);
#pragma unroll
        for (int i = 0; i < 4; ++i)
            st64(o[i], ws, (unsigned)((l15 >> 2) * 65536 + (16 * (l15 & 3) + 4 * gq) * 2),
                 WS_XT + L * (2u << 20) + (unsigned)(4 * (4 * par + i)) * 65536 + (unsigned)(2 * b + ch) * 128);
        TS(11 + 10 * L);
    };

    {
        v4u kp[4]; unsigned zp;
        Cx wv; float ar2, air;
        if (w == 0) red_issue(LC<0>{}, kp, zp);
        kgen1(0, wv, ar2, air);
        if (w == 0) red_finish(LC<0>{}, kp, zp);
#pragma unroll
        for (int h = 0; h < 32; ++h) WO[h] = ld128(ws, (unsigned)lane * 16, WS_WOF + (unsigned)((4 * cb + w) * 32 + h) * 1024);
        kgen1(1, wv, ar2, air);
        TS(5);
        phaseA2(LC<0>{});
        poison_kvq(ws, 1, tt, g, w, lane, b);
        poison_ux(ws, 1, rt, cb, w, lane, tid, b);
        BAR();
        kgen3();
        phaseB(LC<0>{});
    }

    {
        constexpr unsigned SA = L_KG;
        v4u xv[16];
        const unsigned xoff = (unsigned)((16 * w + (lane >> 3)) * 128 + (lane & 7) * 16);
        auto x_addr = [&](int c, int xh) { return WS_XT + (unsigned)tt * 65536 + (unsigned)(((c + rot) & 7) * 8192 + xh * 1024); };
#pragma unroll
        for (int c = 0; c < 8; ++c) { xv[2 * c] = ld128(ws, xoff, x_addr(c, 0)); xv[2 * c + 1] = ld128(ws, xoff, x_addr(c, 1)); }
        auto fill = [&](auto Cc) {
            constexpr int c = decltype(Cc)::v;
            bool ok = ok4(xv[2 * c]) & ok4(xv[2 * c + 1]);
            for (unsigned s = 0; !wall(ok) && s < SPIN_CAP; ++s) {
            asm volatile("" ::: "memory");
                xv[2 * c] = ld128(ws, xoff, x_addr(c, 0)); xv[2 * c + 1] = ld128(ws, xoff, x_addr(c, 1));
                ok = ok4(xv[2 * c]) & ok4(xv[2 * c + 1]);
            }
#pragma unroll
            for (int xh = 0; xh < 2; ++xh) {
                const unsigned row = (unsigned)(16 * w + 8 * xh + (lane >> 3));
                const unsigned f = (((row >> 1) & 1) << 1) | ((row >> 3) & 1);
                *(LDSP v4u*)(lds + L_XS + c * 8192 + row * 128 + (((lane & 7) * 16) ^ (f << 5))) = xv[2 * c + xh];
            }
        };
        f32x16 acc0, acc1;
#pragma unroll
        for (int i = 0; i < 16; ++i) { acc0[i] = 0.f; acc1[i] = 0.f; }
        float s1a = 0, s2a = 0, s1b = 0, s2b = 0;
        const unsigned fa = ((q4 >> 1) << 1) | (unsigned)(gq >> 1);
        const unsigned aA0 = (unsigned)((8 * (gq >> 1) + q4) * 128) + ((((unsigned)(gq & 1)) ^ fa) * 32) + p4 * 8;
        const unsigned aA1 = (unsigned)((8 * (gq >> 1) + q4) * 128) + (((2u + (unsigned)(gq & 1)) ^ fa) * 32) + p4 * 8;
        auto chunk1 = [&](auto Cc) {
            constexpr int c = decltype(Cc)::v;
            const unsigned cbase = L_XS + c * 8192;
#pragma unroll
            for (int ks = 0; ks < 4; ++ks) {
                v4u a0 = tr2(lds, cbase + ks * 2048 + aA0, cbase + ks * 2048 + 512 + aA0);
                v4u a1 = tr2(lds, cbase + ks * 2048 + aA1, cbase + ks * 2048 + 512 + aA1);
                acc0 = mfma32(a0, Wkv[4 * c + ks], acc0);
                acc1 = mfma32(a1, Wkv[4 * c + ks], acc1);
            }
            v4u f0 = tr2(lds, cbase + w * 2048 + aA0, cbase + w * 2048 + 512 + aA0);
            v4u f1 = tr2(lds, cbase + w * 2048 + aA1, cbase + w * 2048 + 512 + aA1);
#pragma unroll
            for (int i = 0; i < 4; ++i) {
                s1a = dot2(f0[i], 0x3F803F80u, s1a); s2a = dot2(f0[i], f0[i], s2a);
                s1b = dot2(f1[i], 0x3F803F80u, s1b); s2b = dot2(f1[i], f1[i], s2b);
            }
        };
        BAR();
        fill(LC<0>{});
        TS(12);
        BAR(); fill(LC<1>{}); chunk1(LC<0>{});
        BAR(); fill(LC<2>{}); chunk1(LC<1>{});
        BAR(); fill(LC<3>{}); chunk1(LC<2>{});
        BAR(); fill(LC<4>{}); chunk1(LC<3>{});
        BAR(); fill(LC<5>{}); chunk1(LC<4>{});
        BAR(); fill(LC<6>{}); chunk1(LC<5>{});
        BAR(); fill(LC<7>{}); chunk1(LC<6>{});
        BAR(); chunk1(LC<7>{});
        TS(13);
        kv_epilogue(LC<1>{}, acc0, acc1, s1a, s2a, s1b, s2b, SA);
        TS(14);
        f32x4 aq[4];
#pragma unroll
        for (int i = 0; i < 4; ++i) aq[i] = f32x4{0, 0, 0, 0};
        const unsigned fb = ((q4 >> 1) << 1) | (unsigned)(gq & 1);
#pragma unroll
        for (int k2 = 0; k2 < 16; ++k2) {
#pragma unroll
            for (int tq = 0; tq < 4; ++tq) {
                const unsigned o = L_XS + k2 * 4096 + (unsigned)((8 * gq + q4) * 128) + (((unsigned)tq ^ fb) * 32) + p4 * 8;
                aq[tq] = mfma16(Wq[k2], tr2(lds, o, o + 512), aq[tq]);
            }
        }
        q_epilogue(LC<1>{}, aq, SA);
        {
            const int d0 = 128 * cb + 32 * w, kc = d0 >> 6, pos = (kc - rot) & 7;
#pragma unroll
            for (int q = 0; q < 4; ++q) {
                const unsigned row = (unsigned)((d0 & 63) + 8 * q + 4 * (gq >> 1)) + q4;
                const unsigned f = (((row >> 1) & 1) << 1) | ((row >> 3) & 1);
                const unsigned unit = (unsigned)(2 * (rt & 1) + (gq & 1));
                resid[q] = tr64(lds, L_XS + pos * 8192 + row * 128 + ((unit ^ f) * 32) + p4 * 8);
            }
        }
        TS(15);
        v4u kp[4]; unsigned zp;
        if (w == 0) { red_issue(LC<1>{}, kp, zp); red_finish(LC<1>{}, kp, zp); }
#pragma unroll
        for (int h = 0; h < 32; ++h) WO[h] = ld128(ws, (unsigned)lane * 16, WS_WOF + (unsigned)((4 * cb + w) * 32 + h) * 1024);
        BAR();
        phaseA2(LC<1>{});
        phaseB(LC<1>{});
    }

    {
        v4u fv[2];
        const unsigned fbase = WS_XT + (2u << 20) + (unsigned)tt * 65536 + (unsigned)g * 8192;
        fv[0] = ld128(ws, (unsigned)tid * 16, fbase); fv[1] = ld128(ws, (unsigned)tid * 16, fbase + 4096);
        bool ok = ok4(fv[0]) & ok4(fv[1]);
        for (unsigned s = 0; !wall(ok) && s < SPIN_CAP; ++s) {
            asm volatile("" ::: "memory");
            fv[0] = ld128(ws, (unsigned)tid * 16, fbase); fv[1] = ld128(ws, (unsigned)tid * 16, fbase + 4096);
            ok = ok4(fv[0]) & ok4(fv[1]);
        }
        TS(22);
#pragma unroll
        for (int xh = 0; xh < 2; ++xh) {
            const unsigned row = (unsigned)(32 * xh + (tid >> 3));
            const unsigned f = (((row >> 1) & 1) << 1) | ((row >> 3) & 1);
            *(LDSP v4u*)(lds + row * 128 + (((tid & 7) * 16) ^ (f << 5))) = fv[xh];
        }
        BAR();
#pragma unroll
        for (int r = 0; r < 2; ++r) {
            const unsigned row0 = (unsigned)(32 * r + 8 * gq) + q4;
            const unsigned f = (((row0 >> 1) & 1) << 1) | ((row0 >> 3) & 1);
            const unsigned o = row0 * 128 + (((unsigned)w ^ f) * 32) + p4 * 8;
            v4u v = tr2(lds, o, o + 512);
            __builtin_amdgcn_raw_buffer_store_b128(v, rsO, (unsigned)((64 * tt + 16 * w + l15) * 1024 + (64 * g + 32 * r + 8 * gq) * 2), 0, 0);
        }
        if (b == 0 && tid == 0) st32(tag, ws, 0, WS_EPOCH);
    }
    TS(23);
}

extern "C" void kernel_launch(void* const* d_in, const int* in_sizes, int n_in, void* d_out, int out_size, void* d_ws, size_t ws_size, hipStream_t stream) {
    fused_kernel<<<256, 256, 0, stream>>>((const uint16_t*)d_in[0], (const uint16_t*)d_in[1], (const uint16_t*)d_in[2], (const uint16_t*)d_in[4],
                                          (const uint16_t*)d_in[5], (const uint16_t*)d_in[6], (const uint16_t*)d_in[7], (const uint16_t*)d_in[8],
                                          (const uint16_t*)d_in[9], (const uint16_t*)d_in[10], (const uint16_t*)d_in[11], (uint16_t*)d_out,
                                          (unsigned char*)d_ws);
}
```

```cpp
#include <hip/hip_runtime.h>
#include <stdint.h>

constexpr unsigned WS_EPOCH = 0u;
constexpr unsigned WS_RDY = 4096u;
constexpr unsigned WS_KVR = 1572864u;
constexpr unsigned WS_ZP = 131072u;
constexpr unsigned WS_STX = 262144u;
constexpr unsigned WS_WOF = 1048576u;
constexpr unsigned WS_KVP = 2097152u;
constexpr unsigned WS_Q = 4194304u;
constexpr unsigned WS_X1 = 8388608u;
constexpr unsigned WS_U = 12582912u;
constexpr unsigned WS_XT = 16777216u;
constexpr unsigned WS_DBG = 20971520u;
constexpr unsigned WS_SIZE = 25165824u;

__global__ void __launch_bounds__(256, 1) fused_kernel(
    const uint16_t* x, const uint16_t* wqkv, const uint16_t* wo, const uint16_t* pAre, const uint16_t* pAim, const uint16_t* pBre,
    const uint16_t* pBim, const uint16_t* pCre, const uint16_t* pCim, const uint16_t* pLdt, const uint16_t* pDsk, uint16_t* out,
    unsigned char* wsp) {
    __shared__ __attribute__((aligned(128))) unsigned char lds_raw[163840];
#if defined(__HIP_DEVICE_COMPILE__)
    const unsigned ldsbase = (unsigned)(uintptr_t)(__attribute__((address_space(3))) unsigned char*)lds_raw;
    asm volatile(
    "  s_load_dwordx2 s[4:5], s[0:1], 0\n"
    "  s_load_dwordx2 s[8:9], s[0:1], 8\n"
    "  s_load_dwordx2 s[12:13], s[0:1], 16\n"
    "  s_load_dwordx2 s[16:17], s[0:1], 96\n"
    "  s_load_dwordx2 s[20:21], s[0:1], 88\n"
    "  s_load_dwordx16 s[24:39], s[0:1], 24\n"
    "  v_lshrrev_b32 v10, 6, v0\n"
    "  v_and_b32 v1, 63, v0\n"
    "  v_readfirstlane_b32 s40, v10\n"
    "  s_and_b32 s67, s2, 7\n"
    "  s_lshr_b32 s66, s67, 1\n"
    "  s_lshr_b32 s65, s2, 3\n"
    "  s_lshr_b32 s64, s65, 2\n"
    "  s_lshl3_add_u32 s41, s66, s64\n"
    "  s_and_b32 s67, s67, 1\n"
    "  s_and_b32 s65, s65, 3\n"
    "  s_lshl2_add_u32 s42, s67, s65\n"
    "  s_lshl1_add_u32 s64, s65, s64\n"
    "  s_and_b32 s45, s64, 7\n"
    "  s_mov_b32 s14, 0x00080000\n"
    "  s_mov_b32 s15, 0x20000\n"
    "  s_mov_b32 s6, 0x00200000\n"
    "  s_mov_b32 s7, 0x20000\n"
    "  s_mov_b32 s10, 0x00180000\n"
    "  s_mov_b32 s11, 0x20000\n"
    "  s_mov_b32 s18, 0x01800000\n"
    "  s_mov_b32 s19, 0x20000\n"
    "  v_lshrrev_b32 v107, 2, v1\n"
    "  v_and_b32 v106, 3, v1\n"
    "  v_lshlrev_b32 v106, 4, v106\n"
    "  v_lshl_add_u32 v138, v107, 10, v106\n"
    "  v_lshrrev_b32 v107, 3, v1\n"
    "  v_and_b32 v106, 7, v1\n"
    "  v_lshrrev_b32 v105, 4, v1\n"
    "  v_xor_b32 v104, v106, v105\n"
    "  v_lshlrev_b32 v104, 4, v104\n"
    "  v_lshl_add_u32 v128, v107, 10, v104\n"
    "  v_lshlrev_b32 v107, 10, v107\n"
    "  v_add_u32 v104, 4, v105\n"
    "  v_and_b32 v104, 7, v104\n"
    "  v_xor_b32 v104, v106, v104\n"
    "  v_lshlrev_b32 v104, 4, v104\n"
    "  s_mov_b32 s67, 0x1c00\n"
    "  v_add3_u32 v129, v107, v104, s67\n"
    "  v_and_b32 v104, 1, v1\n"
    "  v_bfe_u32 v107, v1, 1, 3\n"
    "  v_xor_b32 v107, v107, v105\n"
    "  v_and_b32 v106, 3, v107\n"
    "  v_mul_u32_u24 v105, 0xc00, v105\n"
    "  v_add_u32 v105, 0x400, v105\n"
    "  v_and_b32 v103, 4, v107\n"
    "  v_lshlrev_b32 v103, 8, v103\n"
    "  s_lshl_b32 s67, s42, 7\n"
    "  v_add3_u32 v103, v105, v103, s67\n"
    "  v_lshl_add_u32 v103, v106, 5, v103\n"
    "  v_lshl_add_u32 v130, v104, 4, v103\n"
    "  v_xor_b32 v107, 4, v107\n"
    "  v_and_b32 v103, 3, v107\n"
    "  v_and_b32 v107, 4, v107\n"
    "  v_lshlrev_b32 v107, 8, v107\n"
    "  v_add3_u32 v105, v105, v107, s67\n"
    "  v_lshl_add_u32 v105, v103, 5, v105\n"
    "  v_lshlrev_b32 v104, 4, v104\n"
    "  s_mov_b32 s67, 0x5800\n"
    "  v_add3_u32 v132, v105, v104, s67\n"
    "  s_lshl_b32 s67, s41, 6\n"
    "  s_lshl4_add_u32 s67, s40, s67\n"
    "  s_lshl_b32 s48, s67, 10\n"
    "  s_lshl_b32 s49, s40, 11\n"
    "  s_mul_i32 s50, s40, 0xc000\n"
    "  s_lshl_b32 s67, s40, 12\n"
    "  s_add_u32 s51, s67, 0x10000\n"
    "  s_and_b32 s67, s40, 1\n"
    "  s_and_b32 s66, s2, 15\n"
    "  s_lshl_b32 s66, s66, 5\n"
    "  s_lshl4_add_u32 s67, s67, s66\n"
    "  s_lshl_b32 s67, s67, 10\n"
    "  s_lshr_b32 s66, s2, 4\n"
    "  s_lshl_b32 s66, s66, 6\n"
    "  s_add_u32 s52, s67, s66\n"
    "  s_add_u32 m0, s49, 0xe000\n"
    "  v_add_u32 v131, 0x00002c00, v130\n"
    "  v_add_u32 v133, 0x00002c00, v132\n"
    "  s_waitcnt lgkmcnt(0)\n"
    "  buffer_load_dwordx4 v138, s[12:15], s52 offen lds\n"
    "  s_add_u32 s52, s45, 0\n"
    "  s_and_b32 s52, s52, 7\n"
    "  s_lshl_b32 s52, s52, 7\n"
    "  s_add_u32 m0, s49, 0x00000000\n"
    "  s_add_u32 s52, s52, s48\n"
    "  buffer_load_dwordx4 v128, s[4:7], s52 offen lds\n"
    "  buffer_load_dwordx4 v129, s[4:7], s52 offen offset:1024 lds\n"
    "  s_add_u32 s53, s45, 0\n"
    "  s_and_b32 s53, s53, 7\n"
    "  s_mul_i32 s53, s53, 0x00030000\n"
    "  s_add_u32 m0, s51, 0x00000000\n"
    "  s_add_u32 s53, s53, s50\n"
    "  buffer_load_dwordx4 v130, s[8:11], s53 offen lds\n"
    "  buffer_load_dwordx4 v131, s[8:11], s53 offen offset:1024 lds\n"
    "  buffer_load_dwordx4 v132, s[8:11], s53 offen offset:2048 lds\n"
    "  buffer_load_dwordx4 v133, s[8:11], s53 offen offset:3072 lds\n"
    "  s_lshr_b32 s67, s2, 3\n"
    "  s_lshl2_add_u32 s67, s67, s40\n"
    "  s_lshl_b32 s67, s67, 6\n"
    "  v_add_lshl_u32 v170, s67, v1, 4\n"
    "  s_getpc_b64 s[58:59]\n"
    "Lpc%=:\n"
    "  s_mov_b32 s60, Lfin%=-Lpc%=-16\n"
    "  v_cmp_gt_u32 vcc, s60, v170\n"
    "  s_mov_b64 exec, vcc\n"
    "  global_load_dwordx4 v[172:175], v170, s[58:59]\n"
    "  s_mov_b64 exec, -1\n"
    "  s_add_u32 s52, s45, 1\n"
    "  s_and_b32 s52, s52, 7\n"
    "  s_lshl_b32 s52, s52, 7\n"
    "  s_add_u32 m0, s49, 0x00002000\n"
    "  s_add_u32 s52, s52, s48\n"
    "  buffer_load_dwordx4 v128, s[4:7], s52 offen lds\n"
    "  s_and_b32 s67, s2, 7\n"
    "  s_and_b32 s67, s67, 1\n"
    "  s_lshl1_add_u32 s43, s41, s67\n"
    "  s_lshr_b32 s67, s2, 3\n"
    "  s_and_b32 s44, s67, 3\n"
    "  buffer_load_dwordx4 v129, s[4:7], s52 offen offset:1024 lds\n"
    "  s_lshl2_add_u32 s47, s42, s40\n"
    "  v_lshlrev_b32 v2, 4, v1\n"
    "  v_lshrrev_b32 v107, 4, v1\n"
    "  v_lshrrev_b32 v106, 1, v107\n"
    "  v_and_b32 v107, 1, v107\n"
    "  s_add_u32 s53, s45, 1\n"
    "  s_and_b32 s53, s53, 7\n"
    "  s_mul_i32 s53, s53, 0x00030000\n"
    "  s_add_u32 m0, s51, 0x00004000\n"
    "  s_add_u32 s53, s53, s50\n"
    "  buffer_load_dwordx4 v130, s[8:11], s53 offen lds\n"
    "  v_bfe_u32 v105, v1, 2, 2\n"
    "  v_lshl_add_u32 v105, v107, 2, v105\n"
    "  v_and_b32 v107, 3, v1\n"
    "  v_lshlrev_b32 v107, 5, v107\n"
    "  v_lshl_add_u32 v105, v105, 12, v107\n"
    "  buffer_load_dwordx4 v131, s[8:11], s53 offen offset:1024 lds\n"
    "  v_lshl_add_u32 v8, v106, 4, v105\n"
    "  v_lshlrev_b32 v9, 2, v1\n"
    "  v_mov_b32 v140, -1\n"
    "  v_mov_b32 v141, -1\n"
    "  v_mov_b32 v142, -1\n"
    "  buffer_load_dwordx4 v132, s[8:11], s53 offen offset:2048 lds\n"
    "  v_mov_b32 v143, -1\n"
    "  buffer_load_dword v145, off, s[16:19], 0 sc1\n"
    "  s_lshl_b32 s67, s2, 8\n"
    "  s_lshr_b32 s66, s40, 1\n"
    "  s_lshl_b32 s65, s66, 7\n"
    "  buffer_load_dwordx4 v133, s[8:11], s53 offen offset:3072 lds\n"
    "  s_add_u32 s67, s67, s65\n"
    "  s_and_b32 s65, s40, 1\n"
    "  s_lshl_b32 s65, s65, 6\n"
    "  s_add_u32 s65, s67, s65\n"
    "  v_add_lshl_u32 v233, s65, v1, 1\n"
    "  s_add_u32 s52, s45, 2\n"
    "  s_and_b32 s52, s52, 7\n"
    "  s_lshl_b32 s52, s52, 7\n"
    "  s_add_u32 m0, s49, 0x00004000\n"
    "  s_add_u32 s52, s52, s48\n"
    "  buffer_load_dwordx4 v128, s[4:7], s52 offen lds\n"
    "  s_lshl1_add_u32 s66, s2, s66\n"
    "  s_lshl_b32 s66, s66, 1\n"
    "  v_mov_b32 v234, s66\n"
    "  global_load_ushort v225, v233, s[24:25]\n"
    "  global_load_ushort v226, v233, s[26:27]\n"
    "  buffer_load_dwordx4 v129, s[4:7], s52 offen offset:1024 lds\n"
    "  global_load_ushort v227, v233, s[28:29]\n"
    "  global_load_ushort v228, v233, s[30:31]\n"
    "  global_load_ushort v229, v233, s[32:33]\n"
    "  global_load_ushort v230, v233, s[34:35]\n"
    "  global_load_ushort v231, v234, s[36:37]\n"
    "  s_add_u32 s53, s45, 2\n"
    "  s_and_b32 s53, s53, 7\n"
    "  s_mul_i32 s53, s53, 0x00030000\n"
    "  s_add_u32 m0, s51, 0x00008000\n"
    "  s_add_u32 s53, s53, s50\n"
    "  buffer_load_dwordx4 v130, s[8:11], s53 offen lds\n"
    "  global_load_ushort v232, v234, s[38:39]\n"
    "  s_and_b32 s67, s40, 3\n"
    "  s_lshl_b32 s67, s67, 11\n"
    "  v_and_b32 v107, 15, v1\n"
    "  v_lshrrev_b32 v106, 4, v1\n"
    "  buffer_load_dwordx4 v131, s[8:11], s53 offen offset:1024 lds\n"
    "  v_lshrrev_b32 v105, 1, v107\n"
    "  v_xor_b32 v104, v106, v105\n"
    "  v_lshlrev_b32 v104, 4, v104\n"
    "  v_lshl_add_u32 v104, v107, 7, v104\n"
    "  v_add_u32 v116, s67, v104\n"
    "  buffer_load_dwordx4 v132, s[8:11], s53 offen offset:2048 lds\n"
    "  s_add_u32 s66, s40, 1\n"
    "  s_and_b32 s66, s66, 3\n"
    "  s_lshl_b32 s66, s66, 11\n"
    "  v_add_u32 v117, s66, v104\n"
    "  s_add_u32 s65, s40, 2\n"
    "  buffer_load_dwordx4 v133, s[8:11], s53 offen offset:3072 lds\n"
    "  s_and_b32 s65, s65, 3\n"
    "  s_lshl_b32 s65, s65, 11\n"
    "  v_add_u32 v118, s65, v104\n"
    "  s_add_u32 s64, s40, 3\n"
    "  s_and_b32 s64, s64, 3\n"
    "  s_lshl_b32 s64, s64, 11\n"
    "  v_add_u32 v119, s64, v104\n"
    "  v_add_u32 v104, 4, v106\n"
    "  v_xor_b32 v105, v104, v105\n"
    "  v_lshlrev_b32 v105, 4, v105\n"
    "  v_lshl_add_u32 v107, v107, 7, v105\n"
    "  v_add_u32 v120, s67, v107\n"
    "  v_add_u32 v121, s66, v107\n"
    "  v_add_u32 v122, s65, v107\n"
    "  v_add_u32 v123, s64, v107\n"
    "  v_bfe_u32 v107, v1, 2, 2\n"
    "  v_lshl_add_u32 v105, v106, 3, v107\n"
    "  v_and_b32 v104, 1, v106\n"
    "  v_lshl_or_b32 v103, v104, 2, v107\n"
    "  v_xor_b32 v103, s40, v103\n"
    "  v_lshlrev_b32 v103, 5, v103\n"
    "  v_lshl_add_u32 v105, v105, 8, v103\n"
    "  v_and_b32 v103, 3, v1\n"
    "  v_lshlrev_b32 v103, 3, v103\n"
    "  s_mov_b32 s64, 0x10000\n"
    "  v_add3_u32 v124, v105, v103, s64\n"
    "  v_lshrrev_b32 v106, 1, v106\n"
    "  v_lshl_add_u32 v107, v106, 2, v107\n"
    "  s_lshl_b32 s64, s40, 11\n"
    "  v_lshl_add_u32 v107, v107, 6, s64\n"
    "  v_lshl_add_u32 v107, v104, 5, v107\n"
    "  s_mov_b32 s64, 0xe000\n"
    "  v_add3_u32 v154, v107, v103, s64\n"
    "  v_xor_b32 v125, 128, v124\n"
    "  v_mov_b32 v112, 0x3f803f80\n"
    "  v_mov_b32 v113, 0x3f803f80\n"
    "  v_mov_b32 v114, 0x3f803f80\n"
    "  v_mov_b32 v115, 0x3f803f80\n"
    "  v_and_b32 v107, 1, v1\n"
    "  v_bfe_u32 v106, v1, 1, 2\n"
    "  v_bfe_u32 v105, v1, 4, 1\n"
    "  v_lshlrev_b32 v105, 1, v105\n"
    "  v_xor_b32 v106, v106, v105\n"
    "  v_lshrrev_b32 v105, 3, v1\n"
    "  v_mul_u32_u24 v105, 0xc00, v105\n"
    "  s_lshl_b32 s67, s42, 7\n"
    "  v_add_u32 v105, s67, v105\n"
    "  v_lshl_add_u32 v105, v106, 5, v105\n"
    "  v_lshl_add_u32 v134, v107, 4, v105\n"
    "  v_add_u32 v135, 0x00005c00, v134\n"
    "  v_add_u32 v136, 0x00005c00, v135\n"
    "  v_add_u32 v137, 0x00005c00, v136\n"
    "  s_mov_b32 s22, 0x00200000\n"
    "  s_mov_b32 s23, 0x20000\n"
    "  s_waitcnt vmcnt(28)\n"
    "  ds_read_b64_tr_b16 v[156:157], v154\n"
    "  ds_read_b64_tr_b16 v[158:159], v154 offset:512\n"
    "  s_and_b32 s59, s2, 15\n"
    "  s_lshr_b32 s58, s2, 4\n"
    "  s_lshl_b32 s58, s58, 5\n"
    "  s_lshl1_add_u32 s59, s59, s58\n"
    "  s_and_b32 s58, s40, 1\n"
    "  s_add_u32 s59, s59, s58\n"
    "  s_lshl_b32 s59, s59, 10\n"
    "  s_add_u32 s54, s59, 0x100000\n"
    "  s_cmp_lt_u32 s40, 2\n"
    "  s_cselect_b64 exec, -1, 0\n"
    "  s_waitcnt lgkmcnt(0)\n"
    "  buffer_store_dwordx4 v[156:159], v2, s[16:19], s54 offen sc1\n"
    "  s_mov_b64 exec, -1\n"
    "  s_lshl_b32 s59, s47, 8\n"
    "  s_add_u32 s59, s59, s41\n"
    "  s_lshl_b32 s59, s59, 7\n"
    "  s_add_u32 s54, s59, 0x200000\n"
    "  buffer_store_dwordx4 v[140:143], v8, s[16:19], s54 offen sc1\n"
    "  s_lshl_b32 s59, s47, 5\n"
    "  s_add_u32 s59, s59, s41\n"
    "  s_lshl_b32 s59, s59, 6\n"
    "  s_add_u32 s55, s59, 0x20000\n"
    "  s_mov_b64 exec, 0xffff\n"
    "  buffer_store_dword v140, v9, s[16:19], s55 offen sc1\n"
    "  s_mov_b64 exec, -1\n"
    "  s_waitcnt vmcnt(25)\n"
    "  s_barrier\n"
    "  ds_read_b64_tr_b16 a[0:1], v124\n"
    "  ds_read_b64_tr_b16 a[2:3], v124 offset:1024\n"
    "  s_add_u32 s52, s45, 3\n"
    "  s_and_b32 s52, s52, 7\n"
    "  s_lshl_b32 s52, s52, 7\n"
    "  s_add_u32 m0, s49, 0x00006000\n"
    "  s_add_u32 s52, s52, s48\n"
    "  buffer_load_dwordx4 v128, s[4:7], s52 offen lds\n"
    "  ds_read_b64_tr_b16 a[4:5], v125\n"
    "  ds_read_b64_tr_b16 a[6:7], v125 offset:1024\n"
    "  buffer_load_dwordx4 v129, s[4:7], s52 offen offset:1024 lds\n"
    "  ds_read_b128 v[64:67], v116\n"
    "  ds_read_b128 v[68:71], v117\n"
    "  s_add_u32 s53, s45, 3\n"
    "  s_and_b32 s53, s53, 7\n"
    "  s_mul_i32 s53, s53, 0x00030000\n"
    "  s_add_u32 m0, s51, 0x0000c000\n"
    "  s_add_u32 s53, s53, s50\n"
    "  buffer_load_dwordx4 v130, s[8:11], s53 offen lds\n"
    "  ds_read_b128 v[72:75], v118\n"
    "  ds_read_b128 v[76:79], v119\n"
    "  buffer_load_dwordx4 v131, s[8:11], s53 offen offset:1024 lds\n"
    "  ds_read_b64_tr_b16 a[8:9], v124 offset:8192\n"
    "  ds_read_b64_tr_b16 a[10:11], v124 offset:9216\n"
    "  buffer_load_dwordx4 v132, s[8:11], s53 offen offset:2048 lds\n"
    "  ds_read_b64_tr_b16 a[12:13], v125 offset:8192\n"
    "  ds_read_b64_tr_b16 a[14:15], v125 offset:9216\n"
    "  buffer_load_dwordx4 v133, s[8:11], s53 offen offset:3072 lds\n"
    "  ds_read_b128 v[80:83], v120\n"
    "  ds_read_b128 v[84:87], v121\n"
    "  ds_read_b128 v[88:91], v122\n"
    "  ds_read_b128 v[92:95], v123\n"
    "  s_waitcnt vmcnt(23)\n"
    "  s_barrier\n"
    "  s_waitcnt lgkmcnt(11)\n"
    "  v_mfma_f32_16x16x32_bf16 v[16:19], v[64:67], a[0:3], 0\n"
    "  ds_read_b128 v[96:99], v116 offset:8192\n"
    "  s_add_u32 s52, s45, 4\n"
    "  s_and_b32 s52, s52, 7\n"
    "  s_lshl_b32 s52, s52, 7\n"
    "  s_add_u32 m0, s49, 0x00008000\n"
    "  s_add_u32 s52, s52, s48\n"
    "  buffer_load_dwordx4 v128, s[4:7], s52 offen lds\n"
    "  v_mfma_f32_16x16x32_bf16 v[32:35], v[64:67], a[4:7], 0\n"
    "  ds_read_b128 v[100:103], v117 offset:8192\n"
    "  s_waitcnt lgkmcnt(11)\n"
    "  v_mfma_f32_16x16x32_bf16 v[20:23], v[68:71], a[0:3], 0\n"
    "  ds_read_b64_tr_b16 a[16:17], v124 offset:16384\n"
    "  v_mfma_f32_16x16x32_bf16 v[36:39], v[68:71], a[4:7], 0\n"
    "  ds_read_b64_tr_b16 a[18:19], v124 offset:17408\n"
    "  buffer_load_dwordx4 v129, s[4:7], s52 offen offset:1024 lds\n"
    "  v_mfma_f32_16x16x32_bf16 v[24:27], v[72:75], a[0:3], 0\n"
    "  ds_read_b128 v[104:107], v118 offset:8192\n"
    "  v_mfma_f32_16x16x32_bf16 v[40:43], v[72:75], a[4:7], 0\n"
    "  ds_read_b128 v[108:111], v119 offset:8192\n"
    "  s_waitcnt lgkmcnt(14)\n"
    "  v_mfma_f32_16x16x32_bf16 v[28:31], v[76:79], a[0:3], 0\n"
    "  ds_read_b64_tr_b16 a[20:21], v125 offset:16384\n"
    "  s_add_u32 s53, s45, 4\n"
    "  s_and_b32 s53, s53, 7\n"
    "  s_mul_i32 s53, s53, 0x00030000\n"
    "  s_add_u32 m0, s51, 0x00010000\n"
    "  s_add_u32 s53, s53, s50\n"
    "  buffer_load_dwordx4 v130, s[8:11], s53 offen lds\n"
    "  v_mfma_f32_16x16x32_bf16 v[44:47], v[76:79], a[4:7], 0\n"
    "  ds_read_b64_tr_b16 a[22:23], v125 offset:17408\n"
    "  v_mfma_f32_16x16x32_bf16 v[48:51], v[64:67], v[64:67], 0\n"
    "  ds_read_b64_tr_b16 a[24:25], v124 offset:24576\n"
    "  v_mfma_f32_16x16x32_bf16 v[52:55], v[64:67], v[112:115], 0\n"
    "  ds_read_b64_tr_b16 a[26:27], v124 offset:25600\n"
    "  buffer_load_dwordx4 v131, s[8:11], s53 offen offset:1024 lds\n"
    "  v_mfma_f32_16x16x32_bf16 v[56:59], v[112:115], a[0:3], 0\n"
    "  ds_read_b64_tr_b16 a[28:29], v125 offset:24576\n"
    "  v_mfma_f32_16x16x32_bf16 v[60:63], v[112:115], a[4:7], 0\n"
    "  ds_read_b64_tr_b16 a[30:31], v125 offset:25600\n"
    "  v_mfma_f32_16x16x32_bf16 v[16:19], v[80:83], a[8:11], v[16:19]\n"
    "  ds_read_b128 v[64:67], v120 offset:8192\n"
    "  buffer_load_dwordx4 v132, s[8:11], s53 offen offset:2048 lds\n"
    "  v_mfma_f32_16x16x32_bf16 v[32:35], v[80:83], a[12:15], v[32:35]\n"
    "  ds_read_b128 v[68:71], v121 offset:8192\n"
    "  v_mfma_f32_16x16x32_bf16 v[20:23], v[84:87], a[8:11], v[20:23]\n"
    "  ds_read_b128 v[72:75], v122 offset:8192\n"
    "  v_mfma_f32_16x16x32_bf16 v[36:39], v[84:87], a[12:15], v[36:39]\n"
    "  ds_read_b128 v[76:79], v123 offset:8192\n"
    "  buffer_load_dwordx4 v133, s[8:11], s53 offen offset:3072 lds\n"
    "  v_mfma_f32_16x16x32_bf16 v[24:27], v[88:91], a[8:11], v[24:27]\n"
    "  v_mfma_f32_16x16x32_bf16 v[40:43], v[88:91], a[12:15], v[40:43]\n"
    "  v_mfma_f32_16x16x32_bf16 v[28:31], v[92:95], a[8:11], v[28:31]\n"
    "  v_mfma_f32_16x16x32_bf16 v[44:47], v[92:95], a[12:15], v[44:47]\n"
    "  v_mfma_f32_16x16x32_bf16 v[48:51], v[80:83], v[80:83], v[48:51]\n"
    "  v_mfma_f32_16x16x32_bf16 v[52:55], v[80:83], v[112:115], v[52:55]\n"
    "  v_mfma_f32_16x16x32_bf16 v[56:59], v[112:115], a[8:11], v[56:59]\n"
    "  v_mfma_f32_16x16x32_bf16 v[60:63], v[112:115], a[12:15], v[60:63]\n"
    "  s_waitcnt vmcnt(15)\n"
    "  s_barrier\n"
    "  s_waitcnt lgkmcnt(12)\n"
    "  v_mfma_f32_16x16x32_bf16 v[16:19], v[96:99], a[16:19], v[16:19]\n"
    "  ds_read_b128 v[80:83], v116 offset:16384\n"
    "  s_add_u32 s52, s45, 5\n"
    "  s_and_b32 s52, s52, 7\n"
    "  s_lshl_b32 s52, s52, 7\n"
    "  s_add_u32 m0, s49, 0x0000a000\n"
    "  s_add_u32 s52, s52, s48\n"
    "  buffer_load_dwordx4 v128, s[4:7], s52 offen lds\n"
    "  s_waitcnt lgkmcnt(9)\n"
    "  v_mfma_f32_16x16x32_bf16 v[32:35], v[96:99], a[20:23], v[32:35]\n"
    "  ds_read_b128 v[84:87], v117 offset:16384\n"
    "  v_mfma_f32_16x16x32_bf16 v[20:23], v[100:103], a[16:19], v[20:23]\n"
    "  ds_read_b64_tr_b16 a[32:33], v124 offset:32768\n"
    "  v_mfma_f32_16x16x32_bf16 v[36:39], v[100:103], a[20:23], v[36:39]\n"
    "  ds_read_b64_tr_b16 a[34:35], v124 offset:33792\n"
    "  buffer_load_dwordx4 v129, s[4:7], s52 offen offset:1024 lds\n"
    "  v_mfma_f32_16x16x32_bf16 v[24:27], v[104:107], a[16:19], v[24:27]\n"
    "  ds_read_b128 v[88:91], v118 offset:16384\n"
    "  v_mfma_f32_16x16x32_bf16 v[40:43], v[104:107], a[20:23], v[40:43]\n"
    "  ds_read_b128 v[92:95], v119 offset:16384\n"
    "  v_mfma_f32_16x16x32_bf16 v[28:31], v[108:111], a[16:19], v[28:31]\n"
    "  ds_read_b64_tr_b16 a[36:37], v125 offset:32768\n"
    "  s_add_u32 s53, s45, 5\n"
    "  s_and_b32 s53, s53, 7\n"
    "  s_mul_i32 s53, s53, 0x00030000\n"
    "  s_add_u32 m0, s51, 0x00014000\n"
    "  s_add_u32 s53, s53, s50\n"
    "  buffer_load_dwordx4 v130, s[8:11], s53 offen lds\n"
    "  v_mfma_f32_16x16x32_bf16 v[44:47], v[108:111], a[20:23], v[44:47]\n"
    "  ds_read_b64_tr_b16 a[38:39], v125 offset:33792\n"
    "  v_mfma_f32_16x16x32_bf16 v[48:51], v[96:99], v[96:99], v[48:51]\n"
    "  ds_read_b64_tr_b16 a[40:41], v124 offset:40960\n"
    "  v_mfma_f32_16x16x32_bf16 v[52:55], v[96:99], v[112:115], v[52:55]\n"
    "  ds_read_b64_tr_b16 a[42:43], v124 offset:41984\n"
    "  buffer_load_dwordx4 v131, s[8:11], s53 offen offset:1024 lds\n"
    "  v_mfma_f32_16x16x32_bf16 v[56:59], v[112:115], a[16:19], v[56:59]\n"
    "  ds_read_b64_tr_b16 a[44:45], v125 offset:40960\n"
    "  v_mfma_f32_16x16x32_bf16 v[60:63], v[112:115], a[20:23], v[60:63]\n"
    "  ds_read_b64_tr_b16 a[46:47], v125 offset:41984\n"
    "  v_mfma_f32_16x16x32_bf16 v[16:19], v[64:67], a[24:27], v[16:19]\n"
    "  ds_read_b128 v[96:99], v120 offset:16384\n"
    "  buffer_load_dwordx4 v132, s[8:11], s53 offen offset:2048 lds\n"
    "  v_mfma_f32_16x16x32_bf16 v[32:35], v[64:67], a[28:31], v[32:35]\n"
    "  ds_read_b128 v[100:103], v121 offset:16384\n"
    "  v_mfma_f32_16x16x32_bf16 v[20:23], v[68:71], a[24:27], v[20:23]\n"
    "  ds_read_b128 v[104:107], v122 offset:16384\n"
    "  v_mfma_f32_16x16x32_bf16 v[36:39], v[68:71], a[28:31], v[36:39]\n"
    "  ds_read_b128 v[108:111], v123 offset:16384\n"
    "  buffer_load_dwordx4 v133, s[8:11], s53 offen offset:3072 lds\n"
    "  v_mfma_f32_16x16x32_bf16 v[24:27], v[72:75], a[24:27], v[24:27]\n"
    "  v_mfma_f32_16x16x32_bf16 v[40:43], v[72:75], a[28:31], v[40:43]\n"
    "  v_mfma_f32_16x16x32_bf16 v[28:31], v[76:79], a[24:27], v[28:31]\n"
    "  v_mfma_f32_16x16x32_bf16 v[44:47], v[76:79], a[28:31], v[44:47]\n"
    "  v_mfma_f32_16x16x32_bf16 v[48:51], v[64:67], v[64:67], v[48:51]\n"
    "  v_mfma_f32_16x16x32_bf16 v[52:55], v[64:67], v[112:115], v[52:55]\n"
    "  v_mfma_f32_16x16x32_bf16 v[56:59], v[112:115], a[24:27], v[56:59]\n"
    "  v_mfma_f32_16x16x32_bf16 v[60:63], v[112:115], a[28:31], v[60:63]\n"
    "  s_waitcnt vmcnt(18)\n"
    "  s_waitcnt vmcnt(12)\n"
    "  s_barrier\n"
    "  v_readfirstlane_b32 s46, v145\n"
    "  s_add_u32 s46, s46, 1\n"
    "  v_mov_b32 v154, s46\n"
    "  v_lshlrev_b32 v179, 12, v1\n"
    "  s_lshl2_add_u32 s54, s2, 0x1000\n"
    "  s_cmp_eq_u32 s40, 0\n"
    "  s_cselect_b64 exec, 0xff, 0\n"
    "  buffer_store_dword v154, v179, s[16:19], s54 offen sc1\n"
    "  s_mov_b64 exec, -1\n"
    "  s_waitcnt lgkmcnt(12)\n"
    "  v_mfma_f32_16x16x32_bf16 v[16:19], v[80:83], a[32:35], v[16:19]\n"
    "  ds_read_b128 v[64:67], v116 offset:24576\n"
    "  s_add_u32 s52, s45, 6\n"
    "  s_and_b32 s52, s52, 7\n"
    "  s_lshl_b32 s52, s52, 7\n"
    "  s_add_u32 m0, s49, 0x0000c000\n"
    "  s_add_u32 s52, s52, s48\n"
    "  buffer_load_dwordx4 v128, s[4:7], s52 offen lds\n"
    "  s_waitcnt lgkmcnt(9)\n"
    "  v_mfma_f32_16x16x32_bf16 v[32:35], v[80:83], a[36:39], v[32:35]\n"
    "  ds_read_b128 v[68:71], v117 offset:24576\n"
    "  v_mfma_f32_16x16x32_bf16 v[20:23], v[84:87], a[32:35], v[20:23]\n"
    "  ds_read_b64_tr_b16 a[48:49], v124 offset:49152\n"
    "  v_mfma_f32_16x16x32_bf16 v[36:39], v[84:87], a[36:39], v[36:39]\n"
    "  ds_read_b64_tr_b16 a[50:51], v124 offset:50176\n"
    "  buffer_load_dwordx4 v129, s[4:7], s52 offen offset:1024 lds\n"
    "  v_mfma_f32_16x16x32_bf16 v[24:27], v[88:91], a[32:35], v[24:27]\n"
    "  ds_read_b128 v[72:75], v118 offset:24576\n"
    "  v_mfma_f32_16x16x32_bf16 v[40:43], v[88:91], a[36:39], v[40:43]\n"
    "  ds_read_b128 v[76:79], v119 offset:24576\n"
    "  v_mfma_f32_16x16x32_bf16 v[28:31], v[92:95], a[32:35], v[28:31]\n"
    "  ds_read_b64_tr_b16 a[52:53], v125 offset:49152\n"
    "  s_add_u32 s53, s45, 6\n"
    "  s_and_b32 s53, s53, 7\n"
    "  s_mul_i32 s53, s53, 0x00030000\n"
    "  s_add_u32 m0, s51, 0x00000000\n"
    "  s_add_u32 s53, s53, s50\n"
    "  buffer_load_dwordx4 v130, s[8:11], s53 offen lds\n"
    "  v_mfma_f32_16x16x32_bf16 v[44:47], v[92:95], a[36:39], v[44:47]\n"
    "  ds_read_b64_tr_b16 a[54:55], v125 offset:50176\n"
    "  v_mfma_f32_16x16x32_bf16 v[48:51], v[80:83], v[80:83], v[48:51]\n"
    "  ds_read_b64_tr_b16 a[56:57], v124 offset:57344\n"
    "  v_mfma_f32_16x16x32_bf16 v[52:55], v[80:83], v[112:115], v[52:55]\n"
    "  ds_read_b64_tr_b16 a[58:59], v124 offset:58368\n"
    "  buffer_load_dwordx4 v131, s[8:11], s53 offen offset:1024 lds\n"
    "  v_mfma_f32_16x16x32_bf16 v[56:59], v[112:115], a[32:35], v[56:59]\n"
    "  ds_read_b64_tr_b16 a[60:61], v125 offset:57344\n"
    "  v_mfma_f32_16x16x32_bf16 v[60:63], v[112:115], a[36:39], v[60:63]\n"
    "  ds_read_b64_tr_b16 a[62:63], v125 offset:58368\n"
    "  v_mfma_f32_16x16x32_bf16 v[16:19], v[96:99], a[40:43], v[16:19]\n"
    "  ds_read_b128 v[80:83], v120 offset:24576\n"
    "  buffer_load_dwordx4 v132, s[8:11], s53 offen offset:2048 lds\n"
    "  v_mfma_f32_16x16x32_bf16 v[32:35], v[96:99], a[44:47], v[32:35]\n"
    "  ds_read_b128 v[84:87], v121 offset:24576\n"
    "  v_mfma_f32_16x16x32_bf16 v[20:23], v[100:103], a[40:43], v[20:23]\n"
    "  ds_read_b128 v[88:91], v122 offset:24576\n"
    "  v_mfma_f32_16x16x32_bf16 v[36:39], v[100:103], a[44:47], v[36:39]\n"
    "  ds_read_b128 v[92:95], v123 offset:24576\n"
    "  buffer_load_dwordx4 v133, s[8:11], s53 offen offset:3072 lds\n"
    "  v_mfma_f32_16x16x32_bf16 v[24:27], v[104:107], a[40:43], v[24:27]\n"
    "  v_mfma_f32_16x16x32_bf16 v[40:43], v[104:107], a[44:47], v[40:43]\n"
    "  v_mfma_f32_16x16x32_bf16 v[28:31], v[108:111], a[40:43], v[28:31]\n"
    "  v_mfma_f32_16x16x32_bf16 v[44:47], v[108:111], a[44:47], v[44:47]\n"
    "  v_mfma_f32_16x16x32_bf16 v[48:51], v[96:99], v[96:99], v[48:51]\n"
    "  v_mfma_f32_16x16x32_bf16 v[52:55], v[96:99], v[112:115], v[52:55]\n"
    "  v_mfma_f32_16x16x32_bf16 v[56:59], v[112:115], a[40:43], v[56:59]\n"
    "  v_mfma_f32_16x16x32_bf16 v[60:63], v[112:115], a[44:47], v[60:63]\n"
    "  s_waitcnt vmcnt(13)\n"
    "  s_barrier\n"
    "  v_add_u32 v124, 0x00010000, v124\n"
    "  v_add_u32 v125, 0x00010000, v125\n"
    "  s_waitcnt lgkmcnt(12)\n"
    "  v_mfma_f32_16x16x32_bf16 v[16:19], v[64:67], a[48:51], v[16:19]\n"
    "  ds_read_b128 v[96:99], v116 offset:32768\n"
    "  s_add_u32 s52, s45, 7\n"
    "  s_and_b32 s52, s52, 7\n"
    "  s_lshl_b32 s52, s52, 7\n"
    "  s_add_u32 m0, s49, 0x0000e000\n"
    "  s_add_u32 s52, s52, s48\n"
    "  buffer_load_dwordx4 v128, s[4:7], s52 offen lds\n"
    "  s_waitcnt lgkmcnt(9)\n"
    "  v_mfma_f32_16x16x32_bf16 v[32:35], v[64:67], a[52:55], v[32:35]\n"
    "  ds_read_b128 v[100:103], v117 offset:32768\n"
    "  v_mfma_f32_16x16x32_bf16 v[20:23], v[68:71], a[48:51], v[20:23]\n"
    "  ds_read_b64_tr_b16 a[64:65], v124\n"
    "  v_mfma_f32_16x16x32_bf16 v[36:39], v[68:71], a[52:55], v[36:39]\n"
    "  ds_read_b64_tr_b16 a[66:67], v124 offset:1024\n"
    "  buffer_load_dwordx4 v129, s[4:7], s52 offen offset:1024 lds\n"
    "  v_mfma_f32_16x16x32_bf16 v[24:27], v[72:75], a[48:51], v[24:27]\n"
    "  ds_read_b128 v[104:107], v118 offset:32768\n"
    "  v_mfma_f32_16x16x32_bf16 v[40:43], v[72:75], a[52:55], v[40:43]\n"
    "  ds_read_b128 v[108:111], v119 offset:32768\n"
    "  v_mfma_f32_16x16x32_bf16 v[28:31], v[76:79], a[48:51], v[28:31]\n"
    "  ds_read_b64_tr_b16 a[68:69], v125\n"
    "  s_add_u32 s53, s45, 7\n"
    "  s_and_b32 s53, s53, 7\n"
    "  s_mul_i32 s53, s53, 0x00030000\n"
    "  s_add_u32 m0, s51, 0x00004000\n"
    "  s_add_u32 s53, s53, s50\n"
    "  buffer_load_dwordx4 v130, s[8:11], s53 offen lds\n"
    "  v_mfma_f32_16x16x32_bf16 v[44:47], v[76:79], a[52:55], v[44:47]\n"
    "  ds_read_b64_tr_b16 a[70:71], v125 offset:1024\n"
    "  v_mfma_f32_16x16x32_bf16 v[48:51], v[64:67], v[64:67], v[48:51]\n"
    "  ds_read_b64_tr_b16 a[72:73], v124 offset:8192\n"
    "  v_mfma_f32_16x16x32_bf16 v[52:55], v[64:67], v[112:115], v[52:55]\n"
    "  ds_read_b64_tr_b16 a[74:75], v124 offset:9216\n"
    "  buffer_load_dwordx4 v131, s[8:11], s53 offen offset:1024 lds\n"
    "  v_mfma_f32_16x16x32_bf16 v[56:59], v[112:115], a[48:51], v[56:59]\n"
    "  ds_read_b64_tr_b16 a[76:77], v125 offset:8192\n"
    "  v_mfma_f32_16x16x32_bf16 v[60:63], v[112:115], a[52:55], v[60:63]\n"
    "  ds_read_b64_tr_b16 a[78:79], v125 offset:9216\n"
    "  v_mfma_f32_16x16x32_bf16 v[16:19], v[80:83], a[56:59], v[16:19]\n"
    "  ds_read_b128 v[64:67], v120 offset:32768\n"
    "  buffer_load_dwordx4 v132, s[8:11], s53 offen offset:2048 lds\n"
    "  v_mfma_f32_16x16x32_bf16 v[32:35], v[80:83], a[60:63], v[32:35]\n"
    "  ds_read_b128 v[68:71], v121 offset:32768\n"
    "  v_mfma_f32_16x16x32_bf16 v[20:23], v[84:87], a[56:59], v[20:23]\n"
    "  ds_read_b128 v[72:75], v122 offset:32768\n"
    "  v_mfma_f32_16x16x32_bf16 v[36:39], v[84:87], a[60:63], v[36:39]\n"
    "  ds_read_b128 v[76:79], v123 offset:32768\n"
    "  buffer_load_dwordx4 v133, s[8:11], s53 offen offset:3072 lds\n"
    "  v_mfma_f32_16x16x32_bf16 v[24:27], v[88:91], a[56:59], v[24:27]\n"
    "  v_mfma_f32_16x16x32_bf16 v[40:43], v[88:91], a[60:63], v[40:43]\n"
    "  v_mfma_f32_16x16x32_bf16 v[28:31], v[92:95], a[56:59], v[28:31]\n"
    "  v_mfma_f32_16x16x32_bf16 v[44:47], v[92:95], a[60:63], v[44:47]\n"
    "  v_mfma_f32_16x16x32_bf16 v[48:51], v[80:83], v[80:83], v[48:51]\n"
    "  v_mfma_f32_16x16x32_bf16 v[52:55], v[80:83], v[112:115], v[52:55]\n"
    "  v_mfma_f32_16x16x32_bf16 v[56:59], v[112:115], a[56:59], v[56:59]\n"
    "  v_mfma_f32_16x16x32_bf16 v[60:63], v[112:115], a[60:63], v[60:63]\n"
    "  s_waitcnt vmcnt(13)\n"
    "  s_barrier\n"
    "  s_waitcnt lgkmcnt(12)\n"
    "  v_mfma_f32_16x16x32_bf16 v[16:19], v[96:99], a[64:67], v[16:19]\n"
    "  ds_read_b128 v[80:83], v116 offset:40960\n"
    "  s_lshr_b32 s52, s40, 1\n"
    "  s_add_u32 s53, s52, s45\n"
    "  s_add_u32 s53, s53, 0\n"
    "  s_and_b32 s53, s53, 7\n"
    "  s_mul_i32 s53, s53, 0x00030000\n"
    "  s_and_b32 s52, s40, 1\n"
    "  s_mul_i32 s52, s52, 0x00018000\n"
    "  s_add_u32 m0, s51, 0x00008000\n"
    "  s_add_u32 s53, s53, s52\n"
    "  buffer_load_dwordx4 v134, s[8:11], s53 offen lds\n"
    "  s_waitcnt lgkmcnt(9)\n"
    "  v_mfma_f32_16x16x32_bf16 v[32:35], v[96:99], a[68:71], v[32:35]\n"
    "  ds_read_b128 v[84:87], v117 offset:40960\n"
    "  v_mfma_f32_16x16x32_bf16 v[20:23], v[100:103], a[64:67], v[20:23]\n"
    "  ds_read_b64_tr_b16 a[80:81], v124 offset:16384\n"
    "  v_mfma_f32_16x16x32_bf16 v[36:39], v[100:103], a[68:71], v[36:39]\n"
    "  ds_read_b64_tr_b16 a[82:83], v124 offset:17408\n"
    "  buffer_load_dwordx4 v135, s[8:11], s53 offen offset:1024 lds\n"
    "  v_mfma_f32_16x16x32_bf16 v[24:27], v[104:107], a[64:67], v[24:27]\n"
    "  ds_read_b128 v[88:91], v118 offset:40960\n"
    "  v_mfma_f32_16x16x32_bf16 v[40:43], v[104:107], a[68:71], v[40:43]\n"
    "  ds_read_b128 v[92:95], v119 offset:40960\n"
    "  v_mfma_f32_16x16x32_bf16 v[28:31], v[108:111], a[64:67], v[28:31]\n"
    "  ds_read_b64_tr_b16 a[84:85], v125 offset:16384\n"
    "  buffer_load_dwordx4 v136, s[8:11], s53 offen offset:2048 lds\n"
    "  v_mfma_f32_16x16x32_bf16 v[44:47], v[108:111], a[68:71], v[44:47]\n"
    "  ds_read_b64_tr_b16 a[86:87], v125 offset:17408\n"
    "  v_mfma_f32_16x16x32_bf16 v[48:51], v[96:99], v[96:99], v[48:51]\n"
    "  ds_read_b64_tr_b16 a[88:89], v124 offset:24576\n"
    "  v_mfma_f32_16x16x32_bf16 v[52:55], v[96:99], v[112:115], v[52:55]\n"
    "  ds_read_b64_tr_b16 a[90:91], v124 offset:25600\n"
    "  buffer_load_dwordx4 v137, s[8:11], s53 offen offset:3072 lds\n"
    "  v_mfma_f32_16x16x32_bf16 v[56:59], v[112:115], a[64:67], v[56:59]\n"
    "  ds_read_b64_tr_b16 a[92:93], v125 offset:24576\n"
    "  v_mfma_f32_16x16x32_bf16 v[60:63], v[112:115], a[68:71], v[60:63]\n"
    "  ds_read_b64_tr_b16 a[94:95], v125 offset:25600\n"
    "  v_mfma_f32_16x16x32_bf16 v[16:19], v[64:67], a[72:75], v[16:19]\n"
    "  ds_read_b128 v[96:99], v120 offset:40960\n"
    "  v_mfma_f32_16x16x32_bf16 v[32:35], v[64:67], a[76:79], v[32:35]\n"
    "  ds_read_b128 v[100:103], v121 offset:40960\n"
    "  v_mfma_f32_16x16x32_bf16 v[20:23], v[68:71], a[72:75], v[20:23]\n"
    "  ds_read_b128 v[104:107], v122 offset:40960\n"
    "  v_mfma_f32_16x16x32_bf16 v[36:39], v[68:71], a[76:79], v[36:39]\n"
    "  ds_read_b128 v[108:111], v123 offset:40960\n"
    "  v_mfma_f32_16x16x32_bf16 v[24:27], v[72:75], a[72:75], v[24:27]\n"
    "  s_and_b32 s67, s40, 1\n"
    "  s_lshl_b32 s66, s67, 5\n"
    "  v_and_b32 v153, 31, v1\n"
    "  v_mfma_f32_16x16x32_bf16 v[40:43], v[72:75], a[76:79], v[40:43]\n"
    "  v_add_lshl_u32 v152, s66, v153, 7\n"
    "  v_lshrrev_b32 v151, 5, v1\n"
    "  v_bfe_u32 v153, v153, 1, 3\n"
    "  v_mfma_f32_16x16x32_bf16 v[28:31], v[76:79], a[72:75], v[28:31]\n"
    "  v_xor_b32 v151, v151, v153\n"
    "  v_lshlrev_b32 v153, 4, v151\n"
    "  s_lshl_b32 s67, s67, 15\n"
    "  v_mfma_f32_16x16x32_bf16 v[44:47], v[76:79], a[76:79], v[44:47]\n"
    "  v_add3_u32 v128, v152, v153, s67\n"
    "  v_xor_b32 v153, 2, v151\n"
    "  v_lshlrev_b32 v153, 4, v153\n"
    "  v_mfma_f32_16x16x32_bf16 v[48:51], v[64:67], v[64:67], v[48:51]\n"
    "  v_add3_u32 v129, v152, v153, s67\n"
    "  v_xor_b32 v153, 4, v151\n"
    "  v_lshlrev_b32 v153, 4, v153\n"
    "  v_mfma_f32_16x16x32_bf16 v[52:55], v[64:67], v[112:115], v[52:55]\n"
    "  v_add3_u32 v130, v152, v153, s67\n"
    "  v_xor_b32 v151, 6, v151\n"
    "  v_lshlrev_b32 v151, 4, v151\n"
    "  v_mfma_f32_16x16x32_bf16 v[56:59], v[112:115], a[72:75], v[56:59]\n"
    "  v_add3_u32 v131, v152, v151, s67\n"
    "  v_lshrrev_b32 v152, 4, v1\n"
    "  v_lshrrev_b32 v151, 1, v152\n"
    "  v_mfma_f32_16x16x32_bf16 v[60:63], v[112:115], a[76:79], v[60:63]\n"
    "  v_bfe_u32 v153, v1, 2, 2\n"
    "  v_lshl_add_u32 v151, v151, 3, v153\n"
    "  s_and_b32 s66, s40, 2\n"
    "  s_waitcnt vmcnt(10)\n"
    "  s_barrier\n"
    "  v_add_u32 v124, 0xffff0000, v124\n"
    "  v_add_u32 v125, 0xffff0000, v125\n"
    "  s_waitcnt lgkmcnt(12)\n"
    "  v_mfma_f32_16x16x32_bf16 v[16:19], v[80:83], a[80:83], v[16:19]\n"
    "  ds_read_b128 v[64:67], v116 offset:49152\n"
    "  s_lshr_b32 s52, s40, 1\n"
    "  s_add_u32 s53, s52, s45\n"
    "  s_add_u32 s53, s53, 2\n"
    "  s_and_b32 s53, s53, 7\n"
    "  s_mul_i32 s53, s53, 0x00030000\n"
    "  s_and_b32 s52, s40, 1\n"
    "  s_mul_i32 s52, s52, 0x00018000\n"
    "  s_add_u32 m0, s51, 0x0000c000\n"
    "  s_add_u32 s53, s53, s52\n"
    "  buffer_load_dwordx4 v134, s[8:11], s53 offen lds\n"
    "  s_waitcnt lgkmcnt(9)\n"
    "  v_mfma_f32_16x16x32_bf16 v[32:35], v[80:83], a[84:87], v[32:35]\n"
    "  ds_read_b128 v[68:71], v117 offset:49152\n"
    "  v_mfma_f32_16x16x32_bf16 v[20:23], v[84:87], a[80:83], v[20:23]\n"
    "  ds_read_b64_tr_b16 a[96:97], v124\n"
    "  v_mfma_f32_16x16x32_bf16 v[36:39], v[84:87], a[84:87], v[36:39]\n"
    "  ds_read_b64_tr_b16 a[98:99], v124 offset:1024\n"
    "  buffer_load_dwordx4 v135, s[8:11], s53 offen offset:1024 lds\n"
    "  v_mfma_f32_16x16x32_bf16 v[24:27], v[88:91], a[80:83], v[24:27]\n"
    "  ds_read_b128 v[72:75], v118 offset:49152\n"
    "  v_mfma_f32_16x16x32_bf16 v[40:43], v[88:91], a[84:87], v[40:43]\n"
    "  ds_read_b128 v[76:79], v119 offset:49152\n"
    "  v_mfma_f32_16x16x32_bf16 v[28:31], v[92:95], a[80:83], v[28:31]\n"
    "  ds_read_b64_tr_b16 a[100:101], v125\n"
    "  buffer_load_dwordx4 v136, s[8:11], s53 offen offset:2048 lds\n"
    "  v_mfma_f32_16x16x32_bf16 v[44:47], v[92:95], a[84:87], v[44:47]\n"
    "  ds_read_b64_tr_b16 a[102:103], v125 offset:1024\n"
    "  v_mfma_f32_16x16x32_bf16 v[48:51], v[80:83], v[80:83], v[48:51]\n"
    "  ds_read_b64_tr_b16 a[104:105], v124 offset:8192\n"
    "  v_mfma_f32_16x16x32_bf16 v[52:55], v[80:83], v[112:115], v[52:55]\n"
    "  ds_read_b64_tr_b16 a[106:107], v124 offset:9216\n"
    "  buffer_load_dwordx4 v137, s[8:11], s53 offen offset:3072 lds\n"
    "  v_mfma_f32_16x16x32_bf16 v[56:59], v[112:115], a[80:83], v[56:59]\n"
    "  ds_read_b64_tr_b16 a[108:109], v125 offset:8192\n"
    "  v_mfma_f32_16x16x32_bf16 v[60:63], v[112:115], a[84:87], v[60:63]\n"
    "  ds_read_b64_tr_b16 a[110:111], v125 offset:9216\n"
    "  v_mfma_f32_16x16x32_bf16 v[16:19], v[96:99], a[88:91], v[16:19]\n"
    "  ds_read_b128 v[80:83], v120 offset:49152\n"
    "  s_lshr_b32 s52, s40, 1\n"
    "  s_add_u32 s53, s52, s45\n"
    "  s_add_u32 s53, s53, 4\n"
    "  s_and_b32 s53, s53, 7\n"
    "  s_mul_i32 s53, s53, 0x00030000\n"
    "  s_and_b32 s52, s40, 1\n"
    "  s_mul_i32 s52, s52, 0x00018000\n"
    "  s_add_u32 m0, s51, 0x00010000\n"
    "  s_add_u32 s53, s53, s52\n"
    "  buffer_load_dwordx4 v134, s[8:11], s53 offen lds\n"
    "  v_mfma_f32_16x16x32_bf16 v[32:35], v[96:99], a[92:95], v[32:35]\n"
    "  ds_read_b128 v[84:87], v121 offset:49152\n"
    "  v_mfma_f32_16x16x32_bf16 v[20:23], v[100:103], a[88:91], v[20:23]\n"
    "  ds_read_b128 v[88:91], v122 offset:49152\n"
    "  v_mfma_f32_16x16x32_bf16 v[36:39], v[100:103], a[92:95], v[36:39]\n"
    "  ds_read_b128 v[92:95], v123 offset:49152\n"
    "  buffer_load_dwordx4 v135, s[8:11], s53 offen offset:1024 lds\n"
    "  v_mfma_f32_16x16x32_bf16 v[24:27], v[104:107], a[88:91], v[24:27]\n"
    "  v_and_b32 v150, 1, v152\n"
    "  v_add_u32 v150, s66, v150\n"
    "  v_lshrrev_b32 v153, 1, v153\n"
    "  v_mfma_f32_16x16x32_bf16 v[40:43], v[104:107], a[92:95], v[40:43]\n"
    "  v_lshlrev_b32 v153, 1, v153\n"
    "  v_xor_b32 v153, v150, v153\n"
    "  v_lshlrev_b32 v153, 5, v153\n"
    "  v_mfma_f32_16x16x32_bf16 v[28:31], v[108:111], a[88:91], v[28:31]\n"
    "  buffer_load_dwordx4 v136, s[8:11], s53 offen offset:2048 lds\n"
    "  v_lshl_add_u32 v153, v151, 7, v153\n"
    "  v_and_b32 v151, 3, v1\n"
    "  v_lshlrev_b32 v151, 3, v151\n"
    "  v_mfma_f32_16x16x32_bf16 v[44:47], v[108:111], a[92:95], v[44:47]\n"
    "  s_add_u32 s67, s67, 0x18000\n"
    "  v_add3_u32 v132, v153, v151, s67\n"
    "  v_xor_b32 v152, v152, v1\n"
    "  v_mfma_f32_16x16x32_bf16 v[48:51], v[96:99], v[96:99], v[48:51]\n"
    "  v_and_b32 v152, 1, v152\n"
    "  v_xor_b32 v168, 1, v152\n"
    "  v_mul_u32_u24 v168, 0x3f80, v168\n"
    "  v_mfma_f32_16x16x32_bf16 v[52:55], v[96:99], v[112:115], v[52:55]\n"
    "  buffer_load_dwordx4 v137, s[8:11], s53 offen offset:3072 lds\n"
    "  v_lshl_or_b32 v168, v168, 16, v168\n"
    "  v_mov_b32 v169, v168\n"
    "  v_mov_b32 v170, v168\n"
    "  v_mfma_f32_16x16x32_bf16 v[56:59], v[112:115], a[88:91], v[56:59]\n"
    "  v_mov_b32 v171, v168\n"
    "  v_mfma_f32_16x16x32_bf16 v[60:63], v[112:115], a[92:95], v[60:63]\n"
    "  s_waitcnt vmcnt(12) lgkmcnt(0)\n"
    "  s_barrier\n"
    "  v_mfma_f32_16x16x32_bf16 v[48:51], v[64:67], v[64:67], v[48:51]\n"
    "  ds_read_b128 v[96:99], v116 offset:57344\n"
    "  s_lshr_b32 s52, s40, 1\n"
    "  s_add_u32 s53, s52, s45\n"
    "  s_add_u32 s53, s53, 6\n"
    "  s_and_b32 s53, s53, 7\n"
    "  s_mul_i32 s53, s53, 0x00030000\n"
    "  s_and_b32 s52, s40, 1\n"
    "  s_mul_i32 s52, s52, 0x00018000\n"
    "  s_add_u32 m0, s51, 0x00014000\n"
    "  s_add_u32 s53, s53, s52\n"
    "  buffer_load_dwordx4 v134, s[8:11], s53 offen lds\n"
    "  v_mfma_f32_16x16x32_bf16 v[52:55], v[64:67], v[112:115], v[52:55]\n"
    "  ds_read_b64_tr_b16 a[112:113], v124 offset:16384\n"
    "  buffer_load_dwordx4 v135, s[8:11], s53 offen offset:1024 lds\n"
    "  v_mfma_f32_16x16x32_bf16 v[16:19], v[64:67], a[96:99], v[16:19]\n"
    "  ds_read_b64_tr_b16 a[114:115], v124 offset:17408\n"
    "  buffer_load_dwordx4 v136, s[8:11], s53 offen offset:2048 lds\n"
    "  v_mfma_f32_16x16x32_bf16 v[32:35], v[64:67], a[100:103], v[32:35]\n"
    "  ds_read_b128 v[100:103], v117 offset:57344\n"
    "  buffer_load_dwordx4 v137, s[8:11], s53 offen offset:3072 lds\n"
    "  v_mfma_f32_16x16x32_bf16 v[20:23], v[68:71], a[96:99], v[20:23]\n"
    "  ds_read_b128 v[64:67], v120 offset:57344\n"
    "  v_mfma_f32_16x16x32_bf16 v[36:39], v[68:71], a[100:103], v[36:39]\n"
    "  ds_read_b64_tr_b16 a[116:117], v125 offset:16384\n"
    "  v_mfma_f32_16x16x32_bf16 v[24:27], v[72:75], a[96:99], v[24:27]\n"
    "  ds_read_b64_tr_b16 a[118:119], v125 offset:17408\n"
    "  v_mfma_f32_16x16x32_bf16 v[40:43], v[72:75], a[100:103], v[40:43]\n"
    "  ds_read_b128 v[104:107], v118 offset:57344\n"
    "  v_mfma_f32_16x16x32_bf16 v[28:31], v[76:79], a[96:99], v[28:31]\n"
    "  ds_read_b128 v[108:111], v119 offset:57344\n"
    "  v_mfma_f32_16x16x32_bf16 v[44:47], v[76:79], a[100:103], v[44:47]\n"
    "  v_mfma_f32_16x16x32_bf16 v[48:51], v[80:83], v[80:83], v[48:51]\n"
    "  ds_read_b64_tr_b16 a[120:121], v124 offset:24576\n"
    "  ds_read_b64_tr_b16 a[122:123], v124 offset:25600\n"
    "  v_mfma_f32_16x16x32_bf16 v[52:55], v[80:83], v[112:115], v[52:55]\n"
    "  ds_read_b64_tr_b16 a[124:125], v125 offset:24576\n"
    "  ds_read_b64_tr_b16 a[126:127], v125 offset:25600\n"
    "  v_mfma_f32_16x16x32_bf16 v[56:59], v[112:115], a[96:99], v[56:59]\n"
    "  ds_read_b128 v[68:71], v121 offset:57344\n"
    "  ds_read_b128 v[72:75], v122 offset:57344\n"
    "  v_mfma_f32_16x16x32_bf16 v[60:63], v[112:115], a[100:103], v[60:63]\n"
    "  ds_read_b128 v[76:79], v123 offset:57344\n"
    "  v_mfma_f32_16x16x32_bf16 v[56:59], v[112:115], a[104:107], v[56:59]\n"
    "  v_mfma_f32_16x16x32_bf16 v[60:63], v[112:115], a[108:111], v[60:63]\n"
    "  v_mfma_f32_16x16x32_bf16 v[48:51], v[96:99], v[96:99], v[48:51]\n"
    "  v_mfma_f32_16x16x32_bf16 v[52:55], v[96:99], v[112:115], v[52:55]\n"
    "  s_waitcnt lgkmcnt(11)\n"
    "  v_mfma_f32_16x16x32_bf16 v[48:51], v[64:67], v[64:67], v[48:51]\n"
    "  v_mfma_f32_16x16x32_bf16 v[52:55], v[64:67], v[112:115], v[52:55]\n"
    "  v_mfma_f32_16x16x32_bf16 v[16:19], v[80:83], a[104:107], v[16:19]\n"
    "  s_mov_b32 s58, 0xaaaaaaaa\n"
    "  s_mov_b32 s59, 0xaaaaaaaa\n"
    "  s_mov_b32 s60, 0xcccccccc\n"
    "  s_mov_b32 s61, 0xcccccccc\n"
    "  v_mfma_f32_16x16x32_bf16 v[32:35], v[80:83], a[108:111], v[32:35]\n"
    "  s_nop 1\n"
    "  v_cndmask_b32_e64 v150, v52, v53, s[58:59]\n"
    "  v_cndmask_b32_e64 v151, v54, v55, s[58:59]\n"
    "  v_cndmask_b32_e64 v126, v150, v151, s[60:61]\n"
    "  v_cndmask_b32_e64 v150, v48, v49, s[58:59]\n"
    "  v_mfma_f32_16x16x32_bf16 v[20:23], v[84:87], a[104:107], v[20:23]\n"
    "  v_cndmask_b32_e64 v151, v50, v51, s[58:59]\n"
    "  v_cndmask_b32_e64 v127, v150, v151, s[60:61]\n"
    "  v_mul_f32 v126, 0x3b000000, v126\n"
    "  v_mul_f32 v127, 0x3b000000, v127\n"
    "  v_mfma_f32_16x16x32_bf16 v[36:39], v[84:87], a[108:111], v[36:39]\n"
    "  v_fma_f32 v127, -v126, v126, v127\n"
    "  v_max_f32 v127, 0, v127\n"
    "  v_cvt_pk_bf16_f32 v126, v126, v126\n"
    "  v_and_b32 v126, 0xffff0000, v126\n"
    "  v_mfma_f32_16x16x32_bf16 v[24:27], v[88:91], a[104:107], v[24:27]\n"
    "  v_cvt_pk_bf16_f32 v127, v127, v127\n"
    "  v_and_b32 v127, 0xffff0000, v127\n"
    "  v_add_f32 v127, 0x3727c5ac, v127\n"
    "  v_cvt_pk_bf16_f32 v127, v127, v127\n"
    "  v_mfma_f32_16x16x32_bf16 v[40:43], v[88:91], a[108:111], v[40:43]\n"
    "  v_and_b32 v127, 0xffff0000, v127\n"
    "  v_sqrt_f32 v127, v127\n"
    "  s_nop 0\n"
    "  v_cvt_pk_bf16_f32 v127, v127, v127\n"
    "  v_and_b32 v127, 0xffff0000, v127\n"
    "  v_mfma_f32_16x16x32_bf16 v[28:31], v[92:95], a[104:107], v[28:31]\n"
    "  v_rcp_f32 v127, v127\n"
    "  s_nop 0\n"
    "  v_mul_f32 v150, 0x3fb8aa3b, v127\n"
    "  s_lshl_b32 s67, s40, 4\n"
    "  v_and_b32 v153, 15, v1\n"
    "  v_add_lshl_u32 v151, s67, v153, 2\n"
    "  v_add_u32 v151, 0x00010000, v151\n"
    "  v_mfma_f32_16x16x32_bf16 v[44:47], v[92:95], a[108:111], v[44:47]\n"
    "  s_mov_b32 exec_lo, 0x00f0000f\n"
    "  s_mov_b32 exec_hi, 0xf0000f00\n"
    "  ds_write2st64_b32 v151, v126, v126 offset0:0 offset1:1\n"
    "  ds_write2st64_b32 v151, v127, v127 offset0:4 offset1:5\n"
    "  ds_write2st64_b32 v151, v150, v150 offset0:2 offset1:3\n"
    "  s_mov_b64 exec, -1\n"
    "  s_waitcnt lgkmcnt(0)\n"
    "  s_barrier\n"
    "  s_and_b32 s67, s2, 7\n"
    "  s_lshl_b32 s67, s67, 12\n"
    "  s_add_u32 s48, s67, 0x1000\n"
    "  s_cmp_eq_u32 s40, 0\n"
    "  s_cselect_b64 exec, -1, 0\n"
    "  buffer_load_dwordx4 v[146:149], v2, s[16:19], s48 offen sc1\n"
    "  s_mov_b64 exec, -1\n"
    "  v_lshrrev_b32 v127, 4, v1\n"
    "  s_lshl_b32 s67, s40, 6\n"
    "  s_add_u32 s67, s67, 0x10000\n"
    "  v_lshl_add_u32 v126, v127, 4, s67\n"
    "  v_mfma_f32_16x16x32_bf16 v[56:59], v[112:115], a[112:115], v[56:59]\n"
    "  ds_read_b128 v[80:83], v126\n"
    "  v_mfma_f32_16x16x32_bf16 v[60:63], v[112:115], a[116:119], v[60:63]\n"
    "  ds_read_b128 v[84:87], v126 offset:64\n"
    "  v_mfma_f32_16x16x32_bf16 v[56:59], v[112:115], a[120:123], v[56:59]\n"
    "  ds_read_b128 v[88:91], v126 offset:128\n"
    "  v_mfma_f32_16x16x32_bf16 v[60:63], v[112:115], a[124:127], v[60:63]\n"
    "  ds_read_b128 v[92:95], v126 offset:192\n"
    "  v_mfma_f32_16x16x32_bf16 v[16:19], v[96:99], a[112:115], v[16:19]\n"
    "  v_mfma_f32_16x16x32_bf16 v[32:35], v[96:99], a[116:119], v[32:35]\n"
    "  v_mfma_f32_16x16x32_bf16 v[20:23], v[100:103], a[112:115], v[20:23]\n"
    "  v_mfma_f32_16x16x32_bf16 v[36:39], v[100:103], a[116:119], v[36:39]\n"
    "  v_mfma_f32_16x16x32_bf16 v[24:27], v[104:107], a[112:115], v[24:27]\n"
    "  v_mfma_f32_16x16x32_bf16 v[40:43], v[104:107], a[116:119], v[40:43]\n"
    "  v_mfma_f32_16x16x32_bf16 v[28:31], v[108:111], a[112:115], v[28:31]\n"
    "  v_mfma_f32_16x16x32_bf16 v[44:47], v[108:111], a[116:119], v[44:47]\n"
    "  v_mfma_f32_16x16x32_bf16 v[16:19], v[64:67], a[120:123], v[16:19]\n"
    "  ds_read_b128 v[96:99], v126 offset:512\n"
    "  v_mfma_f32_16x16x32_bf16 v[32:35], v[64:67], a[124:127], v[32:35]\n"
    "  ds_read_b128 v[100:103], v126 offset:576\n"
    "  v_mfma_f32_16x16x32_bf16 v[20:23], v[68:71], a[120:123], v[20:23]\n"
    "  ds_read_b128 v[104:107], v126 offset:640\n"
    "  v_mfma_f32_16x16x32_bf16 v[36:39], v[68:71], a[124:127], v[36:39]\n"
    "  ds_read_b128 v[108:111], v126 offset:704\n"
    "  v_mfma_f32_16x16x32_bf16 v[24:27], v[72:75], a[120:123], v[24:27]\n"
    "  v_mfma_f32_16x16x32_bf16 v[40:43], v[72:75], a[124:127], v[40:43]\n"
    "  v_mfma_f32_16x16x32_bf16 v[28:31], v[76:79], a[120:123], v[28:31]\n"
    "  v_mfma_f32_16x16x32_bf16 v[44:47], v[76:79], a[124:127], v[44:47]\n"
    "  ds_read_b128 v[64:67], v126 offset:1024\n"
    "  ds_read_b128 v[68:71], v126 offset:1088\n"
    "  ds_read_b128 v[72:75], v126 offset:1152\n"
    "  ds_read_b128 v[76:79], v126 offset:1216\n"
    "  s_mov_b32 s62, 0x3f317218\n"
    "  s_mov_b32 s63, 0x3f317218\n"
    "  s_mov_b32 s54, 0x35c1a2a0\n"
    "  v_mov_b32 v4, v56\n"
    "  v_mov_b32 v5, v60\n"
    "  s_waitcnt lgkmcnt(7)\n"
    "  v_pk_fma_f32 v[16:17], v[80:81], v[4:5], v[16:17] op_sel_hi:[1,0,1] neg_lo:[1,0,0] neg_hi:[1,0,0]\n"
    "  v_pk_fma_f32 v[18:19], v[82:83], v[4:5], v[18:19] op_sel_hi:[1,0,1] neg_lo:[1,0,0] neg_hi:[1,0,0]\n"
    "  v_pk_mul_f32 v[16:17], v[16:17], v[96:97]\n"
    "  v_pk_mul_f32 v[18:19], v[18:19], v[98:99]\n"
    "  v_exp_f32_e64 v116, v16 clamp\n"
    "  v_exp_f32_e64 v117, v17 clamp\n"
    "  v_exp_f32_e64 v118, v18 clamp\n"
    "  v_exp_f32_e64 v119, v19 clamp\n"
    "  v_pk_fma_f32 v[32:33], v[80:81], v[4:5], v[32:33] op_sel:[0,1,0] op_sel_hi:[1,1,1] neg_lo:[1,0,0] neg_hi:[1,0,0]\n"
    "  v_pk_fma_f32 v[34:35], v[82:83], v[4:5], v[34:35] op_sel:[0,1,0] op_sel_hi:[1,1,1] neg_lo:[1,0,0] neg_hi:[1,0,0]\n"
    "  v_max_f32 v16, s54, v16\n"
    "  v_max_f32 v17, s54, v17\n"
    "  v_max_f32 v18, s54, v18\n"
    "  v_max_f32 v19, s54, v19\n"
    "  s_waitcnt lgkmcnt(3)\n"
    "  v_pk_mul_f32 v[32:33], v[32:33], v[64:65]\n"
    "  v_pk_mul_f32 v[34:35], v[34:35], v[66:67]\n"
    "  v_pk_fma_f32 v[16:17], v[16:17], s[62:63], v[116:117]\n"
    "  v_pk_fma_f32 v[18:19], v[18:19], s[62:63], v[118:119]\n"
    "  v_cvt_pk_bf16_f32 v56, v32, v33\n"
    "  v_cvt_pk_bf16_f32 v57, v34, v35\n"
    "  v_cvt_pk_bf16_f32 v48, v16, v17\n"
    "  v_cvt_pk_bf16_f32 v49, v18, v19\n"
    "  v_pk_fma_f32 v[20:21], v[84:85], v[4:5], v[20:21] op_sel_hi:[1,0,1] neg_lo:[1,0,0] neg_hi:[1,0,0]\n"
    "  v_pk_fma_f32 v[22:23], v[86:87], v[4:5], v[22:23] op_sel_hi:[1,0,1] neg_lo:[1,0,0] neg_hi:[1,0,0]\n"
    "  v_pk_mul_f32 v[20:21], v[20:21], v[100:101]\n"
    "  v_pk_mul_f32 v[22:23], v[22:23], v[102:103]\n"
    "  v_exp_f32_e64 v116, v20 clamp\n"
    "  v_exp_f32_e64 v117, v21 clamp\n"
    "  v_exp_f32_e64 v118, v22 clamp\n"
    "  v_exp_f32_e64 v119, v23 clamp\n"
    "  v_pk_fma_f32 v[36:37], v[84:85], v[4:5], v[36:37] op_sel:[0,1,0] op_sel_hi:[1,1,1] neg_lo:[1,0,0] neg_hi:[1,0,0]\n"
    "  v_pk_fma_f32 v[38:39], v[86:87], v[4:5], v[38:39] op_sel:[0,1,0] op_sel_hi:[1,1,1] neg_lo:[1,0,0] neg_hi:[1,0,0]\n"
    "  v_max_f32 v20, s54, v20\n"
    "  v_max_f32 v21, s54, v21\n"
    "  v_max_f32 v22, s54, v22\n"
    "  v_max_f32 v23, s54, v23\n"
    "  s_waitcnt lgkmcnt(2)\n"
    "  v_pk_mul_f32 v[36:37], v[36:37], v[68:69]\n"
    "  v_pk_mul_f32 v[38:39], v[38:39], v[70:71]\n"
    "  v_pk_fma_f32 v[20:21], v[20:21], s[62:63], v[116:117]\n"
    "  v_pk_fma_f32 v[22:23], v[22:23], s[62:63], v[118:119]\n"
    "  v_cvt_pk_bf16_f32 v58, v36, v37\n"
    "  v_cvt_pk_bf16_f32 v59, v38, v39\n"
    "  v_cvt_pk_bf16_f32 v50, v20, v21\n"
    "  v_cvt_pk_bf16_f32 v51, v22, v23\n"
    "  s_nop 1\n"
    "  v_mfma_f32_16x16x32_bf16 v[120:123], v[48:51], v[56:59], 0\n"
    "  v_mfma_f32_16x16x32_bf16 v[124:127], v[112:115], v[48:51], 0\n"
    "  v_pk_fma_f32 v[24:25], v[88:89], v[4:5], v[24:25] op_sel_hi:[1,0,1] neg_lo:[1,0,0] neg_hi:[1,0,0]\n"
    "  v_pk_fma_f32 v[26:27], v[90:91], v[4:5], v[26:27] op_sel_hi:[1,0,1] neg_lo:[1,0,0] neg_hi:[1,0,0]\n"
    "  v_pk_mul_f32 v[24:25], v[24:25], v[104:105]\n"
    "  v_pk_mul_f32 v[26:27], v[26:27], v[106:107]\n"
    "  v_exp_f32_e64 v116, v24 clamp\n"
    "  v_exp_f32_e64 v117, v25 clamp\n"
    "  v_exp_f32_e64 v118, v26 clamp\n"
    "  v_exp_f32_e64 v119, v27 clamp\n"
    "  v_pk_fma_f32 v[40:41], v[88:89], v[4:5], v[40:41] op_sel:[0,1,0] op_sel_hi:[1,1,1] neg_lo:[1,0,0] neg_hi:[1,0,0]\n"
    "  v_pk_fma_f32 v[42:43], v[90:91], v[4:5], v[42:43] op_sel:[0,1,0] op_sel_hi:[1,1,1] neg_lo:[1,0,0] neg_hi:[1,0,0]\n"
    "  v_max_f32 v24, s54, v24\n"
    "  v_max_f32 v25, s54, v25\n"
    "  v_max_f32 v26, s54, v26\n"
    "  v_max_f32 v27, s54, v27\n"
    "  s_waitcnt lgkmcnt(1)\n"
    "  v_pk_mul_f32 v[40:41], v[40:41], v[72:73]\n"
    "  v_pk_mul_f32 v[42:43], v[42:43], v[74:75]\n"
    "  v_pk_fma_f32 v[24:25], v[24:25], s[62:63], v[116:117]\n"
    "  v_pk_fma_f32 v[26:27], v[26:27], s[62:63], v[118:119]\n"
    "  v_cvt_pk_bf16_f32 v60, v40, v41\n"
    "  v_cvt_pk_bf16_f32 v61, v42, v43\n"
    "  v_cvt_pk_bf16_f32 v52, v24, v25\n"
    "  v_cvt_pk_bf16_f32 v53, v26, v27\n"
    "  v_pk_fma_f32 v[28:29], v[92:93], v[4:5], v[28:29] op_sel_hi:[1,0,1] neg_lo:[1,0,0] neg_hi:[1,0,0]\n"
    "  v_pk_fma_f32 v[30:31], v[94:95], v[4:5], v[30:31] op_sel_hi:[1,0,1] neg_lo:[1,0,0] neg_hi:[1,0,0]\n"
    "  v_pk_mul_f32 v[28:29], v[28:29], v[108:109]\n"
    "  v_pk_mul_f32 v[30:31], v[30:31], v[110:111]\n"
    "  v_exp_f32_e64 v116, v28 clamp\n"
    "  v_exp_f32_e64 v117, v29 clamp\n"
    "  v_exp_f32_e64 v118, v30 clamp\n"
    "  v_exp_f32_e64 v119, v31 clamp\n"
    "  v_pk_fma_f32 v[44:45], v[92:93], v[4:5], v[44:45] op_sel:[0,1,0] op_sel_hi:[1,1,1] neg_lo:[1,0,0] neg_hi:[1,0,0]\n"
    "  v_pk_fma_f32 v[46:47], v[94:95], v[4:5], v[46:47] op_sel:[0,1,0] op_sel_hi:[1,1,1] neg_lo:[1,0,0] neg_hi:[1,0,0]\n"
    "  v_max_f32 v28, s54, v28\n"
    "  v_max_f32 v29, s54, v29\n"
    "  v_max_f32 v30, s54, v30\n"
    "  v_max_f32 v31, s54, v31\n"
    "  s_waitcnt lgkmcnt(0)\n"
    "  v_pk_mul_f32 v[44:45], v[44:45], v[76:77]\n"
    "  v_pk_mul_f32 v[46:47], v[46:47], v[78:79]\n"
    "  v_pk_fma_f32 v[28:29], v[28:29], s[62:63], v[116:117]\n"
    "  v_pk_fma_f32 v[30:31], v[30:31], s[62:63], v[118:119]\n"
    "  v_cvt_pk_bf16_f32 v62, v44, v45\n"
    "  v_cvt_pk_bf16_f32 v63, v46, v47\n"
    "  v_cvt_pk_bf16_f32 v54, v28, v29\n"
    "  v_cvt_pk_bf16_f32 v55, v30, v31\n"
    "  s_nop 1\n"
    "  v_mfma_f32_16x16x32_bf16 v[120:123], v[52:55], v[60:63], v[120:123]\n"
    "  v_mfma_f32_16x16x32_bf16 v[124:127], v[112:115], v[52:55], v[124:127]\n"
    "  s_lshl_b32 s67, s47, 8\n"
    "  s_add_u32 s67, s67, s41\n"
    "  s_lshl_b32 s67, s67, 7\n"
    "  s_add_u32 s52, s67, 0x200000\n"
    "  s_lshl_b32 s67, s47, 5\n"
    "  s_add_u32 s67, s67, s41\n"
    "  s_lshl_b32 s67, s67, 6\n"
    "  s_add_u32 s53, s67, 0x20000\n"
    "  buffer_store_dwordx4 v[120:123], v8, s[16:19], s52 offen sc1\n"
    "  s_mov_b64 exec, 0xffff\n"
    "  buffer_store_dword v124, v9, s[16:19], s53 offen sc1\n"
    "  s_mov_b64 exec, -1\n"
    "  s_cmp_lg_u32 s40, 0\n"
    "  s_cbranch_scc1 Lnr_1%=\n"
    "  s_waitcnt vmcnt(2)\n"
    "  s_mov_b32 s55, 0x00008000\n"
    "Lc_2%=:\n"
    "  v_xor_b32 v146, s46, v146\n"
    "  v_xor_b32 v147, s46, v147\n"
    "  v_xor_b32 v148, s46, v148\n"
    "  v_xor_b32 v149, s46, v149\n"
    "  v_or3_b32 v82, v146, v147, v148\n"
    "  v_or_b32 v82, v82, v149\n"
    "  v_cmp_ne_u32 vcc, 0, v82\n"
    "  s_cbranch_vccz Lo_3%=\n"
    "  s_sub_u32 s55, s55, 1\n"
    "  s_cbranch_scc1 Lo_3%=\n"
    "  s_sleep 2\n"
    "  buffer_load_dwordx4 v[146:149], v2, s[16:19], s48 offen sc1\n"
    "  s_waitcnt vmcnt(0)\n"
    "  s_branch Lc_2%=\n"
    "Lo_3%=:\n"
    "Lnr_1%=:\n"
    "  s_waitcnt vmcnt(3)\n"
    "  s_barrier\n"
    "  ds_read_b64_tr_b16 a[128:129], v132\n"
    "  ds_read_b64_tr_b16 a[130:131], v132 offset:512\n"
    "  ds_read_b64_tr_b16 a[132:133], v132 offset:2048\n"
    "  ds_read_b64_tr_b16 a[134:135], v132 offset:2560\n"
    "  ds_read_b64_tr_b16 a[136:137], v132 offset:4096\n"
    "  ds_read_b64_tr_b16 a[138:139], v132 offset:4608\n"
    "  ds_read_b64_tr_b16 a[140:141], v132 offset:6144\n"
    "  ds_read_b64_tr_b16 a[142:143], v132 offset:6656\n"
    "  ds_read_b64_tr_b16 a[144:145], v132 offset:8192\n"
    "  ds_read_b64_tr_b16 a[146:147], v132 offset:8704\n"
    "  ds_read_b128 v[64:67], v128\n"
    "  ds_read_b128 v[68:71], v129\n"
    "  ds_read_b128 v[72:75], v130\n"
    "  ds_read_b128 v[76:79], v131\n"
    "  ds_read_b128 v[80:83], v128 offset:8192\n"
    "  s_waitcnt lgkmcnt(4)\n"
    "  v_mfma_f32_32x32x16_bf16 v[16:31], a[128:131], v[64:67], 0\n"
    "  ds_read_b128 v[84:87], v129 offset:8192\n"
    "  ds_read_b64_tr_b16 a[148:149], v132 offset:10240\n"
    "  ds_read_b64_tr_b16 a[150:151], v132 offset:10752\n"
    "  s_and_b32 s59, s40, 1\n"
    "  s_lshl1_add_u32 s59, s41, s59\n"
    "  s_lshl_b32 s59, s59, 5\n"
    "  v_mfma_f32_16x16x32_bf16 v[32:35], v[168:171], a[128:131], 0\n"
    "  s_waitcnt lgkmcnt(6)\n"
    "  v_mfma_f32_32x32x16_bf16 v[16:31], a[132:135], v[68:71], v[16:31]\n"
    "  ds_read_b128 v[64:67], v130 offset:8192\n"
    "  ds_read_b64_tr_b16 a[152:153], v132 offset:12288\n"
    "  ds_read_b64_tr_b16 a[154:155], v132 offset:12800\n"
    "  s_lshl2_add_u32 s59, s42, s59\n"
    "  s_and_b32 s58, s40, 2\n"
    "  s_add_u32 s59, s59, s58\n"
    "  v_mfma_f32_16x16x32_bf16 v[32:35], v[168:171], a[132:135], v[32:35]\n"
    "  s_waitcnt lgkmcnt(8)\n"
    "  v_mfma_f32_32x32x16_bf16 v[16:31], a[136:139], v[72:75], v[16:31]\n"
    "  ds_read_b128 v[68:71], v131 offset:8192\n"
    "  ds_read_b64_tr_b16 a[156:157], v132 offset:14336\n"
    "  ds_read_b64_tr_b16 a[158:159], v132 offset:14848\n"
    "  s_lshl_b32 s59, s59, 10\n"
    "  s_add_u32 s54, s59, 0x400000\n"
    "  buffer_store_dwordx4 v[140:143], v2, s[16:19], s54 offen sc1\n"
    "  v_mfma_f32_16x16x32_bf16 v[32:35], v[168:171], a[136:139], v[32:35]\n"
    "  s_waitcnt lgkmcnt(10)\n"
    "  v_mfma_f32_32x32x16_bf16 v[16:31], a[140:143], v[76:79], v[16:31]\n"
    "  ds_read_b128 v[72:75], v128 offset:16384\n"
    "  ds_read_b64_tr_b16 a[160:161], v132 offset:16384\n"
    "  ds_read_b64_tr_b16 a[162:163], v132 offset:16896\n"
    "  buffer_store_dwordx4 v[140:143], v2, s[16:19], s54 offen offset:1024 sc1\n"
    "  s_lshl_b32 s59, s2, 7\n"
    "  s_add_u32 s54, s59, 0x180000\n"
    "  v_mfma_f32_16x16x32_bf16 v[32:35], v[168:171], a[140:143], v[32:35]\n"
    "  s_waitcnt lgkmcnt(12)\n"
    "  v_mfma_f32_32x32x16_bf16 v[16:31], a[144:147], v[80:83], v[16:31]\n"
    "  ds_read_b128 v[76:79], v129 offset:16384\n"
    "  ds_read_b64_tr_b16 a[164:165], v132 offset:18432\n"
    "  ds_read_b64_tr_b16 a[166:167], v132 offset:18944\n"
    "  v_lshrrev_b32 v177, 3, v1\n"
    "  v_and_b32 v176, 7, v1\n"
    "  v_lshlrev_b32 v176, 3, v176\n"
    "  v_mfma_f32_16x16x32_bf16 v[32:35], v[168:171], a[144:147], v[32:35]\n"
    "  s_waitcnt lgkmcnt(12)\n"
    "  v_mfma_f32_32x32x16_bf16 v[16:31], a[148:151], v[84:87], v[16:31]\n"
    "  ds_read_b128 v[80:83], v130 offset:16384\n"
    "  ds_read_b64_tr_b16 a[168:169], v132 offset:20480\n"
    "  ds_read_b64_tr_b16 a[170:171], v132 offset:20992\n"
    "  v_lshl_add_u32 v144, v177, 15, v176\n"
    "  s_mov_b32 s60, 0x03030303\n"
    "  s_mov_b32 s61, 0x03030303\n"
    "  v_mfma_f32_16x16x32_bf16 v[32:35], v[168:171], a[148:151], v[32:35]\n"
    "  s_waitcnt lgkmcnt(12)\n"
    "  v_mfma_f32_32x32x16_bf16 v[16:31], a[152:155], v[64:67], v[16:31]\n"
    "  ds_read_b128 v[84:87], v131 offset:16384\n"
    "  ds_read_b64_tr_b16 a[172:173], v132 offset:22528\n"
    "  ds_read_b64_tr_b16 a[174:175], v132 offset:23040\n"
    "  s_cmp_eq_u32 s40, 0\n"
    "  s_cselect_b64 exec, -1, 0\n"
    "  buffer_store_dwordx2 v[140:141], v144, s[16:19], s54 offen sc1\n"
    "  s_cselect_b64 exec, s[60:61], 0\n"
    "  buffer_store_dwordx2 v[140:141], v144, s[16:19], s54 offen offset:64 sc1\n"
    "  s_mov_b64 exec, -1\n"
    "  s_lshl_b32 s59, s44, 2\n"
    "  s_lshl4_add_u32 s59, s43, s59\n"
    "  v_mfma_f32_16x16x32_bf16 v[32:35], v[168:171], a[152:155], v[32:35]\n"
    "  s_waitcnt lgkmcnt(12)\n"
    "  v_mfma_f32_32x32x16_bf16 v[16:31], a[156:159], v[68:71], v[16:31]\n"
    "  ds_read_b128 v[64:67], v128 offset:24576\n"
    "  ds_read_b64_tr_b16 a[176:177], v132 offset:24576\n"
    "  ds_read_b64_tr_b16 a[178:179], v132 offset:25088\n"
    "  s_add_u32 s59, s59, s40\n"
    "  s_lshl_b32 s59, s59, 8\n"
    "  s_add_u32 s54, s59, 0x40000\n"
    "  v_mfma_f32_16x16x32_bf16 v[32:35], v[168:171], a[156:159], v[32:35]\n"
    "  s_lshl2_add_u32 s67, s44, s40\n"
    "  s_lshl_b32 s67, s67, 15\n"
    "  s_add_u32 s29, s67, 0x100000\n"
    "  s_lshl_b32 s30, s40, 3\n"
    "  s_add_u32 s28, s30, 28\n"
    "  s_and_b32 s28, s28, 31\n"
    "  s_lshl_b32 s28, s28, 10\n"
    "  s_add_u32 s28, s28, s29\n"
    "  buffer_load_dwordx4 v[240:243], v2, s[16:19], s28 offen sc1\n"
    "  s_waitcnt lgkmcnt(12)\n"
    "  v_mfma_f32_32x32x16_bf16 v[16:31], a[160:163], v[72:75], v[16:31]\n"
    "  ds_read_b128 v[68:71], v129 offset:24576\n"
    "  ds_read_b64_tr_b16 a[180:181], v132 offset:26624\n"
    "  ds_read_b64_tr_b16 a[182:183], v132 offset:27136\n"
    "  v_lshlrev_b32 v178, 2, v1\n"
    "  buffer_store_dword v140, v178, s[16:19], s54 offen sc1\n"
    "  s_cmp_lg_u32 s40, 0\n"
    "  s_cbranch_scc1 Lnr_4%=\n"
    "  s_lshl_b32 s67, s2, 12\n"
    "  s_add_u32 s32, s67, 0x200000\n"
    "  s_and_b32 s67, s2, 7\n"
    "  s_lshr_b32 s67, s67, 2\n"
    "  s_lshr_b32 s66, s2, 3\n"
    "  s_lshl_b32 s66, s66, 11\n"
    "  s_lshl4_add_u32 s67, s67, s66\n"
    "  s_add_u32 s33, s67, 0x20000\n"
    "  v_lshrrev_b32 v115, 1, v1\n"
    "  v_and_b32 v114, 1, v1\n"
    "  v_lshlrev_b32 v114, 5, v114\n"
    "  v_lshl_add_u32 v112, v115, 6, v114\n"
    "  buffer_load_dwordx4 v[88:91], v2, s[16:19], s32 offen sc1\n"
    "  buffer_load_dwordx4 v[92:95], v2, s[16:19], s32 offen offset:1024 sc1\n"
    "  buffer_load_dwordx4 v[96:99], v2, s[16:19], s32 offen offset:2048 sc1\n"
    "  buffer_load_dwordx4 v[100:103], v2, s[16:19], s32 offen offset:3072 sc1\n"
    "  buffer_load_dwordx4 v[104:107], v112, s[16:19], s33 offen sc1\n"
    "Lnr_4%=:\n"
    "  v_mfma_f32_16x16x32_bf16 v[32:35], v[168:171], a[160:163], v[32:35]\n"
    "  s_waitcnt lgkmcnt(12)\n"
    "  v_mfma_f32_32x32x16_bf16 v[16:31], a[164:167], v[76:79], v[16:31]\n"
    "  ds_read_b128 v[72:75], v130 offset:24576\n"
    "  ds_read_b64_tr_b16 a[184:185], v132 offset:28672\n"
    "  ds_read_b64_tr_b16 a[186:187], v132 offset:29184\n"
    "  v_and_b32 v59, 15, v1\n"
    "  s_lshl_b32 s67, s40, 7\n"
    "  s_add_u32 s67, s67, 0x10600\n"
    "  v_mfma_f32_16x16x32_bf16 v[32:35], v[168:171], a[164:167], v[32:35]\n"
    "  buffer_load_dwordx4 v[244:247], v2, s[16:19], s28 offen offset:1024 sc1\n"
    "  s_waitcnt lgkmcnt(12)\n"
    "  v_mfma_f32_32x32x16_bf16 v[16:31], a[168:171], v[80:83], v[16:31]\n"
    "  ds_read_b128 v[76:79], v131 offset:24576\n"
    "  ds_read_b64_tr_b16 a[188:189], v132 offset:30720\n"
    "  ds_read_b64_tr_b16 a[190:191], v132 offset:31232\n"
    "  v_lshl_add_u32 v56, v59, 2, s67\n"
    "  v_lshrrev_b32 v59, 5, v1\n"
    "  s_lshl_b32 s67, s40, 7\n"
    "  v_mfma_f32_16x16x32_bf16 v[32:35], v[168:171], a[168:171], v[32:35]\n"
    "  s_waitcnt lgkmcnt(12)\n"
    "  v_mfma_f32_32x32x16_bf16 v[16:31], a[172:175], v[84:87], v[16:31]\n"
    "  s_and_b32 s51, s40, 1\n"
    "  s_lshl_b32 s51, s51, 16\n"
    "  s_sub_u32 s51, 0x00008000, s51\n"
    "  v_add_u32 v128, s51, v128\n"
    "  v_add_u32 v129, s51, v129\n"
    "  v_add_u32 v130, s51, v130\n"
    "  v_add_u32 v131, s51, v131\n"
    "  ds_read_b128 v[80:83], v128\n"
    "  s_and_b32 s50, s40, 1\n"
    "  s_lshl_b32 s50, s50, 16\n"
    "  s_sub_u32 s50, 0x00008000, s50\n"
    "  v_add_u32 v132, s50, v132\n"
    "  ds_read_b64_tr_b16 a[192:193], v132\n"
    "  ds_read_b64_tr_b16 a[194:195], v132 offset:512\n"
    "  s_add_u32 s67, s67, 0x10600\n"
    "  v_lshl_add_u32 v61, v59, 4, s67\n"
    "  v_and_b32 v59, 31, v1\n"
    "  v_mfma_f32_16x16x32_bf16 v[32:35], v[168:171], a[172:175], v[32:35]\n"
    "  buffer_load_dwordx4 v[248:251], v2, s[16:19], s28 offen offset:2048 sc1\n"
    "  s_waitcnt lgkmcnt(12)\n"
    "  v_mfma_f32_32x32x16_bf16 v[16:31], a[176:179], v[64:67], v[16:31]\n"
    "  ds_read_b128 v[84:87], v129\n"
    "  ds_read_b64_tr_b16 a[196:197], v132 offset:2048\n"
    "  ds_read_b64_tr_b16 a[198:199], v132 offset:2560\n"
    "  s_and_b32 s67, s40, 1\n"
    "  s_lshl_b32 s67, s67, 7\n"
    "  s_add_u32 s67, s67, 0x10000\n"
    "  v_mfma_f32_16x16x32_bf16 v[32:35], v[168:171], a[176:179], v[32:35]\n"
    "  s_waitcnt lgkmcnt(12)\n"
    "  v_mfma_f32_32x32x16_bf16 v[16:31], a[180:183], v[68:71], v[16:31]\n"
    "  ds_read_b128 v[64:67], v130\n"
    "  ds_read_b64_tr_b16 a[200:201], v132 offset:4096\n"
    "  ds_read_b64_tr_b16 a[202:203], v132 offset:4608\n"
    "  v_lshl_add_u32 v60, v59, 2, s67\n"
    "  ds_read_b32 v57, v60\n"
    "  ds_read_b32 v58, v60 offset:512\n"
    "  v_mfma_f32_16x16x32_bf16 v[32:35], v[168:171], a[180:183], v[32:35]\n"
    "  buffer_load_dwordx4 v[252:255], v2, s[16:19], s28 offen offset:3072 sc1\n"
    "  s_waitcnt lgkmcnt(11)\n"
    "  v_mfma_f32_32x32x16_bf16 v[16:31], a[184:187], v[72:75], v[16:31]\n"
    "  ds_read_b128 v[68:71], v131\n"
    "  ds_read_b64_tr_b16 a[204:205], v132 offset:6144\n"
    "  ds_read_b64_tr_b16 a[206:207], v132 offset:6656\n"
    "  v_mfma_f32_16x16x32_bf16 v[32:35], v[168:171], a[184:187], v[32:35]\n"
    "  v_mfma_f32_32x32x16_bf16 v[16:31], a[188:191], v[76:79], v[16:31]\n"
    "  ds_read_b128 v[72:75], v128 offset:8192\n"
    "  ds_read_b64_tr_b16 a[208:209], v132 offset:8192\n"
    "  ds_read_b64_tr_b16 a[210:211], v132 offset:8704\n"
    "  v_mfma_f32_16x16x32_bf16 v[32:35], v[168:171], a[188:191], v[32:35]\n"
    "  s_add_u32 s28, s30, 20\n"
    "  s_and_b32 s28, s28, 31\n"
    "  s_lshl_b32 s28, s28, 10\n"
    "  s_add_u32 s28, s28, s29\n"
    "  buffer_load_dwordx4 v[208:211], v2, s[16:19], s28 offen sc1\n"
    "  s_waitcnt lgkmcnt(11)\n"
    "  v_mfma_f32_32x32x16_bf16 v[16:31], a[192:195], v[80:83], v[16:31]\n"
    "  ds_read_b128 v[76:79], v129 offset:8192\n"
    "  ds_read_b64_tr_b16 a[212:213], v132 offset:10240\n"
    "  ds_read_b64_tr_b16 a[214:215], v132 offset:10752\n"
    "  v_mfma_f32_32x32x16_bf16 v[16:31], a[196:199], v[84:87], v[16:31]\n"
    "  ds_read_b128 v[80:83], v130 offset:8192\n"
    "  ds_read_b64_tr_b16 a[216:217], v132 offset:12288\n"
    "  ds_read_b64_tr_b16 a[218:219], v132 offset:12800\n"
    "  buffer_load_dwordx4 v[212:215], v2, s[16:19], s28 offen offset:1024 sc1\n"
    "  s_waitcnt lgkmcnt(9)\n"
    "  v_mfma_f32_32x32x16_bf16 v[16:31], a[200:203], v[64:67], v[16:31]\n"
    "  ds_read_b128 v[84:87], v131 offset:8192\n"
    "  ds_read_b64_tr_b16 a[220:221], v132 offset:14336\n"
    "  ds_read_b64_tr_b16 a[222:223], v132 offset:14848\n"
    "  v_mfma_f32_32x32x16_bf16 v[16:31], a[204:207], v[68:71], v[16:31]\n"
    "  ds_read_b128 v[64:67], v128 offset:16384\n"
    "  ds_read_b64_tr_b16 a[224:225], v132 offset:16384\n"
    "  ds_read_b64_tr_b16 a[226:227], v132 offset:16896\n"
    "  buffer_load_dwordx4 v[216:219], v2, s[16:19], s28 offen offset:2048 sc1\n"
    "  s_waitcnt lgkmcnt(9)\n"
    "  v_mfma_f32_32x32x16_bf16 v[16:31], a[208:211], v[72:75], v[16:31]\n"
    "  ds_read_b128 v[68:71], v129 offset:16384\n"
    "  ds_read_b64_tr_b16 a[228:229], v132 offset:18432\n"
    "  ds_read_b64_tr_b16 a[230:231], v132 offset:18944\n"
    "  v_mfma_f32_32x32x16_bf16 v[16:31], a[212:215], v[76:79], v[16:31]\n"
    "  ds_read_b128 v[72:75], v130 offset:16384\n"
    "  ds_read_b64_tr_b16 a[232:233], v132 offset:20480\n"
    "  ds_read_b64_tr_b16 a[234:235], v132 offset:20992\n"
    "  buffer_load_dwordx4 v[220:223], v2, s[16:19], s28 offen offset:3072 sc1\n"
    "  s_waitcnt lgkmcnt(9)\n"
    "  v_mfma_f32_32x32x16_bf16 v[16:31], a[216:219], v[80:83], v[16:31]\n"
    "  ds_read_b128 v[76:79], v131 offset:16384\n"
    "  ds_read_b64_tr_b16 a[236:237], v132 offset:22528\n"
    "  ds_read_b64_tr_b16 a[238:239], v132 offset:23040\n"
    "  v_mfma_f32_32x32x16_bf16 v[16:31], a[220:223], v[84:87], v[16:31]\n"
    "  ds_read_b128 v[80:83], v128 offset:24576\n"
    "  ds_read_b64_tr_b16 a[240:241], v132 offset:24576\n"
    "  ds_read_b64_tr_b16 a[242:243], v132 offset:25088\n"
    "  s_add_u32 s28, s30, 16\n"
    "  s_and_b32 s28, s28, 31\n"
    "  s_lshl_b32 s28, s28, 10\n"
    "  s_add_u32 s28, s28, s29\n"
    "  buffer_load_dwordx4 v[192:195], v2, s[16:19], s28 offen sc1\n"
    "  s_waitcnt lgkmcnt(9)\n"
    "  v_mfma_f32_32x32x16_bf16 v[16:31], a[224:227], v[64:67], v[16:31]\n"
    "  ds_read_b128 v[84:87], v129 offset:24576\n"
    "  ds_read_b64_tr_b16 a[244:245], v132 offset:26624\n"
    "  ds_read_b64_tr_b16 a[246:247], v132 offset:27136\n"
    "  v_mfma_f32_32x32x16_bf16 v[16:31], a[228:231], v[68:71], v[16:31]\n"
    "  ds_read_b128 v[64:67], v130 offset:24576\n"
    "  ds_read_b64_tr_b16 a[248:249], v132 offset:28672\n"
    "  ds_read_b64_tr_b16 a[250:251], v132 offset:29184\n"
    "  buffer_load_dwordx4 v[196:199], v2, s[16:19], s28 offen offset:1024 sc1\n"
    "  s_waitcnt lgkmcnt(6)\n"
    "  v_mfma_f32_32x32x16_bf16 v[16:31], a[232:235], v[72:75], v[16:31]\n"
    "  ds_read_b128 v[68:71], v131 offset:24576\n"
    "  ds_read_b64_tr_b16 a[252:253], v132 offset:30720\n"
    "  ds_read_b64_tr_b16 a[254:255], v132 offset:31232\n"
    "  v_mfma_f32_32x32x16_bf16 v[16:31], a[236:239], v[76:79], v[16:31]\n"
    "  buffer_load_dwordx4 v[200:203], v2, s[16:19], s28 offen offset:2048 sc1\n"
    "  v_mfma_f32_32x32x16_bf16 v[16:31], a[240:243], v[80:83], v[16:31]\n"
    "  s_waitcnt lgkmcnt(6)\n"
    "  v_mfma_f32_32x32x16_bf16 v[16:31], a[244:247], v[84:87], v[16:31]\n"
    "  buffer_load_dwordx4 v[204:207], v2, s[16:19], s28 offen offset:3072 sc1\n"
    "  s_waitcnt lgkmcnt(3)\n"
    "  v_mfma_f32_32x32x16_bf16 v[16:31], a[248:251], v[64:67], v[16:31]\n"
    "  s_waitcnt lgkmcnt(0)\n"
    "  v_mfma_f32_32x32x16_bf16 v[16:31], a[252:255], v[68:71], v[16:31]\n"
    "  v_mov_b32 v6, v32\n"
    "  v_mov_b32 v7, v33\n"
    "  ds_write2_b32 v56, v6, v7 offset0:0 offset1:16\n"
    "  s_waitcnt vmcnt(11) lgkmcnt(0)\n"
    "  s_barrier\n"
    "  s_lshl2_add_u32 s48, s2, 0x9000\n"
    "  s_cmp_eq_u32 s40, 0\n"
    "  s_cselect_b64 exec, 0xff, 0\n"
    "  buffer_store_dword v154, v179, s[16:19], s48 offen sc1\n"
    "  s_mov_b64 exec, -1\n"
    "  s_cmp_lg_u32 s40, 0\n"
    "  s_cbranch_scc1 Lnr_5%=\n"
    "  s_lshl_b32 s67, s2, 7\n"
    "  s_add_u32 s39, s67, 0x180000\n"
    "  v_lshrrev_b32 v115, 3, v1\n"
    "  v_and_b32 v114, 7, v1\n"
    "  v_lshlrev_b32 v114, 3, v114\n"
    "  v_lshl_add_u32 v113, v115, 15, v114\n"
    "  s_waitcnt vmcnt(12)\n"
    "  v_max3_u32 v115, v88, v92, v96\n"
    "  v_max3_u32 v115, v115, v100, v104\n"
    "  v_max3_u32 v115, v115, v105, v106\n"
    "  v_max_u32 v115, v115, v107\n"
    "  v_cmp_eq_u32 vcc, -1, v115\n"
    "  s_cbranch_vccnz Lrf_6%=\n"
    "  v_pk_add_f32 v[88:89], v[88:89], v[92:93]\n"
    "  v_pk_add_f32 v[96:97], v[96:97], v[100:101]\n"
    "  v_pk_add_f32 v[90:91], v[90:91], v[94:95]\n"
    "  v_pk_add_f32 v[98:99], v[98:99], v[102:103]\n"
    "  v_pk_add_f32 v[88:89], v[88:89], v[96:97]\n"
    "  v_pk_add_f32 v[90:91], v[90:91], v[98:99]\n"
    "  s_nop 0\n"
    "  v_add_f32_dpp v88, v88, v88 row_ror:8 row_mask:0xf bank_mask:0xf bound_ctrl:1\n"
    "  v_add_f32_dpp v89, v89, v89 row_ror:8 row_mask:0xf bank_mask:0xf bound_ctrl:1\n"
    "  v_add_f32_dpp v90, v90, v90 row_ror:8 row_mask:0xf bank_mask:0xf bound_ctrl:1\n"
    "  v_add_f32_dpp v91, v91, v91 row_ror:8 row_mask:0xf bank_mask:0xf bound_ctrl:1\n"
    "  v_add_f32_dpp v104, v104, v104 quad_perm:[2,3,0,1] row_mask:0xf bank_mask:0xf bound_ctrl:1\n"
    "  v_add_f32_dpp v105, v105, v105 quad_perm:[2,3,0,1] row_mask:0xf bank_mask:0xf bound_ctrl:1\n"
    "  v_add_f32_dpp v106, v106, v106 quad_perm:[2,3,0,1] row_mask:0xf bank_mask:0xf bound_ctrl:1\n"
    "  v_add_f32_dpp v107, v107, v107 quad_perm:[2,3,0,1] row_mask:0xf bank_mask:0xf bound_ctrl:1\n"
    "  v_mov_b32 v92, v88\n"
    "  v_mov_b32 v93, v89\n"
    "  v_mov_b32 v94, v90\n"
    "  v_mov_b32 v95, v91\n"
    "  v_permlane16_swap_b32 v88, v92\n"
    "  v_permlane16_swap_b32 v89, v93\n"
    "  v_permlane16_swap_b32 v90, v94\n"
    "  v_permlane16_swap_b32 v91, v95\n"
    "  v_add_f32 v88, v88, v92\n"
    "  v_add_f32 v89, v89, v93\n"
    "  v_add_f32 v90, v90, v94\n"
    "  v_add_f32 v91, v91, v95\n"
    "  v_mov_b32 v92, v88\n"
    "  v_mov_b32 v93, v89\n"
    "  v_mov_b32 v94, v90\n"
    "  v_mov_b32 v95, v91\n"
    "  v_permlane32_swap_b32 v88, v92\n"
    "  v_permlane32_swap_b32 v89, v93\n"
    "  v_permlane32_swap_b32 v90, v94\n"
    "  v_permlane32_swap_b32 v91, v95\n"
    "  v_add_f32 v88, v88, v92\n"
    "  v_add_f32 v89, v89, v93\n"
    "  v_add_f32 v90, v90, v94\n"
    "  v_add_f32 v91, v91, v95\n"
    "  v_cvt_pk_bf16_f32 v100, v88, v89\n"
    "  v_cvt_pk_bf16_f32 v101, v90, v91\n"
    "  buffer_store_dwordx2 v[100:101], v113, s[16:19], s39 offen sc1\n"
    "  s_and_b32 s53, s2, 3\n"
    "  s_cmp_lg_u32 s53, 0\n"
    "  s_cbranch_scc1 Lrz_8%=\n"
    "  v_add_f32_dpp v104, v104, v104 row_ror:4 row_mask:0xf bank_mask:0xf bound_ctrl:1\n"
    "  v_add_f32_dpp v105, v105, v105 row_ror:4 row_mask:0xf bank_mask:0xf bound_ctrl:1\n"
    "  v_add_f32_dpp v106, v106, v106 row_ror:4 row_mask:0xf bank_mask:0xf bound_ctrl:1\n"
    "  v_add_f32_dpp v107, v107, v107 row_ror:4 row_mask:0xf bank_mask:0xf bound_ctrl:1\n"
    "  v_add_f32_dpp v104, v104, v104 row_ror:8 row_mask:0xf bank_mask:0xf bound_ctrl:1\n"
    "  v_add_f32_dpp v105, v105, v105 row_ror:8 row_mask:0xf bank_mask:0xf bound_ctrl:1\n"
    "  v_add_f32_dpp v106, v106, v106 row_ror:8 row_mask:0xf bank_mask:0xf bound_ctrl:1\n"
    "  v_add_f32_dpp v107, v107, v107 row_ror:8 row_mask:0xf bank_mask:0xf bound_ctrl:1\n"
    "  v_mov_b32 v92, v104\n"
    "  v_mov_b32 v93, v105\n"
    "  v_mov_b32 v94, v106\n"
    "  v_mov_b32 v95, v107\n"
    "  v_permlane16_swap_b32 v104, v92\n"
    "  v_permlane16_swap_b32 v105, v93\n"
    "  v_permlane16_swap_b32 v106, v94\n"
    "  v_permlane16_swap_b32 v107, v95\n"
    "  v_add_f32 v104, v104, v92\n"
    "  v_add_f32 v105, v105, v93\n"
    "  v_add_f32 v106, v106, v94\n"
    "  v_add_f32 v107, v107, v95\n"
    "  v_mov_b32 v92, v104\n"
    "  v_mov_b32 v93, v105\n"
    "  v_mov_b32 v94, v106\n"
    "  v_mov_b32 v95, v107\n"
    "  v_permlane32_swap_b32 v104, v92\n"
    "  v_permlane32_swap_b32 v105, v93\n"
    "  v_permlane32_swap_b32 v106, v94\n"
    "  v_permlane32_swap_b32 v107, v95\n"
    "  v_add_f32 v104, v104, v92\n"
    "  v_add_f32 v105, v105, v93\n"
    "  v_add_f32 v106, v106, v94\n"
    "  v_add_f32 v107, v107, v95\n"
    "  v_cvt_pk_bf16_f32 v102, v104, v105\n"
    "  v_cvt_pk_bf16_f32 v103, v106, v107\n"
    "  s_mov_b32 exec_lo, 0x03030303\n"
    "  s_mov_b32 exec_hi, 0x03030303\n"
    "  buffer_store_dwordx2 v[102:103], v113, s[16:19], s39 offen offset:64 sc1\n"
    "  s_mov_b64 exec, -1\n"
    "Lrz_8%=:\n"
    "  s_mov_b32 s38, 1\n"
    "  s_branch Lre_7%=\n"
    "Lrf_6%=:\n"
    "  buffer_load_dwordx4 v[88:91], v2, s[16:19], s32 offen sc1\n"
    "  buffer_load_dwordx4 v[92:95], v2, s[16:19], s32 offen offset:1024 sc1\n"
    "  buffer_load_dwordx4 v[96:99], v2, s[16:19], s32 offen offset:2048 sc1\n"
    "  buffer_load_dwordx4 v[100:103], v2, s[16:19], s32 offen offset:3072 sc1\n"
    "  buffer_load_dwordx4 v[104:107], v112, s[16:19], s33 offen sc1\n"
    "  s_mov_b32 s38, 0\n"
    "Lre_7%=:\n"
    "Lnr_5%=:\n"
    "  ds_read_b128 v[32:35], v61\n"
    "  ds_read_b128 v[36:39], v61 offset:32\n"
    "  ds_read_b128 v[40:43], v61 offset:64\n"
    "  ds_read_b128 v[44:47], v61 offset:96\n"
    "  v_xor_b32 v81, 128, v61\n"
    "  ds_read_b128 v[64:67], v81\n"
    "  ds_read_b128 v[68:71], v81 offset:32\n"
    "  ds_read_b128 v[72:75], v81 offset:64\n"
    "  ds_read_b128 v[76:79], v81 offset:96\n"
    "  v_xor_b32 v80, 128, v56\n"
    "  ds_read2_b32 v[82:83], v80 offset0:0 offset1:16\n"
    "  s_mov_b32 s62, 0x3f317218\n"
    "  s_mov_b32 s63, 0x3f317218\n"
    "  s_mov_b32 s54, 0x35c1a2a0\n"
    "  s_waitcnt lgkmcnt(4)\n"
    "  v_pk_add_f32 v[32:33], v[32:33], v[64:65]\n"
    "  v_pk_add_f32 v[34:35], v[34:35], v[66:67]\n"
    "  s_waitcnt lgkmcnt(3)\n"
    "  v_pk_add_f32 v[36:37], v[36:37], v[68:69]\n"
    "  v_pk_add_f32 v[38:39], v[38:39], v[70:71]\n"
    "  s_waitcnt lgkmcnt(2)\n"
    "  v_pk_add_f32 v[40:41], v[40:41], v[72:73]\n"
    "  v_pk_add_f32 v[42:43], v[42:43], v[74:75]\n"
    "  s_waitcnt lgkmcnt(1)\n"
    "  v_pk_add_f32 v[44:45], v[44:45], v[76:77]\n"
    "  v_pk_add_f32 v[46:47], v[46:47], v[78:79]\n"
    "  s_waitcnt lgkmcnt(0)\n"
    "  v_pk_add_f32 v[6:7], v[6:7], v[82:83]\n"
    "  v_pk_fma_f32 v[16:17], v[32:33], v[56:57], v[16:17] op_sel:[0,1,0] op_sel_hi:[1,1,1] neg_lo:[1,0,0] neg_hi:[1,0,0]\n"
    "  v_pk_fma_f32 v[18:19], v[34:35], v[56:57], v[18:19] op_sel:[0,1,0] op_sel_hi:[1,1,1] neg_lo:[1,0,0] neg_hi:[1,0,0]\n"
    "  v_pk_mul_f32 v[16:17], v[16:17], v[58:59] op_sel_hi:[1,0]\n"
    "  v_pk_mul_f32 v[18:19], v[18:19], v[58:59] op_sel_hi:[1,0]\n"
    "  v_exp_f32_e64 v60, v16 clamp\n"
    "  v_exp_f32_e64 v61, v17 clamp\n"
    "  v_exp_f32_e64 v62, v18 clamp\n"
    "  v_exp_f32_e64 v63, v19 clamp\n"
    "  v_max_f32 v16, s54, v16\n"
    "  v_max_f32 v17, s54, v17\n"
    "  v_max_f32 v18, s54, v18\n"
    "  v_max_f32 v19, s54, v19\n"
    "  v_pk_fma_f32 v[16:17], v[16:17], s[62:63], v[60:61]\n"
    "  v_pk_fma_f32 v[18:19], v[18:19], s[62:63], v[62:63]\n"
    "  v_cvt_pk_bf16_f32 v48, v16, v17\n"
    "  v_cvt_pk_bf16_f32 v49, v18, v19\n"
    "  v_pk_fma_f32 v[20:21], v[36:37], v[56:57], v[20:21] op_sel:[0,1,0] op_sel_hi:[1,1,1] neg_lo:[1,0,0] neg_hi:[1,0,0]\n"
    "  v_pk_fma_f32 v[22:23], v[38:39], v[56:57], v[22:23] op_sel:[0,1,0] op_sel_hi:[1,1,1] neg_lo:[1,0,0] neg_hi:[1,0,0]\n"
    "  v_pk_mul_f32 v[20:21], v[20:21], v[58:59] op_sel_hi:[1,0]\n"
    "  v_pk_mul_f32 v[22:23], v[22:23], v[58:59] op_sel_hi:[1,0]\n"
    "  v_exp_f32_e64 v60, v20 clamp\n"
    "  v_exp_f32_e64 v61, v21 clamp\n"
    "  v_exp_f32_e64 v62, v22 clamp\n"
    "  v_exp_f32_e64 v63, v23 clamp\n"
    "  v_max_f32 v20, s54, v20\n"
    "  v_max_f32 v21, s54, v21\n"
    "  v_max_f32 v22, s54, v22\n"
    "  v_max_f32 v23, s54, v23\n"
    "  v_pk_fma_f32 v[20:21], v[20:21], s[62:63], v[60:61]\n"
    "  v_pk_fma_f32 v[22:23], v[22:23], s[62:63], v[62:63]\n"
    "  v_cvt_pk_bf16_f32 v50, v20, v21\n"
    "  v_cvt_pk_bf16_f32 v51, v22, v23\n"
    "  v_pk_fma_f32 v[24:25], v[40:41], v[56:57], v[24:25] op_sel:[0,1,0] op_sel_hi:[1,1,1] neg_lo:[1,0,0] neg_hi:[1,0,0]\n"
    "  v_pk_fma_f32 v[26:27], v[42:43], v[56:57], v[26:27] op_sel:[0,1,0] op_sel_hi:[1,1,1] neg_lo:[1,0,0] neg_hi:[1,0,0]\n"
    "  v_pk_mul_f32 v[24:25], v[24:25], v[58:59] op_sel_hi:[1,0]\n"
    "  v_pk_mul_f32 v[26:27], v[26:27], v[58:59] op_sel_hi:[1,0]\n"
    "  v_exp_f32_e64 v60, v24 clamp\n"
    "  v_exp_f32_e64 v61, v25 clamp\n"
    "  v_exp_f32_e64 v62, v26 clamp\n"
    "  v_exp_f32_e64 v63, v27 clamp\n"
    "  v_max_f32 v24, s54, v24\n"
    "  v_max_f32 v25, s54, v25\n"
    "  v_max_f32 v26, s54, v26\n"
    "  v_max_f32 v27, s54, v27\n"
    "  v_pk_fma_f32 v[24:25], v[24:25], s[62:63], v[60:61]\n"
    "  v_pk_fma_f32 v[26:27], v[26:27], s[62:63], v[62:63]\n"
    "  v_cvt_pk_bf16_f32 v52, v24, v25\n"
    "  v_cvt_pk_bf16_f32 v53, v26, v27\n"
    "  v_pk_fma_f32 v[28:29], v[44:45], v[56:57], v[28:29] op_sel:[0,1,0] op_sel_hi:[1,1,1] neg_lo:[1,0,0] neg_hi:[1,0,0]\n"
    "  v_pk_fma_f32 v[30:31], v[46:47], v[56:57], v[30:31] op_sel:[0,1,0] op_sel_hi:[1,1,1] neg_lo:[1,0,0] neg_hi:[1,0,0]\n"
    "  v_pk_mul_f32 v[28:29], v[28:29], v[58:59] op_sel_hi:[1,0]\n"
    "  v_pk_mul_f32 v[30:31], v[30:31], v[58:59] op_sel_hi:[1,0]\n"
    "  v_exp_f32_e64 v60, v28 clamp\n"
    "  v_exp_f32_e64 v61, v29 clamp\n"
    "  v_exp_f32_e64 v62, v30 clamp\n"
    "  v_exp_f32_e64 v63, v31 clamp\n"
    "  v_max_f32 v28, s54, v28\n"
    "  v_max_f32 v29, s54, v29\n"
    "  v_max_f32 v30, s54, v30\n"
    "  v_max_f32 v31, s54, v31\n"
    "  v_pk_fma_f32 v[28:29], v[28:29], s[62:63], v[60:61]\n"
    "  v_pk_fma_f32 v[30:31], v[30:31], s[62:63], v[62:63]\n"
    "  v_cvt_pk_bf16_f32 v54, v28, v29\n"
    "  v_cvt_pk_bf16_f32 v55, v30, v31\n"
    "  s_and_b32 s67, s40, 1\n"
    "  s_lshl1_add_u32 s67, s41, s67\n"
    "  s_lshl_b32 s67, s67, 5\n"
    "  s_lshl2_add_u32 s67, s42, s67\n"
    "  s_and_b32 s66, s40, 2\n"
    "  s_add_u32 s67, s67, s66\n"
    "  s_lshl_b32 s67, s67, 10\n"
    "  s_add_u32 s52, s67, 0x400000\n"
    "  buffer_store_dwordx4 v[48:51], v2, s[16:19], s52 offen sc1\n"
    "  buffer_store_dwordx4 v[52:55], v2, s[16:19], s52 offen offset:1024 sc1\n"
    "  s_cmp_lg_u32 s40, 0\n"
    "  s_cbranch_scc1 Lnr_9%=\n"
    "  s_mov_b32 s55, 0x00008000\n"
    "  s_cmp_eq_u32 s38, 1\n"
    "  s_cbranch_scc1 Lre_12%=\n"
    "  s_waitcnt vmcnt(2)\n"
    "Lrc_10%=:\n"
    "  v_max3_u32 v115, v88, v92, v96\n"
    "  v_max3_u32 v115, v115, v100, v104\n"
    "  v_max3_u32 v115, v115, v105, v106\n"
    "  v_max_u32 v115, v115, v107\n"
    "  v_cmp_eq_u32 vcc, -1, v115\n"
    "  s_cbranch_vccz Lro_11%=\n"
    "  s_sub_u32 s55, s55, 1\n"
    "  s_cbranch_scc1 Lro_11%=\n"
    "  buffer_load_dwordx4 v[88:91], v2, s[16:19], s32 offen sc1\n"
    "  buffer_load_dwordx4 v[92:95], v2, s[16:19], s32 offen offset:1024 sc1\n"
    "  buffer_load_dwordx4 v[96:99], v2, s[16:19], s32 offen offset:2048 sc1\n"
    "  buffer_load_dwordx4 v[100:103], v2, s[16:19], s32 offen offset:3072 sc1\n"
    "  buffer_load_dwordx4 v[104:107], v112, s[16:19], s33 offen sc1\n"
    "  s_waitcnt vmcnt(0)\n"
    "  s_branch Lrc_10%=\n"
    "Lro_11%=:\n"
    "  s_sub_u32 s55, s55, 1\n"
    "  v_pk_add_f32 v[88:89], v[88:89], v[92:93]\n"
    "  v_pk_add_f32 v[96:97], v[96:97], v[100:101]\n"
    "  v_pk_add_f32 v[90:91], v[90:91], v[94:95]\n"
    "  v_pk_add_f32 v[98:99], v[98:99], v[102:103]\n"
    "  v_pk_add_f32 v[88:89], v[88:89], v[96:97]\n"
    "  v_pk_add_f32 v[90:91], v[90:91], v[98:99]\n"
    "  s_nop 0\n"
    "  v_add_f32_dpp v88, v88, v88 row_ror:8 row_mask:0xf bank_mask:0xf bound_ctrl:1\n"
    "  v_add_f32_dpp v89, v89, v89 row_ror:8 row_mask:0xf bank_mask:0xf bound_ctrl:1\n"
    "  v_add_f32_dpp v90, v90, v90 row_ror:8 row_mask:0xf bank_mask:0xf bound_ctrl:1\n"
    "  v_add_f32_dpp v91, v91, v91 row_ror:8 row_mask:0xf bank_mask:0xf bound_ctrl:1\n"
    "  v_add_f32_dpp v104, v104, v104 quad_perm:[2,3,0,1] row_mask:0xf bank_mask:0xf bound_ctrl:1\n"
    "  v_add_f32_dpp v105, v105, v105 quad_perm:[2,3,0,1] row_mask:0xf bank_mask:0xf bound_ctrl:1\n"
    "  v_add_f32_dpp v106, v106, v106 quad_perm:[2,3,0,1] row_mask:0xf bank_mask:0xf bound_ctrl:1\n"
    "  v_add_f32_dpp v107, v107, v107 quad_perm:[2,3,0,1] row_mask:0xf bank_mask:0xf bound_ctrl:1\n"
    "  v_mov_b32 v92, v88\n"
    "  v_mov_b32 v93, v89\n"
    "  v_mov_b32 v94, v90\n"
    "  v_mov_b32 v95, v91\n"
    "  v_permlane16_swap_b32 v88, v92\n"
    "  v_permlane16_swap_b32 v89, v93\n"
    "  v_permlane16_swap_b32 v90, v94\n"
    "  v_permlane16_swap_b32 v91, v95\n"
    "  v_add_f32 v88, v88, v92\n"
    "  v_add_f32 v89, v89, v93\n"
    "  v_add_f32 v90, v90, v94\n"
    "  v_add_f32 v91, v91, v95\n"
    "  v_mov_b32 v92, v88\n"
    "  v_mov_b32 v93, v89\n"
    "  v_mov_b32 v94, v90\n"
    "  v_mov_b32 v95, v91\n"
    "  v_permlane32_swap_b32 v88, v92\n"
    "  v_permlane32_swap_b32 v89, v93\n"
    "  v_permlane32_swap_b32 v90, v94\n"
    "  v_permlane32_swap_b32 v91, v95\n"
    "  v_add_f32 v88, v88, v92\n"
    "  v_add_f32 v89, v89, v93\n"
    "  v_add_f32 v90, v90, v94\n"
    "  v_add_f32 v91, v91, v95\n"
    "  v_cvt_pk_bf16_f32 v100, v88, v89\n"
    "  v_cvt_pk_bf16_f32 v101, v90, v91\n"
    "  buffer_store_dwordx2 v[100:101], v113, s[16:19], s39 offen sc1\n"
    "  s_and_b32 s53, s2, 3\n"
    "  s_cmp_lg_u32 s53, 0\n"
    "  s_cbranch_scc1 Lrz_13%=\n"
    "  v_add_f32_dpp v104, v104, v104 row_ror:4 row_mask:0xf bank_mask:0xf bound_ctrl:1\n"
    "  v_add_f32_dpp v105, v105, v105 row_ror:4 row_mask:0xf bank_mask:0xf bound_ctrl:1\n"
    "  v_add_f32_dpp v106, v106, v106 row_ror:4 row_mask:0xf bank_mask:0xf bound_ctrl:1\n"
    "  v_add_f32_dpp v107, v107, v107 row_ror:4 row_mask:0xf bank_mask:0xf bound_ctrl:1\n"
    "  v_add_f32_dpp v104, v104, v104 row_ror:8 row_mask:0xf bank_mask:0xf bound_ctrl:1\n"
    "  v_add_f32_dpp v105, v105, v105 row_ror:8 row_mask:0xf bank_mask:0xf bound_ctrl:1\n"
    "  v_add_f32_dpp v106, v106, v106 row_ror:8 row_mask:0xf bank_mask:0xf bound_ctrl:1\n"
    "  v_add_f32_dpp v107, v107, v107 row_ror:8 row_mask:0xf bank_mask:0xf bound_ctrl:1\n"
    "  v_mov_b32 v92, v104\n"
    "  v_mov_b32 v93, v105\n"
    "  v_mov_b32 v94, v106\n"
    "  v_mov_b32 v95, v107\n"
    "  v_permlane16_swap_b32 v104, v92\n"
    "  v_permlane16_swap_b32 v105, v93\n"
    "  v_permlane16_swap_b32 v106, v94\n"
    "  v_permlane16_swap_b32 v107, v95\n"
    "  v_add_f32 v104, v104, v92\n"
    "  v_add_f32 v105, v105, v93\n"
    "  v_add_f32 v106, v106, v94\n"
    "  v_add_f32 v107, v107, v95\n"
    "  v_mov_b32 v92, v104\n"
    "  v_mov_b32 v93, v105\n"
    "  v_mov_b32 v94, v106\n"
    "  v_mov_b32 v95, v107\n"
    "  v_permlane32_swap_b32 v104, v92\n"
    "  v_permlane32_swap_b32 v105, v93\n"
    "  v_permlane32_swap_b32 v106, v94\n"
    "  v_permlane32_swap_b32 v107, v95\n"
    "  v_add_f32 v104, v104, v92\n"
    "  v_add_f32 v105, v105, v93\n"
    "  v_add_f32 v106, v106, v94\n"
    "  v_add_f32 v107, v107, v95\n"
    "  v_cvt_pk_bf16_f32 v102, v104, v105\n"
    "  v_cvt_pk_bf16_f32 v103, v106, v107\n"
    "  s_mov_b32 exec_lo, 0x03030303\n"
    "  s_mov_b32 exec_hi, 0x03030303\n"
    "  buffer_store_dwordx2 v[102:103], v113, s[16:19], s39 offen offset:64 sc1\n"
    "  s_mov_b64 exec, -1\n"
    "Lrz_13%=:\n"
    "Lre_12%=:\n"
    "Lnr_9%=:\n"
    "  s_and_b32 s67, s40, 1\n"
    "  s_lshl_b32 s67, s67, 8\n"
    "  s_lshr_b32 s66, s40, 1\n"
    "  s_mul_i32 s66, s66, 0x6d50\n"
    "  s_add_u32 s66, s66, 0x18200\n"
    "  s_add_u32 s66, s67, s66\n"
    "  v_lshl_add_u32 v116, v1, 2, s66\n"
    "  v_lshlrev_b32 v108, 16, v231\n"
    "  s_mov_b32 s50, 0x3fb8aa3b\n"
    "  v_mul_f32 v109, s50, v108\n"
    "  v_fma_f32 v110, v108, s50, -v109\n"
    "  v_fmac_f32 v110, 0x32a57060, v108\n"
    "  s_add_u32 s28, s30, 0\n"
    "  s_and_b32 s28, s28, 31\n"
    "  s_lshl_b32 s28, s28, 10\n"
    "  s_add_u32 s28, s28, s29\n"
    "  v_exp_f32 v111, v109\n"
    "  v_mul_f32 v110, 0x3f317218, v110\n"
    "  v_lshlrev_b32 v3, 16, v232\n"
    "  v_fma_f32 v111, v111, v110, v111\n"
    "  v_lshlrev_b32 v112, 16, v225\n"
    "  v_cvt_pk_bf16_f32 v111, v111, v111\n"
    "  buffer_load_dwordx4 v[128:131], v2, s[16:19], s28 offen sc1\n"
    "  v_lshlrev_b32 v113, 16, v226\n"
    "  v_and_b32 v111, 0xffff0000, v111\n"
    "  v_lshlrev_b32 v104, 16, v227\n"
    "  v_lshlrev_b32 v105, 16, v228\n"
    "  v_lshlrev_b32 v106, 16, v229\n"
    "  v_lshlrev_b32 v107, 16, v230\n"
    "  buffer_load_dwordx4 v[132:135], v2, s[16:19], s28 offen offset:1024 sc1\n"
    "  v_mul_f32 v112, v111, v112\n"
    "  v_mul_f32 v113, v111, v113\n"
    "  v_mul_f32 v104, v111, v104\n"
    "  v_mul_f32 v105, v111, v105\n"
    "  v_mul_f32 v112, 0x3fb8aa3b, v112\n"
    "  v_mul_f32 v113, 0x3e22f983, v113\n"
    "  buffer_load_dwordx4 v[136:139], v2, s[16:19], s28 offen offset:2048 sc1\n"
    "  v_pk_mul_f32 v[96:97], v[106:107], v[104:105] op_sel:[1,1] op_sel_hi:[1,0] neg_lo:[1,0]\n"
    "  v_mul_f32 v108, 0x41000000, v112\n"
    "  v_mul_f32 v109, 0x41000000, v113\n"
    "  v_pk_fma_f32 v[80:81], v[106:107], v[104:105], v[96:97] op_sel:[0,0,0] op_sel_hi:[0,1,1]\n"
    "  v_exp_f32 v108, v108\n"
    "  v_exp_f32 v114, v112\n"
    "  buffer_load_dwordx4 v[140:143], v2, s[16:19], s28 offen offset:3072 sc1\n"
    "  v_fract_f32 v109, v109\n"
    "  v_fract_f32 v115, v113\n"
    "  v_cos_f32 v110, v109\n"
    "  v_cos_f32 v122, v115\n"
    "  v_sin_f32 v111, v109\n"
    "  v_mul_f32 v126, v108, v110\n"
    "  s_add_u32 s28, s30, 4\n"
    "  s_and_b32 s28, s28, 31\n"
    "  s_lshl_b32 s28, s28, 10\n"
    "  s_add_u32 s28, s28, s29\n"
    "  v_sin_f32 v123, v115\n"
    "  v_mul_f32 v127, v108, v111\n"
    "  v_mul_f32 v82, v114, v122\n"
    "  v_pk_mul_f32 v[124:125], v[126:127], v[126:127] op_sel:[1,1] op_sel_hi:[1,0] neg_lo:[1,0]\n"
    "  v_mul_f32 v83, v114, v123\n"
    "  v_pk_fma_f32 v[102:103], v[126:127], v[126:127], v[124:125] op_sel:[0,0,0] op_sel_hi:[0,1,1]\n"
    "  buffer_load_dwordx4 v[144:147], v2, s[16:19], s28 offen sc1\n"
    "  v_mul_f32 v114, 0x43800000, v112\n"
    "  v_cvt_pk_bf16_f32 v120, v126, v127\n"
    "  v_mul_f32 v115, 0x43800000, v113\n"
    "  ds_write_b32 v116, v120 offset:11616\n"
    "  v_cvt_pk_bf16_f32 v119, v102, v103\n"
    "  v_exp_f32 v114, v114\n"
    "  buffer_load_dwordx4 v[148:151], v2, s[16:19], s28 offen offset:1024 sc1\n"
    "  ds_write_b32 v116, v119 offset:12144\n"
    "  v_fract_f32 v115, v115\n"
    "  v_pk_mul_f32 v[124:125], v[102:103], v[102:103] op_sel:[1,1] op_sel_hi:[1,0] neg_lo:[1,0]\n"
    "  v_cos_f32 v122, v115\n"
    "  v_pk_fma_f32 v[98:99], v[102:103], v[102:103], v[124:125] op_sel:[0,0,0] op_sel_hi:[0,1,1]\n"
    "  v_sin_f32 v123, v115\n"
    "  buffer_load_dwordx4 v[152:155], v2, s[16:19], s28 offen offset:2048 sc1\n"
    "  v_pk_mul_f32 v[124:125], v[126:127], v[102:103] op_sel:[1,1] op_sel_hi:[1,0] neg_lo:[1,0]\n"
    "  v_pk_fma_f32 v[100:101], v[126:127], v[102:103], v[124:125] op_sel:[0,0,0] op_sel_hi:[0,1,1]\n"
    "  v_mul_f32 v88, v114, v122\n"
    "  v_mov_b32 v120, 0x3f80\n"
    "  v_mul_f32 v89, v114, v123\n"
    "  ds_write_b32 v116, v120 offset:11088\n"
    "  buffer_load_dwordx4 v[156:159], v2, s[16:19], s28 offen offset:3072 sc1\n"
    "  v_pk_mul_f32 v[96:97], v[82:83], v[82:83] op_sel:[1,1] op_sel_hi:[1,0] neg_lo:[1,0]\n"
    "  v_cvt_pk_bf16_f32 v119, v100, v101\n"
    "  v_pk_fma_f32 v[84:85], v[82:83], v[82:83], v[96:97] op_sel:[0,0,0] op_sel_hi:[0,1,1]\n"
    "  ds_write_b32 v116, v119 offset:12672\n"
    "  v_pk_mul_f32 v[96:97], v[84:85], v[84:85] op_sel:[1,1] op_sel_hi:[1,0] neg_lo:[1,0]\n"
    "  v_pk_mul_f32 v[124:125], v[100:101], v[102:103] op_sel:[1,1] op_sel_hi:[1,0] neg_lo:[1,0]\n"
    "  s_add_u32 s28, s30, 8\n"
    "  s_and_b32 s28, s28, 31\n"
    "  s_lshl_b32 s28, s28, 10\n"
    "  s_add_u32 s28, s28, s29\n"
    "  v_pk_fma_f32 v[100:101], v[100:101], v[102:103], v[124:125] op_sel:[0,0,0] op_sel_hi:[0,1,1]\n"
    "  v_pk_fma_f32 v[86:87], v[84:85], v[84:85], v[96:97] op_sel:[0,0,0] op_sel_hi:[0,1,1]\n"
    "  v_cvt_pk_bf16_f32 v120, v98, v99\n"
    "  v_cvt_pk_bf16_f32 v117, v80, -v81\n"
    "  ds_write_b32 v116, v120 offset:13200\n"
    "  ds_write_b32 v116, v117\n"
    "  buffer_load_dwordx4 v[160:163], v2, s[16:19], s28 offen sc1\n"
    "  v_pk_mul_f32 v[124:125], v[98:99], v[102:103] op_sel:[1,1] op_sel_hi:[1,0] neg_lo:[1,0]\n"
    "  v_pk_mul_f32 v[96:97], v[80:81], v[82:83] op_sel:[1,1] op_sel_hi:[1,0] neg_lo:[1,0]\n"
    "  v_pk_fma_f32 v[98:99], v[98:99], v[102:103], v[124:125] op_sel:[0,0,0] op_sel_hi:[0,1,1]\n"
    "  v_cvt_pk_bf16_f32 v119, v100, v101\n"
    "  v_pk_fma_f32 v[94:95], v[80:81], v[82:83], v[96:97] op_sel:[0,0,0] op_sel_hi:[0,1,1]\n"
    "  ds_write_b32 v116, v119 offset:13728\n"
    "  buffer_load_dwordx4 v[164:167], v2, s[16:19], s28 offen offset:1024 sc1\n"
    "  v_cvt_pk_bf16_f32 v118, v94, -v95\n"
    "  v_pk_mul_f32 v[124:125], v[100:101], v[102:103] op_sel:[1,1] op_sel_hi:[1,0] neg_lo:[1,0]\n"
    "  s_and_b32 s67, s2, 7\n"
    "  s_lshl_b32 s67, s67, 12\n"
    "  s_add_u32 s48, s67, 0x9000\n"
    "  s_cmp_eq_u32 s40, 0\n"
    "  s_cselect_b64 exec, -1, 0\n"
    "  buffer_load_dwordx4 v[16:19], v2, s[16:19], s48 offen sc1\n"
    "  s_mov_b64 exec, -1\n"
    "  ds_write_b32 v116, v118 offset:528\n"
    "  v_pk_fma_f32 v[100:101], v[100:101], v[102:103], v[124:125] op_sel:[0,0,0] op_sel_hi:[0,1,1]\n"
    "  v_pk_mul_f32 v[96:97], v[94:95], v[82:83] op_sel:[1,1] op_sel_hi:[1,0] neg_lo:[1,0]\n"
    "  v_cvt_pk_bf16_f32 v120, v98, v99\n"
    "  buffer_load_dwordx4 v[168:171], v2, s[16:19], s28 offen offset:2048 sc1\n"
    "  v_pk_fma_f32 v[94:95], v[94:95], v[82:83], v[96:97] op_sel:[0,0,0] op_sel_hi:[0,1,1]\n"
    "  ds_write_b32 v116, v120 offset:14256\n"
    "  v_pk_mul_f32 v[124:125], v[98:99], v[102:103] op_sel:[1,1] op_sel_hi:[1,0] neg_lo:[1,0]\n"
    "  v_cvt_pk_bf16_f32 v117, v94, -v95\n"
    "  v_pk_fma_f32 v[98:99], v[98:99], v[102:103], v[124:125] op_sel:[0,0,0] op_sel_hi:[0,1,1]\n"
    "  ds_write_b32 v116, v117 offset:1056\n"
    "  buffer_load_dwordx4 v[172:175], v2, s[16:19], s28 offen offset:3072 sc1\n"
    "  v_cvt_pk_bf16_f32 v119, v100, v101\n"
    "  v_pk_mul_f32 v[96:97], v[94:95], v[82:83] op_sel:[1,1] op_sel_hi:[1,0] neg_lo:[1,0]\n"
    "  ds_write_b32 v116, v119 offset:14784\n"
    "  v_pk_fma_f32 v[94:95], v[94:95], v[82:83], v[96:97] op_sel:[0,0,0] op_sel_hi:[0,1,1]\n"
    "  v_pk_mul_f32 v[124:125], v[100:101], v[102:103] op_sel:[1,1] op_sel_hi:[1,0] neg_lo:[1,0]\n"
    "  v_pk_fma_f32 v[100:101], v[100:101], v[102:103], v[124:125] op_sel:[0,0,0] op_sel_hi:[0,1,1]\n"
    "  s_add_u32 s28, s30, 12\n"
    "  s_and_b32 s28, s28, 31\n"
    "  s_lshl_b32 s28, s28, 10\n"
    "  s_add_u32 s28, s28, s29\n"
    "  v_cvt_pk_bf16_f32 v118, v94, -v95\n"
    "  v_cvt_pk_bf16_f32 v120, v98, v99\n"
    "  ds_write_b32 v116, v118 offset:1584\n"
    "  ds_write_b32 v116, v120 offset:15312\n"
    "  v_pk_mul_f32 v[96:97], v[94:95], v[82:83] op_sel:[1,1] op_sel_hi:[1,0] neg_lo:[1,0]\n"
    "  v_pk_mul_f32 v[124:125], v[98:99], v[102:103] op_sel:[1,1] op_sel_hi:[1,0] neg_lo:[1,0]\n"
    "  buffer_load_dwordx4 v[176:179], v2, s[16:19], s28 offen sc1\n"
    "  v_pk_fma_f32 v[94:95], v[94:95], v[82:83], v[96:97] op_sel:[0,0,0] op_sel_hi:[0,1,1]\n"
    "  v_pk_fma_f32 v[98:99], v[98:99], v[102:103], v[124:125] op_sel:[0,0,0] op_sel_hi:[0,1,1]\n"
    "  v_cvt_pk_bf16_f32 v117, v94, -v95\n"
    "  v_cvt_pk_bf16_f32 v119, v100, v101\n"
    "  ds_write_b32 v116, v119 offset:15840\n"
    "  ds_write_b32 v116, v117 offset:2112\n"
    "  buffer_load_dwordx4 v[180:183], v2, s[16:19], s28 offen offset:1024 sc1\n"
    "  v_pk_mul_f32 v[124:125], v[100:101], v[102:103] op_sel:[1,1] op_sel_hi:[1,0] neg_lo:[1,0]\n"
    "  v_pk_mul_f32 v[96:97], v[94:95], v[82:83] op_sel:[1,1] op_sel_hi:[1,0] neg_lo:[1,0]\n"
    "  v_pk_fma_f32 v[100:101], v[100:101], v[102:103], v[124:125] op_sel:[0,0,0] op_sel_hi:[0,1,1]\n"
    "  v_pk_fma_f32 v[94:95], v[94:95], v[82:83], v[96:97] op_sel:[0,0,0] op_sel_hi:[0,1,1]\n"
    "  v_cvt_pk_bf16_f32 v120, v98, v99\n"
    "  v_cvt_pk_bf16_f32 v118, v94, -v95\n"
    "  buffer_load_dwordx4 v[184:187], v2, s[16:19], s28 offen offset:2048 sc1\n"
    "  ds_write_b32 v116, v120 offset:16368\n"
    "  v_pk_mul_f32 v[124:125], v[98:99], v[102:103] op_sel:[1,1] op_sel_hi:[1,0] neg_lo:[1,0]\n"
    "  ds_write_b32 v116, v118 offset:2640\n"
    "  v_pk_fma_f32 v[98:99], v[98:99], v[102:103], v[124:125] op_sel:[0,0,0] op_sel_hi:[0,1,1]\n"
    "  v_pk_mul_f32 v[96:97], v[94:95], v[82:83] op_sel:[1,1] op_sel_hi:[1,0] neg_lo:[1,0]\n"
    "  v_cvt_pk_bf16_f32 v119, v100, v101\n"
    "  buffer_load_dwordx4 v[188:191], v2, s[16:19], s28 offen offset:3072 sc1\n"
    "  v_pk_fma_f32 v[94:95], v[94:95], v[82:83], v[96:97] op_sel:[0,0,0] op_sel_hi:[0,1,1]\n"
    "  ds_write_b32 v116, v119 offset:16896\n"
    "  v_cvt_pk_bf16_f32 v117, v94, -v95\n"
    "  v_pk_mul_f32 v[124:125], v[100:101], v[102:103] op_sel:[1,1] op_sel_hi:[1,0] neg_lo:[1,0]\n"
    "  ds_write_b32 v116, v117 offset:3168\n"
    "  v_pk_fma_f32 v[100:101], v[100:101], v[102:103], v[124:125] op_sel:[0,0,0] op_sel_hi:[0,1,1]\n"
    "  v_cvt_pk_bf16_f32 v120, v98, v99\n"
    "  v_pk_mul_f32 v[96:97], v[94:95], v[82:83] op_sel:[1,1] op_sel_hi:[1,0] neg_lo:[1,0]\n"
    "  ds_write_b32 v116, v120 offset:17424\n"
    "  v_pk_fma_f32 v[94:95], v[94:95], v[82:83], v[96:97] op_sel:[0,0,0] op_sel_hi:[0,1,1]\n"
    "  v_pk_mul_f32 v[124:125], v[98:99], v[102:103] op_sel:[1,1] op_sel_hi:[1,0] neg_lo:[1,0]\n"
    "  v_cvt_pk_bf16_f32 v118, v94, -v95\n"
    "  v_pk_fma_f32 v[98:99], v[98:99], v[102:103], v[124:125] op_sel:[0,0,0] op_sel_hi:[0,1,1]\n"
    "  ds_write_b32 v116, v118 offset:3696\n"
    "  v_cvt_pk_bf16_f32 v119, v100, v101\n"
    "  ds_write_b32 v116, v119 offset:17952\n"
    "  v_pk_mul_f32 v[96:97], v[80:81], v[88:89] op_sel:[1,1] op_sel_hi:[1,0] neg_lo:[1,0]\n"
    "  v_pk_mul_f32 v[124:125], v[100:101], v[102:103] op_sel:[1,1] op_sel_hi:[1,0] neg_lo:[1,0]\n"
    "  v_pk_fma_f32 v[94:95], v[80:81], v[88:89], v[96:97] op_sel:[0,0,0] op_sel_hi:[0,1,1]\n"
    "  v_pk_fma_f32 v[100:101], v[100:101], v[102:103], v[124:125] op_sel:[0,0,0] op_sel_hi:[0,1,1]\n"
    "  v_cvt_pk_bf16_f32 v117, v94, -v95\n"
    "  v_cvt_pk_bf16_f32 v120, v98, v99\n"
    "  ds_write_b32 v116, v117 offset:4224\n"
    "  ds_write_b32 v116, v120 offset:18480\n"
    "  v_pk_mul_f32 v[96:97], v[94:95], v[84:85] op_sel:[1,1] op_sel_hi:[1,0] neg_lo:[1,0]\n"
    "  v_pk_mul_f32 v[124:125], v[98:99], v[102:103] op_sel:[1,1] op_sel_hi:[1,0] neg_lo:[1,0]\n"
    "  s_cmp_lg_u32 s40, 0\n"
    "  s_cbranch_scc1 Lnr_14%=\n"
    "  s_waitcnt vmcnt(6)\n"
    "  s_mov_b32 s55, 0x00008000\n"
    "Lc_15%=:\n"
    "  v_xor_b32 v16, s46, v16\n"
    "  v_xor_b32 v17, s46, v17\n"
    "  v_xor_b32 v18, s46, v18\n"
    "  v_xor_b32 v19, s46, v19\n"
    "  v_or3_b32 v20, v16, v17, v18\n"
    "  v_or_b32 v20, v20, v19\n"
    "  v_cmp_ne_u32 vcc, 0, v20\n"
    "  s_cbranch_vccz Lo_16%=\n"
    "  s_sub_u32 s55, s55, 1\n"
    "  s_cbranch_scc1 Lo_16%=\n"
    "  s_sleep 2\n"
    "  buffer_load_dwordx4 v[16:19], v2, s[16:19], s48 offen sc1\n"
    "  s_waitcnt vmcnt(0)\n"
    "  s_branch Lc_15%=\n"
    "Lo_16%=:\n"
    "Lnr_14%=:\n"
    "  s_barrier\n"
    "  s_lshl_b32 s67, s43, 5\n"
    "  s_lshl3_add_u32 s67, s40, s67\n"
    "  s_lshl_b32 s67, s67, 10\n"
    "  s_add_u32 s24, s67, 0x400000\n"
    "  s_and_b32 s67, s2, 7\n"
    "  s_lshl_b32 s67, s67, 15\n"
    "  s_lshl_b32 s66, s40, 13\n"
    "  s_add_u32 s67, s67, s66\n"
    "  s_add_u32 s26, s67, 0x180000\n"
    "  v_lshrrev_b32 v12, 5, v1\n"
    "  v_and_b32 v13, 7, v1\n"
    "  v_lshlrev_b32 v13, 2, v13\n"
    "  v_bfe_u32 v14, v1, 3, 2\n"
    "  v_add_lshl_u32 v14, v13, v14, 4\n"
    "  v_lshl_add_u32 v15, v12, 9, v14\n"
    "  v_and_b32 v14, 15, v1\n"
    "  v_and_b32 v13, 3, v14\n"
    "  v_lshrrev_b32 v14, 2, v14\n"
    "  v_lshlrev_b32 v14, 7, v14\n"
    "  v_lshl_add_u32 v14, v12, 9, v14\n"
    "  v_lshl_add_u32 v10, v13, 4, v14\n"
    "  v_lshl_add_u32 v11, v12, 9, 64\n"
    "  s_add_u32 s25, s24, 4096\n"
    "  s_add_u32 s27, s26, 4096\n"
    "  s_mov_b32 s31, 0xffff0000\n"
    "  v_mov_b32 v13, s31\n"
    "  v_bfe_u32 v13, v13, v1, 1\n"
    "  v_cmp_ne_u32 vcc, 0, v13\n"
    "  s_nop 1\n"
    "  v_cndmask_b32 v12, v10, v11\n"
    "  s_waitcnt vmcnt(6)\n"
    "  buffer_load_dwordx4 v[16:19], v15, s[16:19], s24 offen sc1\n"
    "  buffer_load_dwordx4 v[20:23], v15, s[16:19], s24 offen offset:1024 sc1\n"
    "  buffer_load_dwordx4 v[24:27], v15, s[16:19], s24 offen offset:2048 sc1\n"
    "  buffer_load_dwordx4 v[28:31], v15, s[16:19], s24 offen offset:3072 sc1\n"
    "  buffer_load_dwordx4 v[32:35], v15, s[16:19], s25 offen sc1\n"
    "  buffer_load_dwordx4 v[36:39], v15, s[16:19], s25 offen offset:1024 sc1\n"
    "  buffer_load_dwordx4 v[40:43], v15, s[16:19], s25 offen offset:2048 sc1\n"
    "  buffer_load_dwordx4 v[44:47], v15, s[16:19], s25 offen offset:3072 sc1\n"
    "  v_pk_fma_f32 v[98:99], v[98:99], v[102:103], v[124:125] op_sel:[0,0,0] op_sel_hi:[0,1,1]\n"
    "  v_pk_fma_f32 v[94:95], v[94:95], v[84:85], v[96:97] op_sel:[0,0,0] op_sel_hi:[0,1,1]\n"
    "  v_cvt_pk_bf16_f32 v119, v100, v101\n"
    "  v_cvt_pk_bf16_f32 v118, v94, -v95\n"
    "  ds_write_b32 v116, v119 offset:19008\n"
    "  ds_write_b32 v116, v118 offset:4752\n"
    "  v_pk_mul_f32 v[124:125], v[100:101], v[102:103] op_sel:[1,1] op_sel_hi:[1,0] neg_lo:[1,0]\n"
    "  v_pk_mul_f32 v[96:97], v[94:95], v[84:85] op_sel:[1,1] op_sel_hi:[1,0] neg_lo:[1,0]\n"
    "  v_pk_fma_f32 v[100:101], v[100:101], v[102:103], v[124:125] op_sel:[0,0,0] op_sel_hi:[0,1,1]\n"
    "  v_cvt_pk_bf16_f32 v120, v98, v99\n"
    "  v_pk_fma_f32 v[94:95], v[94:95], v[84:85], v[96:97] op_sel:[0,0,0] op_sel_hi:[0,1,1]\n"
    "  ds_write_b32 v116, v120 offset:19536\n"
    "  v_cvt_pk_bf16_f32 v117, v94, -v95\n"
    "  v_pk_mul_f32 v[124:125], v[98:99], v[102:103] op_sel:[1,1] op_sel_hi:[1,0] neg_lo:[1,0]\n"
    "  ds_write_b32 v116, v117 offset:5280\n"
    "  v_pk_fma_f32 v[98:99], v[98:99], v[102:103], v[124:125] op_sel:[0,0,0] op_sel_hi:[0,1,1]\n"
    "  v_pk_mul_f32 v[96:97], v[94:95], v[84:85] op_sel:[1,1] op_sel_hi:[1,0] neg_lo:[1,0]\n"
    "  v_cvt_pk_bf16_f32 v119, v100, v101\n"
    "  v_pk_fma_f32 v[94:95], v[94:95], v[84:85], v[96:97] op_sel:[0,0,0] op_sel_hi:[0,1,1]\n"
    "  ds_write_b32 v116, v119 offset:20064\n"
    "  v_pk_mul_f32 v[124:125], v[100:101], v[102:103] op_sel:[1,1] op_sel_hi:[1,0] neg_lo:[1,0]\n"
    "  v_cvt_pk_bf16_f32 v118, v94, -v95\n"
    "  v_pk_fma_f32 v[100:101], v[100:101], v[102:103], v[124:125] op_sel:[0,0,0] op_sel_hi:[0,1,1]\n"
    "  ds_write_b32 v116, v118 offset:5808\n"
    "  v_cvt_pk_bf16_f32 v120, v98, v99\n"
    "  v_pk_mul_f32 v[96:97], v[88:89], v[88:89] op_sel:[1,1] op_sel_hi:[1,0] neg_lo:[1,0]\n"
    "  ds_write_b32 v116, v120 offset:20592\n"
    "  v_pk_fma_f32 v[90:91], v[88:89], v[88:89], v[96:97] op_sel:[0,0,0] op_sel_hi:[0,1,1]\n"
    "  v_pk_mul_f32 v[124:125], v[98:99], v[102:103] op_sel:[1,1] op_sel_hi:[1,0] neg_lo:[1,0]\n"
    "  v_pk_fma_f32 v[98:99], v[98:99], v[102:103], v[124:125] op_sel:[0,0,0] op_sel_hi:[0,1,1]\n"
    "  v_pk_mul_f32 v[96:97], v[90:91], v[88:89] op_sel:[1,1] op_sel_hi:[1,0] neg_lo:[1,0]\n"
    "  v_cvt_pk_bf16_f32 v119, v100, v101\n"
    "  v_pk_fma_f32 v[92:93], v[90:91], v[88:89], v[96:97] op_sel:[0,0,0] op_sel_hi:[0,1,1]\n"
    "  ds_write_b32 v116, v119 offset:21120\n"
    "  v_pk_mul_f32 v[96:97], v[80:81], v[90:91] op_sel:[1,1] op_sel_hi:[1,0] neg_lo:[1,0]\n"
    "  v_pk_mul_f32 v[124:125], v[100:101], v[102:103] op_sel:[1,1] op_sel_hi:[1,0] neg_lo:[1,0]\n"
    "  v_pk_fma_f32 v[94:95], v[80:81], v[90:91], v[96:97] op_sel:[0,0,0] op_sel_hi:[0,1,1]\n"
    "  v_pk_fma_f32 v[100:101], v[100:101], v[102:103], v[124:125] op_sel:[0,0,0] op_sel_hi:[0,1,1]\n"
    "  v_cvt_pk_bf16_f32 v117, v94, -v95\n"
    "  v_cvt_pk_bf16_f32 v120, v98, v99\n"
    "  ds_write_b32 v116, v120 offset:21648\n"
    "  ds_write_b32 v116, v117 offset:6336\n"
    "  v_pk_mul_f32 v[124:125], v[98:99], v[102:103] op_sel:[1,1] op_sel_hi:[1,0] neg_lo:[1,0]\n"
    "  v_pk_mul_f32 v[96:97], v[94:95], v[86:87] op_sel:[1,1] op_sel_hi:[1,0] neg_lo:[1,0]\n"
    "  v_pk_fma_f32 v[98:99], v[98:99], v[102:103], v[124:125] op_sel:[0,0,0] op_sel_hi:[0,1,1]\n"
    "  v_pk_fma_f32 v[94:95], v[94:95], v[86:87], v[96:97] op_sel:[0,0,0] op_sel_hi:[0,1,1]\n"
    "  v_cvt_pk_bf16_f32 v119, v100, v101\n"
    "  v_cvt_pk_bf16_f32 v118, v94, -v95\n"
    "  ds_write_b32 v116, v119 offset:22176\n"
    "  v_pk_mul_f32 v[124:125], v[100:101], v[102:103] op_sel:[1,1] op_sel_hi:[1,0] neg_lo:[1,0]\n"
    "  ds_write_b32 v116, v118 offset:6864\n"
    "  v_pk_fma_f32 v[100:101], v[100:101], v[102:103], v[124:125] op_sel:[0,0,0] op_sel_hi:[0,1,1]\n"
    "  v_pk_mul_f32 v[96:97], v[80:81], v[92:93] op_sel:[1,1] op_sel_hi:[1,0] neg_lo:[1,0]\n"
    "  v_cvt_pk_bf16_f32 v120, v98, v99\n"
    "  v_pk_fma_f32 v[94:95], v[80:81], v[92:93], v[96:97] op_sel:[0,0,0] op_sel_hi:[0,1,1]\n"
    "  ds_write_b32 v116, v120 offset:22704\n"
    "  v_cvt_pk_bf16_f32 v117, v94, -v95\n"
    "  v_pk_mul_f32 v[124:125], v[98:99], v[102:103] op_sel:[1,1] op_sel_hi:[1,0] neg_lo:[1,0]\n"
    "  ds_write_b32 v116, v117 offset:7392\n"
    "  v_pk_fma_f32 v[98:99], v[98:99], v[102:103], v[124:125] op_sel:[0,0,0] op_sel_hi:[0,1,1]\n"
    "  v_cvt_pk_bf16_f32 v119, v100, v101\n"
    "  v_pk_mul_f32 v[96:97], v[94:95], v[86:87] op_sel:[1,1] op_sel_hi:[1,0] neg_lo:[1,0]\n"
    "  ds_write_b32 v116, v119 offset:23232\n"
    "  v_pk_fma_f32 v[94:95], v[94:95], v[86:87], v[96:97] op_sel:[0,0,0] op_sel_hi:[0,1,1]\n"
    "  v_pk_mul_f32 v[124:125], v[100:101], v[102:103] op_sel:[1,1] op_sel_hi:[1,0] neg_lo:[1,0]\n"
    "  v_cvt_pk_bf16_f32 v118, v94, -v95\n"
    "  v_pk_fma_f32 v[100:101], v[100:101], v[102:103], v[124:125] op_sel:[0,0,0] op_sel_hi:[0,1,1]\n"
    "  ds_write_b32 v116, v118 offset:7920\n"
    "  v_cvt_pk_bf16_f32 v120, v98, v99\n"
    "  ds_write_b32 v116, v120 offset:23760\n"
    "  v_pk_mul_f32 v[96:97], v[90:91], v[90:91] op_sel:[1,1] op_sel_hi:[1,0] neg_lo:[1,0]\n"
    "  v_pk_mul_f32 v[124:125], v[98:99], v[102:103] op_sel:[1,1] op_sel_hi:[1,0] neg_lo:[1,0]\n"
    "  v_pk_fma_f32 v[90:91], v[90:91], v[90:91], v[96:97] op_sel:[0,0,0] op_sel_hi:[0,1,1]\n"
    "  v_pk_fma_f32 v[98:99], v[98:99], v[102:103], v[124:125] op_sel:[0,0,0] op_sel_hi:[0,1,1]\n"
    "  v_pk_mul_f32 v[96:97], v[80:81], v[90:91] op_sel:[1,1] op_sel_hi:[1,0] neg_lo:[1,0]\n"
    "  v_cvt_pk_bf16_f32 v119, v100, v101\n"
    "  v_pk_fma_f32 v[94:95], v[80:81], v[90:91], v[96:97] op_sel:[0,0,0] op_sel_hi:[0,1,1]\n"
    "  ds_write_b32 v116, v119 offset:24288\n"
    "  v_cvt_pk_bf16_f32 v117, v94, -v95\n"
    "  v_pk_mul_f32 v[124:125], v[100:101], v[102:103] op_sel:[1,1] op_sel_hi:[1,0] neg_lo:[1,0]\n"
    "  v_pk_fma_f32 v[100:101], v[100:101], v[102:103], v[124:125] op_sel:[0,0,0] op_sel_hi:[0,1,1]\n"
    "  ds_write_b32 v116, v117 offset:8448\n"
    "  v_cvt_pk_bf16_f32 v120, v98, v99\n"
    "  v_pk_mul_f32 v[96:97], v[94:95], v[88:89] op_sel:[1,1] op_sel_hi:[1,0] neg_lo:[1,0]\n"
    "  ds_write_b32 v116, v120 offset:24816\n"
    "  v_pk_fma_f32 v[94:95], v[94:95], v[88:89], v[96:97] op_sel:[0,0,0] op_sel_hi:[0,1,1]\n"
    "  v_pk_mul_f32 v[124:125], v[98:99], v[102:103] op_sel:[1,1] op_sel_hi:[1,0] neg_lo:[1,0]\n"
    "  v_cvt_pk_bf16_f32 v118, v94, -v95\n"
    "  v_pk_fma_f32 v[98:99], v[98:99], v[102:103], v[124:125] op_sel:[0,0,0] op_sel_hi:[0,1,1]\n"
    "  v_cvt_pk_bf16_f32 v119, v100, v101\n"
    "  ds_write_b32 v116, v118 offset:8976\n"
    "  ds_write_b32 v116, v119 offset:25344\n"
    "  s_mov_b32 exec_lo, 0x0011ffff\n"
    "  s_mov_b32 exec_hi, 0x0011ffff\n"
    "  buffer_load_dwordx4 v[48:51], v12, s[16:19], s26 offen sc1\n"
    "  buffer_load_dwordx4 v[52:55], v12, s[16:19], s26 offen offset:1024 sc1\n"
    "  buffer_load_dwordx4 v[56:59], v12, s[16:19], s26 offen offset:2048 sc1\n"
    "  buffer_load_dwordx4 v[60:63], v12, s[16:19], s26 offen offset:3072 sc1\n"
    "  buffer_load_dwordx4 v[64:67], v12, s[16:19], s27 offen sc1\n"
    "  buffer_load_dwordx4 v[68:71], v12, s[16:19], s27 offen offset:1024 sc1\n"
    "  buffer_load_dwordx4 v[72:75], v12, s[16:19], s27 offen offset:2048 sc1\n"
    "  buffer_load_dwordx4 v[76:79], v12, s[16:19], s27 offen offset:3072 sc1\n"
    "  s_mov_b64 exec, -1\n"
    "  v_pk_mul_f32 v[96:97], v[94:95], v[88:89] op_sel:[1,1] op_sel_hi:[1,0] neg_lo:[1,0]\n"
    "  v_pk_mul_f32 v[124:125], v[100:101], v[102:103] op_sel:[1,1] op_sel_hi:[1,0] neg_lo:[1,0]\n"
    "  v_pk_fma_f32 v[94:95], v[94:95], v[88:89], v[96:97] op_sel:[0,0,0] op_sel_hi:[0,1,1]\n"
    "  v_pk_fma_f32 v[100:101], v[100:101], v[102:103], v[124:125] op_sel:[0,0,0] op_sel_hi:[0,1,1]\n"
    "  v_cvt_pk_bf16_f32 v117, v94, -v95\n"
    "  v_cvt_pk_bf16_f32 v120, v98, v99\n"
    "  ds_write_b32 v116, v117 offset:9504\n"
    "  ds_write_b32 v116, v120 offset:25872\n"
    "  v_pk_mul_f32 v[124:125], v[98:99], v[102:103] op_sel:[1,1] op_sel_hi:[1,0] neg_lo:[1,0]\n"
    "  v_pk_mul_f32 v[96:97], v[94:95], v[88:89] op_sel:[1,1] op_sel_hi:[1,0] neg_lo:[1,0]\n"
    "  v_pk_fma_f32 v[98:99], v[98:99], v[102:103], v[124:125] op_sel:[0,0,0] op_sel_hi:[0,1,1]\n"
    "  v_pk_fma_f32 v[94:95], v[94:95], v[88:89], v[96:97] op_sel:[0,0,0] op_sel_hi:[0,1,1]\n"
    "  v_cvt_pk_bf16_f32 v119, v100, v101\n"
    "  v_cvt_pk_bf16_f32 v118, v94, -v95\n"
    "  ds_write_b32 v116, v119 offset:26400\n"
    "  ds_write_b32 v116, v118 offset:10032\n"
    "  v_pk_mul_f32 v[124:125], v[100:101], v[102:103] op_sel:[1,1] op_sel_hi:[1,0] neg_lo:[1,0]\n"
    "  v_pk_fma_f32 v[100:101], v[100:101], v[102:103], v[124:125] op_sel:[0,0,0] op_sel_hi:[0,1,1]\n"
    "  v_pk_mul_f32 v[96:97], v[94:95], v[88:89] op_sel:[1,1] op_sel_hi:[1,0] neg_lo:[1,0]\n"
    "  v_cvt_pk_bf16_f32 v120, v98, v99\n"
    "  v_pk_fma_f32 v[94:95], v[94:95], v[88:89], v[96:97] op_sel:[0,0,0] op_sel_hi:[0,1,1]\n"
    "  ds_write_b32 v116, v120 offset:26928\n"
    "  v_cvt_pk_bf16_f32 v117, v94, -v95\n"
    "  v_cvt_pk_bf16_f32 v119, v100, v101\n"
    "  ds_write_b32 v116, v117 offset:10560\n"
    "  ds_write_b32 v116, v119 offset:27456\n"
    "  v_and_b32 v97, 7, v1\n"
    "  v_lshlrev_b32 v97, 2, v97\n"
    "  v_bfe_u32 v96, v1, 3, 2\n"
    "  s_and_b32 s67, s43, 1\n"
    "  s_lshl_b32 s67, s67, 5\n"
    "  v_add3_u32 v100, v97, v96, s67\n"
    "  v_lshrrev_b32 v97, 5, v1\n"
    "  s_and_b32 s67, s40, 1\n"
    "  s_lshl_b32 s67, s67, 2\n"
    "  v_bfe_u32 v96, v100, 1, 3\n"
    "  v_xor_b32 v96, s67, v96\n"
    "  s_lshr_b32 s67, s40, 1\n"
    "  s_lshl1_add_u32 s67, s44, s67\n"
    "  s_sub_u32 s67, s67, s45\n"
    "  s_and_b32 s67, s67, 7\n"
    "  s_lshl_b32 s67, s67, 13\n"
    "  v_lshl_add_u32 v13, v100, 7, s67\n"
    "  v_lshl_add_u32 v96, v96, 4, v13\n"
    "  v_lshl_add_u32 v98, v97, 3, v96\n"
    "  ds_read_b64 v[80:81], v98\n"
    "  v_lshrrev_b32 v97, 5, v1\n"
    "  s_and_b32 s67, s40, 1\n"
    "  s_lshl2_add_u32 s67, s67, 1\n"
    "  v_bfe_u32 v96, v100, 1, 3\n"
    "  v_xor_b32 v96, s67, v96\n"
    "  s_lshr_b32 s67, s40, 1\n"
    "  s_lshl1_add_u32 s67, s44, s67\n"
    "  s_sub_u32 s67, s67, s45\n"
    "  s_and_b32 s67, s67, 7\n"
    "  s_lshl_b32 s67, s67, 13\n"
    "  v_lshl_add_u32 v13, v100, 7, s67\n"
    "  v_lshl_add_u32 v96, v96, 4, v13\n"
    "  v_lshl_add_u32 v99, v97, 3, v96\n"
    "  ds_read_b64 v[82:83], v99\n"
    "  v_lshrrev_b32 v97, 5, v1\n"
    "  s_and_b32 s67, s40, 1\n"
    "  s_lshl2_add_u32 s67, s67, 2\n"
    "  v_bfe_u32 v96, v100, 1, 3\n"
    "  v_xor_b32 v96, s67, v96\n"
    "  s_lshr_b32 s67, s40, 1\n"
    "  s_lshl1_add_u32 s67, s44, s67\n"
    "  s_sub_u32 s67, s67, s45\n"
    "  s_and_b32 s67, s67, 7\n"
    "  s_lshl_b32 s67, s67, 13\n"
    "  v_lshl_add_u32 v13, v100, 7, s67\n"
    "  v_lshl_add_u32 v96, v96, 4, v13\n"
    "  v_lshl_add_u32 v98, v97, 3, v96\n"
    "  ds_read_b64 v[84:85], v98\n"
    "  v_lshrrev_b32 v97, 5, v1\n"
    "  s_and_b32 s67, s40, 1\n"
    "  s_lshl2_add_u32 s67, s67, 3\n"
    "  v_bfe_u32 v96, v100, 1, 3\n"
    "  v_xor_b32 v96, s67, v96\n"
    "  s_lshr_b32 s67, s40, 1\n"
    "  s_lshl1_add_u32 s67, s44, s67\n"
    "  s_sub_u32 s67, s67, s45\n"
    "  s_and_b32 s67, s67, 7\n"
    "  s_lshl_b32 s67, s67, 13\n"
    "  v_lshl_add_u32 v13, v100, 7, s67\n"
    "  v_lshl_add_u32 v13, v96, 4, v13\n"
    "  v_lshl_add_u32 v99, v97, 3, v13\n"
    "  ds_read_b64 v[86:87], v99\n"
    "  s_waitcnt lgkmcnt(3)\n"
    "  v_lshlrev_b32 v112, 16, v80\n"
    "  v_and_b32 v113, 0xffff0000, v80\n"
    "  v_lshlrev_b32 v114, 16, v81\n"
    "  v_and_b32 v115, 0xffff0000, v81\n"
    "  s_waitcnt lgkmcnt(2)\n"
    "  v_lshlrev_b32 v116, 16, v82\n"
    "  v_and_b32 v117, 0xffff0000, v82\n"
    "  v_lshlrev_b32 v118, 16, v83\n"
    "  v_and_b32 v119, 0xffff0000, v83\n"
    "  s_waitcnt lgkmcnt(1)\n"
    "  v_lshlrev_b32 v120, 16, v84\n"
    "  v_and_b32 v121, 0xffff0000, v84\n"
    "  v_lshlrev_b32 v122, 16, v85\n"
    "  v_and_b32 v123, 0xffff0000, v85\n"
    "  s_waitcnt lgkmcnt(0)\n"
    "  v_lshlrev_b32 v124, 16, v86\n"
    "  v_and_b32 v125, 0xffff0000, v86\n"
    "  v_lshlrev_b32 v126, 16, v87\n"
    "  v_and_b32 v127, 0xffff0000, v87\n"
    "  s_lshl_b32 s67, s44, 6\n"
    "  s_lshl4_add_u32 s67, s40, s67\n"
    "  s_lshl_b32 s67, s67, 13\n"
    "  s_lshl_b32 s66, s43, 7\n"
    "  s_add_u32 s66, s67, s66\n"
    "  s_add_u32 s48, s66, 0xc00000\n"
    "  v_lshrrev_b32 v111, 3, v1\n"
    "  v_bfe_u32 v110, v111, 1, 1\n"
    "  v_lshlrev_b32 v110, 2, v110\n"
    "  v_lshrrev_b32 v103, 5, v1\n"
    "  v_lshlrev_b32 v103, 1, v103\n"
    "  v_and_b32 v111, 3, v111\n"
    "  v_and_b32 v111, 1, v111\n"
    "  v_add3_u32 v111, v110, v103, v111\n"
    "  v_and_b32 v110, 7, v1\n"
    "  v_lshlrev_b32 v110, 4, v110\n"
    "  v_lshl_add_u32 v108, v111, 13, v110\n"
    "  s_lshl_b32 s66, s2, 8\n"
    "  s_add_u32 s50, s66, 0x1000000\n"
    "  v_lshrrev_b32 v111, 3, v0\n"
    "  v_and_b32 v110, 7, v0\n"
    "  v_lshlrev_b32 v110, 4, v110\n"
    "  v_lshl_add_u32 v109, v111, 16, v110\n"
    "  s_add_u32 s49, s48, 0x00010000\n"
    "  v_mov_b32 v104, -1\n"
    "  v_mov_b32 v105, -1\n"
    "  v_mov_b32 v106, -1\n"
    "  v_mov_b32 v107, -1\n"
    "  s_lshl_b32 s67, s40, 13\n"
    "  s_add_u32 s67, s67, 0x10000\n"
    "  v_add_u32 v11, s67, v2\n"
    "  s_add_u32 s67, s40, 1\n"
    "  s_and_b32 s67, s67, 3\n"
    "  s_lshl_b32 s67, s67, 13\n"
    "  s_add_u32 s67, s67, 0x10000\n"
    "  v_add_u32 v13, s67, v2\n"
    "  s_add_u32 s67, s40, 1\n"
    "  s_add_u32 s67, s67, 1\n"
    "  s_and_b32 s67, s67, 3\n"
    "  s_lshl_b32 s67, s67, 13\n"
    "  s_add_u32 s67, s67, 0x10000\n"
    "  v_add_u32 v14, s67, v2\n"
    "  s_add_u32 s67, s40, 3\n"
    "  s_and_b32 s67, s67, 3\n"
    "  s_lshl_b32 s67, s67, 13\n"
    "  s_add_u32 s34, s67, 0x10000\n"
    "  s_waitcnt vmcnt(0)\n"
    "  s_mov_b32 s55, 0x00008000\n"
    "Lac_17%=:\n"
    "  s_mov_b32 s54, 0\n"
    "  v_max3_u32 v10, v19, v23, v27\n"
    "  v_max3_u32 v10, v10, v31, v35\n"
    "  v_max3_u32 v10, v10, v39, v43\n"
    "  v_max_u32 v10, v10, v47\n"
    "  v_cmp_eq_u32 vcc, -1, v10\n"
    "  s_cbranch_vccz Laq_18%=\n"
    "  s_mov_b32 s54, 1\n"
    "Laq_18%=:\n"
    "  v_max3_u32 v10, v48, v49, v50\n"
    "  v_max3_u32 v10, v10, v51, v52\n"
    "  v_max3_u32 v10, v10, v53, v54\n"
    "  v_max3_u32 v10, v10, v55, v56\n"
    "  v_max3_u32 v10, v10, v57, v58\n"
    "  v_max3_u32 v10, v10, v59, v60\n"
    "  v_max3_u32 v10, v10, v61, v62\n"
    "  v_max3_u32 v10, v10, v63, v64\n"
    "  v_max3_u32 v10, v10, v65, v66\n"
    "  v_max3_u32 v10, v10, v67, v68\n"
    "  v_max3_u32 v10, v10, v69, v70\n"
    "  v_max3_u32 v10, v10, v71, v72\n"
    "  v_max3_u32 v10, v10, v73, v74\n"
    "  v_max3_u32 v10, v10, v75, v76\n"
    "  v_max3_u32 v10, v10, v77, v78\n"
    "  v_max_u32 v10, v10, v79\n"
    "  s_mov_b32 exec_lo, 0x0011ffff\n"
    "  s_mov_b32 exec_hi, 0x0011ffff\n"
    "  v_cmp_eq_u32 vcc, -1, v10\n"
    "  s_mov_b64 exec, -1\n"
    "  s_cbranch_vccz Lak_19%=\n"
    "  s_or_b32 s54, s54, 2\n"
    "Lak_19%=:\n"
    "  s_cmp_eq_u32 s54, 0\n"
    "  s_cbranch_scc1 Lao_20%=\n"
    "  s_sub_u32 s55, s55, 1\n"
    "  s_cbranch_scc1 Lao_20%=\n"
    "  s_sleep 2\n"
    "  s_bitcmp1_b32 s54, 1\n"
    "  s_cbranch_scc0 Laq2_21%=\n"
    "  s_mov_b32 exec_lo, 0x0011ffff\n"
    "  s_mov_b32 exec_hi, 0x0011ffff\n"
    "  v_max3_u32 v10, v48, v49, v50\n"
    "  v_max_u32 v10, v10, v51\n"
    "  v_cmp_eq_u32 vcc, -1, v10\n"
    "  s_cbranch_vccz Las_23%=\n"
    "  buffer_load_dwordx4 v[48:51], v12, s[16:19], s26 offen sc1\n"
    "Las_23%=:\n"
    "  v_max3_u32 v10, v52, v53, v54\n"
    "  v_max_u32 v10, v10, v55\n"
    "  v_cmp_eq_u32 vcc, -1, v10\n"
    "  s_cbranch_vccz Las_24%=\n"
    "  buffer_load_dwordx4 v[52:55], v12, s[16:19], s26 offen offset:1024 sc1\n"
    "Las_24%=:\n"
    "  v_max3_u32 v10, v56, v57, v58\n"
    "  v_max_u32 v10, v10, v59\n"
    "  v_cmp_eq_u32 vcc, -1, v10\n"
    "  s_cbranch_vccz Las_25%=\n"
    "  buffer_load_dwordx4 v[56:59], v12, s[16:19], s26 offen offset:2048 sc1\n"
    "Las_25%=:\n"
    "  v_max3_u32 v10, v60, v61, v62\n"
    "  v_max_u32 v10, v10, v63\n"
    "  v_cmp_eq_u32 vcc, -1, v10\n"
    "  s_cbranch_vccz Las_26%=\n"
    "  buffer_load_dwordx4 v[60:63], v12, s[16:19], s26 offen offset:3072 sc1\n"
    "Las_26%=:\n"
    "  v_max3_u32 v10, v64, v65, v66\n"
    "  v_max_u32 v10, v10, v67\n"
    "  v_cmp_eq_u32 vcc, -1, v10\n"
    "  s_cbranch_vccz Las_27%=\n"
    "  buffer_load_dwordx4 v[64:67], v12, s[16:19], s27 offen sc1\n"
    "Las_27%=:\n"
    "  v_max3_u32 v10, v68, v69, v70\n"
    "  v_max_u32 v10, v10, v71\n"
    "  v_cmp_eq_u32 vcc, -1, v10\n"
    "  s_cbranch_vccz Las_28%=\n"
    "  buffer_load_dwordx4 v[68:71], v12, s[16:19], s27 offen offset:1024 sc1\n"
    "Las_28%=:\n"
    "  v_max3_u32 v10, v72, v73, v74\n"
    "  v_max_u32 v10, v10, v75\n"
    "  v_cmp_eq_u32 vcc, -1, v10\n"
    "  s_cbranch_vccz Las_29%=\n"
    "  buffer_load_dwordx4 v[72:75], v12, s[16:19], s27 offen offset:2048 sc1\n"
    "Las_29%=:\n"
    "  v_max3_u32 v10, v76, v77, v78\n"
    "  v_max_u32 v10, v10, v79\n"
    "  v_cmp_eq_u32 vcc, -1, v10\n"
    "  s_cbranch_vccz Las_30%=\n"
    "  buffer_load_dwordx4 v[76:79], v12, s[16:19], s27 offen offset:3072 sc1\n"
    "Las_30%=:\n"
    "  s_mov_b64 exec, -1\n"
    "Laq2_21%=:\n"
    "  s_bitcmp1_b32 s54, 0\n"
    "  s_cbranch_scc0 Lak2_22%=\n"
    "  v_cmp_eq_u32 vcc, -1, v19\n"
    "  s_cbranch_vccz Las_31%=\n"
    "  buffer_load_dwordx4 v[16:19], v15, s[16:19], s24 offen sc1\n"
    "Las_31%=:\n"
    "  v_cmp_eq_u32 vcc, -1, v23\n"
    "  s_cbranch_vccz Las_32%=\n"
    "  buffer_load_dwordx4 v[20:23], v15, s[16:19], s24 offen offset:1024 sc1\n"
    "Las_32%=:\n"
    "  v_cmp_eq_u32 vcc, -1, v27\n"
    "  s_cbranch_vccz Las_33%=\n"
    "  buffer_load_dwordx4 v[24:27], v15, s[16:19], s24 offen offset:2048 sc1\n"
    "Las_33%=:\n"
    "  v_cmp_eq_u32 vcc, -1, v31\n"
    "  s_cbranch_vccz Las_34%=\n"
    "  buffer_load_dwordx4 v[28:31], v15, s[16:19], s24 offen offset:3072 sc1\n"
    "Las_34%=:\n"
    "  v_cmp_eq_u32 vcc, -1, v35\n"
    "  s_cbranch_vccz Las_35%=\n"
    "  buffer_load_dwordx4 v[32:35], v15, s[16:19], s25 offen sc1\n"
    "Las_35%=:\n"
    "  v_cmp_eq_u32 vcc, -1, v39\n"
    "  s_cbranch_vccz Las_36%=\n"
    "  buffer_load_dwordx4 v[36:39], v15, s[16:19], s25 offen offset:1024 sc1\n"
    "Las_36%=:\n"
    "  v_cmp_eq_u32 vcc, -1, v43\n"
    "  s_cbranch_vccz Las_37%=\n"
    "  buffer_load_dwordx4 v[40:43], v15, s[16:19], s25 offen offset:2048 sc1\n"
    "Las_37%=:\n"
    "  v_cmp_eq_u32 vcc, -1, v47\n"
    "  s_cbranch_vccz Las_38%=\n"
    "  buffer_load_dwordx4 v[44:47], v15, s[16:19], s25 offen offset:3072 sc1\n"
    "Las_38%=:\n"
    "Lak2_22%=:\n"
    "  s_waitcnt vmcnt(0)\n"
    "  s_branch Lac_17%=\n"
    "Lao_20%=:\n"
    "  buffer_store_dwordx4 v[104:107], v108, s[16:19], s48 offen sc1\n"
    "  buffer_store_dwordx4 v[104:107], v108, s[16:19], s49 offen sc1\n"
    "  buffer_store_dwordx4 v[104:107], v109, s[16:19], s50 offen sc1\n"
    "  buffer_store_dwordx4 v[104:107], v109, s[16:19], s50 offen offset:128 sc1\n"
    "  v_add_u32 v10, s34, v2\n"
    "  v_mfma_f32_32x32x16_bf16 v[80:95], v[48:51], v[16:19], 0\n"
    "  v_mfma_f32_32x32x16_bf16 v[96:111], v[52:55], v[20:23], 0\n"
    "  s_nop 10\n"
    "  v_add_f32 v88, 0x358637bd, v88\n"
    "  v_rcp_f32 v88, v88\n"
    "  s_nop 0\n"
    "  v_pk_mul_f32 v[80:81], v[80:81], v[88:89] op_sel_hi:[1,0]\n"
    "  v_pk_mul_f32 v[82:83], v[82:83], v[88:89] op_sel_hi:[1,0]\n"
    "  v_pk_mul_f32 v[84:85], v[84:85], v[88:89] op_sel_hi:[1,0]\n"
    "  v_pk_mul_f32 v[86:87], v[86:87], v[88:89] op_sel_hi:[1,0]\n"
    "  v_cvt_pk_bf16_f32 v16, v80, v81\n"
    "  v_cvt_pk_bf16_f32 v17, v82, v83\n"
    "  v_cvt_pk_bf16_f32 v18, v84, v85\n"
    "  v_cvt_pk_bf16_f32 v19, v86, v87\n"
    "  v_mfma_f32_32x32x16_bf16 v[80:95], v[56:59], v[24:27], 0\n"
    "  ds_write_b128 v11, v[16:19]\n"
    "  v_mfma_f32_32x32x16_bf16 v[112:127], v[128:131], v[16:19], v[112:127]\n"
    "  s_add_u32 s28, s30, 24\n"
    "  s_and_b32 s28, s28, 31\n"
    "  s_lshl_b32 s28, s28, 10\n"
    "  s_add_u32 s28, s28, s29\n"
    "  v_add_f32 v104, 0x358637bd, v104\n"
    "  v_rcp_f32 v104, v104\n"
    "  s_nop 0\n"
    "  v_pk_mul_f32 v[96:97], v[96:97], v[104:105] op_sel_hi:[1,0]\n"
    "  v_pk_mul_f32 v[98:99], v[98:99], v[104:105] op_sel_hi:[1,0]\n"
    "  v_pk_mul_f32 v[100:101], v[100:101], v[104:105] op_sel_hi:[1,0]\n"
    "  v_pk_mul_f32 v[102:103], v[102:103], v[104:105] op_sel_hi:[1,0]\n"
    "  v_cvt_pk_bf16_f32 v20, v96, v97\n"
    "  v_cvt_pk_bf16_f32 v21, v98, v99\n"
    "  v_cvt_pk_bf16_f32 v22, v100, v101\n"
    "  v_cvt_pk_bf16_f32 v23, v102, v103\n"
    "  v_mfma_f32_32x32x16_bf16 v[96:111], v[60:63], v[28:31], 0\n"
    "  ds_write_b128 v11, v[20:23] offset:1024\n"
    "  v_mfma_f32_32x32x16_bf16 v[112:127], v[132:135], v[20:23], v[112:127]\n"
    "  buffer_load_dwordx4 v[224:227], v2, s[16:19], s28 offen sc1\n"
    "  v_add_f32 v88, 0x358637bd, v88\n"
    "  v_rcp_f32 v88, v88\n"
    "  s_nop 0\n"
    "  v_pk_mul_f32 v[80:81], v[80:81], v[88:89] op_sel_hi:[1,0]\n"
    "  v_pk_mul_f32 v[82:83], v[82:83], v[88:89] op_sel_hi:[1,0]\n"
    "  v_pk_mul_f32 v[84:85], v[84:85], v[88:89] op_sel_hi:[1,0]\n"
    "  v_pk_mul_f32 v[86:87], v[86:87], v[88:89] op_sel_hi:[1,0]\n"
    "  v_cvt_pk_bf16_f32 v24, v80, v81\n"
    "  v_cvt_pk_bf16_f32 v25, v82, v83\n"
    "  v_cvt_pk_bf16_f32 v26, v84, v85\n"
    "  v_cvt_pk_bf16_f32 v27, v86, v87\n"
    "  v_mfma_f32_32x32x16_bf16 v[80:95], v[64:67], v[32:35], 0\n"
    "  ds_write_b128 v11, v[24:27] offset:2048\n"
    "  v_mfma_f32_32x32x16_bf16 v[112:127], v[136:139], v[24:27], v[112:127]\n"
    "  buffer_load_dwordx4 v[228:231], v2, s[16:19], s28 offen offset:1024 sc1\n"
    "  v_add_f32 v104, 0x358637bd, v104\n"
    "  v_rcp_f32 v104, v104\n"
    "  s_nop 0\n"
    "  v_pk_mul_f32 v[96:97], v[96:97], v[104:105] op_sel_hi:[1,0]\n"
    "  v_pk_mul_f32 v[98:99], v[98:99], v[104:105] op_sel_hi:[1,0]\n"
    "  v_pk_mul_f32 v[100:101], v[100:101], v[104:105] op_sel_hi:[1,0]\n"
    "  v_pk_mul_f32 v[102:103], v[102:103], v[104:105] op_sel_hi:[1,0]\n"
    "  v_cvt_pk_bf16_f32 v28, v96, v97\n"
    "  v_cvt_pk_bf16_f32 v29, v98, v99\n"
    "  v_cvt_pk_bf16_f32 v30, v100, v101\n"
    "  v_cvt_pk_bf16_f32 v31, v102, v103\n"
    "  v_mfma_f32_32x32x16_bf16 v[96:111], v[68:71], v[36:39], 0\n"
    "  ds_write_b128 v11, v[28:31] offset:3072\n"
    "  v_mfma_f32_32x32x16_bf16 v[112:127], v[140:143], v[28:31], v[112:127]\n"
    "  buffer_load_dwordx4 v[232:235], v2, s[16:19], s28 offen offset:2048 sc1\n"
    "  v_add_f32 v88, 0x358637bd, v88\n"
    "  v_rcp_f32 v88, v88\n"
    "  s_nop 0\n"
    "  v_pk_mul_f32 v[80:81], v[80:81], v[88:89] op_sel_hi:[1,0]\n"
    "  v_pk_mul_f32 v[82:83], v[82:83], v[88:89] op_sel_hi:[1,0]\n"
    "  v_pk_mul_f32 v[84:85], v[84:85], v[88:89] op_sel_hi:[1,0]\n"
    "  v_pk_mul_f32 v[86:87], v[86:87], v[88:89] op_sel_hi:[1,0]\n"
    "  v_cvt_pk_bf16_f32 v32, v80, v81\n"
    "  v_cvt_pk_bf16_f32 v33, v82, v83\n"
    "  v_cvt_pk_bf16_f32 v34, v84, v85\n"
    "  v_cvt_pk_bf16_f32 v35, v86, v87\n"
    "  v_mfma_f32_32x32x16_bf16 v[80:95], v[72:75], v[40:43], 0\n"
    "  ds_write_b128 v11, v[32:35] offset:4096\n"
    "  v_mfma_f32_32x32x16_bf16 v[112:127], v[144:147], v[32:35], v[112:127]\n"
    "  buffer_load_dwordx4 v[236:239], v2, s[16:19], s28 offen offset:3072 sc1\n"
    "  v_add_f32 v104, 0x358637bd, v104\n"
    "  v_rcp_f32 v104, v104\n"
    "  s_nop 0\n"
    "  v_pk_mul_f32 v[96:97], v[96:97], v[104:105] op_sel_hi:[1,0]\n"
    "  v_pk_mul_f32 v[98:99], v[98:99], v[104:105] op_sel_hi:[1,0]\n"
    "  v_pk_mul_f32 v[100:101], v[100:101], v[104:105] op_sel_hi:[1,0]\n"
    "  v_pk_mul_f32 v[102:103], v[102:103], v[104:105] op_sel_hi:[1,0]\n"
    "  v_cvt_pk_bf16_f32 v36, v96, v97\n"
    "  v_cvt_pk_bf16_f32 v37, v98, v99\n"
    "  v_cvt_pk_bf16_f32 v38, v100, v101\n"
    "  v_cvt_pk_bf16_f32 v39, v102, v103\n"
    "  v_mfma_f32_32x32x16_bf16 v[96:111], v[76:79], v[44:47], 0\n"
    "  ds_write_b128 v11, v[36:39] offset:5120\n"
    "  v_mfma_f32_32x32x16_bf16 v[112:127], v[148:151], v[36:39], v[112:127]\n"
    "  v_add_f32 v88, 0x358637bd, v88\n"
    "  v_rcp_f32 v88, v88\n"
    "  s_nop 0\n"
    "  v_pk_mul_f32 v[80:81], v[80:81], v[88:89] op_sel_hi:[1,0]\n"
    "  v_pk_mul_f32 v[82:83], v[82:83], v[88:89] op_sel_hi:[1,0]\n"
    "  v_pk_mul_f32 v[84:85], v[84:85], v[88:89] op_sel_hi:[1,0]\n"
    "  v_pk_mul_f32 v[86:87], v[86:87], v[88:89] op_sel_hi:[1,0]\n"
    "  v_cvt_pk_bf16_f32 v40, v80, v81\n"
    "  v_cvt_pk_bf16_f32 v41, v82, v83\n"
    "  v_cvt_pk_bf16_f32 v42, v84, v85\n"
    "  v_cvt_pk_bf16_f32 v43, v86, v87\n"
    "  ds_write_b128 v11, v[40:43] offset:6144\n"
    "  v_add_f32 v104, 0x358637bd, v104\n"
    "  v_rcp_f32 v104, v104\n"
    "  s_nop 0\n"
    "  v_pk_mul_f32 v[96:97], v[96:97], v[104:105] op_sel_hi:[1,0]\n"
    "  v_pk_mul_f32 v[98:99], v[98:99], v[104:105] op_sel_hi:[1,0]\n"
    "  v_pk_mul_f32 v[100:101], v[100:101], v[104:105] op_sel_hi:[1,0]\n"
    "  v_pk_mul_f32 v[102:103], v[102:103], v[104:105] op_sel_hi:[1,0]\n"
    "  v_cvt_pk_bf16_f32 v44, v96, v97\n"
    "  v_cvt_pk_bf16_f32 v45, v98, v99\n"
    "  v_cvt_pk_bf16_f32 v46, v100, v101\n"
    "  v_cvt_pk_bf16_f32 v47, v102, v103\n"
    "  ds_write_b128 v11, v[44:47] offset:7168\n"
    "  s_waitcnt lgkmcnt(0)\n"
    "  s_barrier\n"
    "  ds_read_b128 v[16:19], v13\n"
    "  ds_read_b128 v[20:23], v13 offset:1024\n"
    "  ds_read_b128 v[24:27], v13 offset:2048\n"
    "  ds_read_b128 v[28:31], v13 offset:3072\n"
    "  ds_read_b128 v[32:35], v13 offset:4096\n"
    "  ds_read_b128 v[36:39], v13 offset:5120\n"
    "  v_mfma_f32_32x32x16_bf16 v[112:127], v[152:155], v[40:43], v[112:127]\n"
    "  ds_read_b128 v[40:43], v13 offset:6144\n"
    "  v_mfma_f32_32x32x16_bf16 v[112:127], v[156:159], v[44:47], v[112:127]\n"
    "  ds_read_b128 v[44:47], v13 offset:7168\n"
    "  s_waitcnt lgkmcnt(6)\n"
    "  v_mfma_f32_32x32x16_bf16 v[112:127], v[160:163], v[16:19], v[112:127]\n"
    "  ds_read_b128 v[16:19], v14\n"
    "  s_lshl_b32 s67, s44, 2\n"
    "  s_lshl4_add_u32 s67, s43, s67\n"
    "  s_add_u32 s67, s67, s40\n"
    "  v_mfma_f32_32x32x16_bf16 v[112:127], v[164:167], v[20:23], v[112:127]\n"
    "  ds_read_b128 v[20:23], v14 offset:1024\n"
    "  s_lshl_b32 s67, s67, 8\n"
    "  s_add_u32 s51, s67, 0x40000\n"
    "  s_lshl_b32 s67, s43, 12\n"
    "  s_waitcnt lgkmcnt(6)\n"
    "  v_mfma_f32_32x32x16_bf16 v[112:127], v[168:171], v[24:27], v[112:127]\n"
    "  ds_read_b128 v[24:27], v14 offset:2048\n"
    "  s_add_u32 s33, s67, 0x40000\n"
    "  s_lshl_b32 s67, s44, 6\n"
    "  s_lshl4_add_u32 s67, s40, s67\n"
    "  v_mfma_f32_32x32x16_bf16 v[112:127], v[172:175], v[28:31], v[112:127]\n"
    "  ds_read_b128 v[28:31], v14 offset:3072\n"
    "  s_lshl_b32 s67, s67, 13\n"
    "  s_lshl_b32 s66, s43, 7\n"
    "  s_add_u32 s67, s67, s66\n"
    "  s_waitcnt lgkmcnt(6)\n"
    "  v_mfma_f32_32x32x16_bf16 v[112:127], v[176:179], v[32:35], v[112:127]\n"
    "  ds_read_b128 v[32:35], v14 offset:4096\n"
    "  s_add_u32 s32, s67, 0x800000\n"
    "  v_mfma_f32_32x32x16_bf16 v[112:127], v[180:183], v[36:39], v[112:127]\n"
    "  ds_read_b128 v[36:39], v14 offset:5120\n"
    "  v_and_b32 v83, 7, v1\n"
    "  v_lshlrev_b32 v83, 2, v83\n"
    "  v_bfe_u32 v82, v1, 3, 2\n"
    "  s_waitcnt lgkmcnt(6)\n"
    "  v_mfma_f32_32x32x16_bf16 v[112:127], v[184:187], v[40:43], v[112:127]\n"
    "  ds_read_b128 v[40:43], v14 offset:6144\n"
    "  v_add_lshl_u32 v13, v83, v82, 3\n"
    "  v_lshrrev_b32 v83, 5, v1\n"
    "  v_lshl_add_u32 v15, v83, 11, v13\n"
    "  v_mfma_f32_32x32x16_bf16 v[112:127], v[188:191], v[44:47], v[112:127]\n"
    "  ds_read_b128 v[44:47], v14 offset:7168\n"
    "  s_waitcnt lgkmcnt(6)\n"
    "  v_mfma_f32_32x32x16_bf16 v[112:127], v[192:195], v[16:19], v[112:127]\n"
    "  ds_read_b128 v[16:19], v10\n"
    "  v_lshrrev_b32 v83, 3, v1\n"
    "  v_bfe_u32 v82, v83, 1, 1\n"
    "  v_lshlrev_b32 v82, 2, v82\n"
    "  v_mfma_f32_32x32x16_bf16 v[112:127], v[196:199], v[20:23], v[112:127]\n"
    "  ds_read_b128 v[20:23], v10 offset:1024\n"
    "  v_lshrrev_b32 v81, 5, v1\n"
    "  v_lshlrev_b32 v81, 1, v81\n"
    "  v_and_b32 v83, 3, v83\n"
    "  s_waitcnt lgkmcnt(6)\n"
    "  v_mfma_f32_32x32x16_bf16 v[112:127], v[200:203], v[24:27], v[112:127]\n"
    "  ds_read_b128 v[24:27], v10 offset:2048\n"
    "  v_and_b32 v83, 1, v83\n"
    "  v_add3_u32 v82, v82, v81, v83\n"
    "  v_and_b32 v81, 7, v1\n"
    "  v_mfma_f32_32x32x16_bf16 v[112:127], v[204:207], v[28:31], v[112:127]\n"
    "  ds_read_b128 v[28:31], v10 offset:3072\n"
    "  v_lshlrev_b32 v81, 4, v81\n"
    "  v_lshl_add_u32 v14, v82, 13, v81\n"
    "  s_waitcnt lgkmcnt(6)\n"
    "  v_mfma_f32_32x32x16_bf16 v[112:127], v[208:211], v[32:35], v[112:127]\n"
    "  ds_read_b128 v[32:35], v10 offset:4096\n"
    "  v_mfma_f32_32x32x16_bf16 v[112:127], v[212:215], v[36:39], v[112:127]\n"
    "  ds_read_b128 v[36:39], v10 offset:5120\n"
    "  s_waitcnt lgkmcnt(6)\n"
    "  v_mfma_f32_32x32x16_bf16 v[112:127], v[216:219], v[40:43], v[112:127]\n"
    "  ds_read_b128 v[40:43], v10 offset:6144\n"
    "  v_mfma_f32_32x32x16_bf16 v[112:127], v[220:223], v[44:47], v[112:127]\n"
    "  ds_read_b128 v[44:47], v10 offset:7168\n"
    "  s_waitcnt vmcnt(3) lgkmcnt(6)\n"
    "  v_mfma_f32_32x32x16_bf16 v[112:127], v[224:227], v[16:19], v[112:127]\n"
    "  s_waitcnt vmcnt(2)\n"
    "  v_mfma_f32_32x32x16_bf16 v[112:127], v[228:231], v[20:23], v[112:127]\n"
    "  s_waitcnt vmcnt(1) lgkmcnt(5)\n"
    "  v_mfma_f32_32x32x16_bf16 v[112:127], v[232:235], v[24:27], v[112:127]\n"
    "  s_waitcnt vmcnt(0) lgkmcnt(4)\n"
    "  v_mfma_f32_32x32x16_bf16 v[112:127], v[236:239], v[28:31], v[112:127]\n"
    "  s_waitcnt lgkmcnt(3)\n"
    "  v_mfma_f32_32x32x16_bf16 v[112:127], v[240:243], v[32:35], v[112:127]\n"
    "  s_waitcnt lgkmcnt(2)\n"
    "  v_mfma_f32_32x32x16_bf16 v[112:127], v[244:247], v[36:39], v[112:127]\n"
    "  s_waitcnt lgkmcnt(1)\n"
    "  v_mfma_f32_32x32x16_bf16 v[112:127], v[248:251], v[40:43], v[112:127]\n"
    "  s_waitcnt lgkmcnt(0)\n"
    "  v_mfma_f32_32x32x16_bf16 v[112:127], v[252:255], v[44:47], v[112:127]\n"
    "  s_barrier\n"
    "  v_mov_b32 v44, s46\n"
    "  v_lshlrev_b32 v45, 12, v1\n"
    "  s_lshl2_add_u32 s48, s2, 0x11000\n"
    "  s_cmp_eq_u32 s40, 0\n"
    "  s_cselect_b64 exec, 0xff, 0\n"
    "  buffer_store_dword v44, v45, s[16:19], s48 offen sc1\n"
    "  s_mov_b64 exec, -1\n"
    "  s_nop 3\n"
    "  v_cvt_pk_bf16_f32 v16, v112, v113\n"
    "  v_cvt_pk_bf16_f32 v17, v114, v115\n"
    "  v_cvt_pk_bf16_f32 v18, v116, v117\n"
    "  v_cvt_pk_bf16_f32 v19, v118, v119\n"
    "  v_cvt_pk_bf16_f32 v20, v120, v121\n"
    "  v_cvt_pk_bf16_f32 v21, v122, v123\n"
    "  v_cvt_pk_bf16_f32 v22, v124, v125\n"
    "  v_cvt_pk_bf16_f32 v23, v126, v127\n"
    "  v_mov_b32 v28, 0\n"
    "  v_mov_b32 v29, 0\n"
    "  v_dot2c_f32_bf16 v28, 0x3f803f80, v16\n"
    "  v_dot2c_f32_bf16 v29, v16, v16\n"
    "  v_dot2c_f32_bf16 v28, 0x3f803f80, v17\n"
    "  v_dot2c_f32_bf16 v29, v17, v17\n"
    "  v_dot2c_f32_bf16 v28, 0x3f803f80, v18\n"
    "  v_dot2c_f32_bf16 v29, v18, v18\n"
    "  v_dot2c_f32_bf16 v28, 0x3f803f80, v19\n"
    "  v_dot2c_f32_bf16 v29, v19, v19\n"
    "  v_dot2c_f32_bf16 v28, 0x3f803f80, v20\n"
    "  v_dot2c_f32_bf16 v29, v20, v20\n"
    "  v_dot2c_f32_bf16 v28, 0x3f803f80, v21\n"
    "  v_dot2c_f32_bf16 v29, v21, v21\n"
    "  v_dot2c_f32_bf16 v28, 0x3f803f80, v22\n"
    "  v_dot2c_f32_bf16 v29, v22, v22\n"
    "  v_dot2c_f32_bf16 v28, 0x3f803f80, v23\n"
    "  v_dot2c_f32_bf16 v29, v23, v23\n"
    "  s_nop 1\n"
    "  v_mov_b32 v30, v28\n"
    "  v_mov_b32 v31, v29\n"
    "  s_nop 0\n"
    "  v_permlane32_swap_b32 v28, v30\n"
    "  v_permlane32_swap_b32 v29, v31\n"
    "  v_add_f32 v28, v28, v30\n"
    "  v_add_f32 v29, v29, v31\n"
    "  s_mov_b32 exec_lo, 0xffffffff\n"
    "  s_mov_b32 exec_hi, 0x00000000\n"
    "  buffer_store_dwordx2 v[28:29], v13, s[16:19], s51 offen sc1\n"
    "  s_mov_b64 exec, -1\n"
    "  s_add_u32 s53, s32, 0x00010000\n"
    "  s_nop 3\n"
    "  v_permlane16_swap_b32 v16, v18\n"
    "  v_permlane16_swap_b32 v17, v19\n"
    "  v_permlane16_swap_b32 v20, v22\n"
    "  v_permlane16_swap_b32 v21, v23\n"
    "  v_mov_b32 v24, v16\n"
    "  v_mov_b32 v25, v18\n"
    "  v_mov_b32 v26, v20\n"
    "  v_mov_b32 v27, v22\n"
    "  v_mov_b32_dpp v16, v17 row_ror:8 row_mask:0xf bank_mask:0xc\n"
    "  v_mov_b32_dpp v18, v19 row_ror:8 row_mask:0xf bank_mask:0xc\n"
    "  v_mov_b32_dpp v20, v21 row_ror:8 row_mask:0xf bank_mask:0xc\n"
    "  v_mov_b32_dpp v22, v23 row_ror:8 row_mask:0xf bank_mask:0xc\n"
    "  v_mov_b32_dpp v17, v24 row_ror:8 row_mask:0xf bank_mask:0x3\n"
    "  v_mov_b32_dpp v19, v25 row_ror:8 row_mask:0xf bank_mask:0x3\n"
    "  v_mov_b32_dpp v21, v26 row_ror:8 row_mask:0xf bank_mask:0x3\n"
    "  v_mov_b32_dpp v23, v27 row_ror:8 row_mask:0xf bank_mask:0x3\n"
    "  buffer_store_dwordx4 v[16:19], v14, s[16:19], s32 offen sc1\n"
    "  buffer_store_dwordx4 v[20:23], v14, s[16:19], s53 offen sc1\n"
    "  s_add_u32 s52, s32, 0x00400000\n"
    "  s_add_u32 s53, s32, 0x00410000\n"
    "  v_and_b32 v109, 15, v1\n"
    "  v_mul_u32_u24 v109, 0x210, v109\n"
    "  v_lshrrev_b32 v108, 4, v1\n"
    "  v_lshlrev_b32 v108, 4, v108\n"
    "  s_lshr_b32 s67, s40, 1\n"
    "  s_mul_i32 s67, s67, 0x6d50\n"
    "  s_add_u32 s67, s67, 0x18200\n"
    "  v_add3_u32 v48, v109, v108, s67\n"
    "  s_and_b32 s67, s40, 1\n"
    "  s_mul_i32 s67, s67, 0x2100\n"
    "  s_add_u32 s67, s67, 0x2b50\n"
    "  v_add_u32 v49, s67, v48\n"
    "  ds_read_b128 v[60:63], v49\n"
    "  ds_read_b128 v[64:67], v48\n"
    "  ds_read_b128 v[68:71], v48 offset:8448\n"
    "  ds_read_b128 v[72:75], v49 offset:64\n"
    "  ds_read_b128 v[76:79], v48 offset:64\n"
    "  ds_read_b128 v[80:83], v48 offset:8512\n"
    "  ds_read_b128 v[84:87], v49 offset:128\n"
    "  ds_read_b128 v[88:91], v48 offset:128\n"
    "  ds_read_b128 v[92:95], v48 offset:8576\n"
    "  s_and_b32 s67, s40, 1\n"
    "  s_lshl_b32 s67, s67, 4\n"
    "  v_and_b32 v108, 15, v1\n"
    "  v_add_u32 v109, s67, v108\n"
    "  s_waitcnt lgkmcnt(6)\n"
    "  v_mfma_f32_16x16x32_bf16 v[52:55], v[64:67], v[60:63], 0\n"
    "  v_mfma_f32_16x16x32_bf16 v[56:59], v[68:71], v[60:63], 0\n"
    "  ds_read_b128 v[96:99], v49 offset:192\n"
    "  ds_read_b128 v[100:103], v48 offset:192\n"
    "  ds_read_b128 v[104:107], v48 offset:8640\n"
    "  v_lshrrev_b32 v108, 4, v1\n"
    "  s_lshr_b32 s67, s40, 1\n"
    "  s_mul_i32 s67, s67, 0x510\n"
    "  s_add_u32 s67, s67, 0x25ca0\n"
    "  v_lshl_add_u32 v108, v108, 3, s67\n"
    "  v_lshl_add_u32 v110, v109, 4, v108\n"
    "  s_waitcnt lgkmcnt(6)\n"
    "  v_mfma_f32_16x16x32_bf16 v[52:55], v[76:79], v[72:75], v[52:55]\n"
    "  v_mfma_f32_16x16x32_bf16 v[56:59], v[80:83], v[72:75], v[56:59]\n"
    "  ds_read_b128 v[60:63], v49 offset:256\n"
    "  ds_read_b128 v[64:67], v48 offset:256\n"
    "  ds_read_b128 v[68:71], v48 offset:8704\n"
    "  s_lshr_b32 s67, s40, 1\n"
    "  s_mul_i32 s67, s67, 0x510\n"
    "  s_add_u32 s67, s67, 0x25ca0\n"
    "  s_add_u32 s67, s67, 0x200\n"
    "  v_lshl_add_u32 v111, v109, 3, s67\n"
    "  s_waitcnt lgkmcnt(6)\n"
    "  v_mfma_f32_16x16x32_bf16 v[52:55], v[88:91], v[84:87], v[52:55]\n"
    "  v_mfma_f32_16x16x32_bf16 v[56:59], v[92:95], v[84:87], v[56:59]\n"
    "  ds_read_b128 v[72:75], v49 offset:320\n"
    "  ds_read_b128 v[76:79], v48 offset:320\n"
    "  ds_read_b128 v[80:83], v48 offset:8768\n"
    "  s_lshr_b32 s67, s40, 1\n"
    "  s_mul_i32 s67, s67, 0x510\n"
    "  s_add_u32 s67, s67, 0x25ca0\n"
    "  s_add_u32 s67, s67, 0x300\n"
    "  v_lshl_add_u32 v50, v109, 2, s67\n"
    "  s_waitcnt lgkmcnt(6)\n"
    "  v_mfma_f32_16x16x32_bf16 v[52:55], v[100:103], v[96:99], v[52:55]\n"
    "  v_mfma_f32_16x16x32_bf16 v[56:59], v[104:107], v[96:99], v[56:59]\n"
    "  ds_read_b128 v[84:87], v49 offset:384\n"
    "  ds_read_b128 v[88:91], v48 offset:384\n"
    "  ds_read_b128 v[92:95], v48 offset:8832\n"
    "  s_lshr_b32 s67, s40, 1\n"
    "  s_mul_i32 s67, s67, 0x510\n"
    "  s_add_u32 s67, s67, 0x25ca0\n"
    "  s_add_u32 s67, s67, 0x400\n"
    "  v_lshl_add_u32 v51, v109, 1, s67\n"
    "  s_waitcnt lgkmcnt(6)\n"
    "  v_mfma_f32_16x16x32_bf16 v[52:55], v[64:67], v[60:63], v[52:55]\n"
    "  v_mfma_f32_16x16x32_bf16 v[56:59], v[68:71], v[60:63], v[56:59]\n"
    "  ds_read_b128 v[96:99], v49 offset:448\n"
    "  ds_read_b128 v[100:103], v48 offset:448\n"
    "  ds_read_b128 v[104:107], v48 offset:8896\n"
    "  s_waitcnt lgkmcnt(6)\n"
    "  v_mfma_f32_16x16x32_bf16 v[52:55], v[76:79], v[72:75], v[52:55]\n"
    "  v_mfma_f32_16x16x32_bf16 v[56:59], v[80:83], v[72:75], v[56:59]\n"
    "  s_waitcnt lgkmcnt(4)\n"
    "  v_mfma_f32_16x16x32_bf16 v[52:55], v[88:91], v[84:87], v[52:55]\n"
    "  s_waitcnt lgkmcnt(3)\n"
    "  v_mfma_f32_16x16x32_bf16 v[56:59], v[92:95], v[84:87], v[56:59]\n"
    "  s_waitcnt lgkmcnt(1)\n"
    "  v_mfma_f32_16x16x32_bf16 v[52:55], v[100:103], v[96:99], v[52:55]\n"
    "  s_waitcnt lgkmcnt(0)\n"
    "  v_mfma_f32_16x16x32_bf16 v[56:59], v[104:107], v[96:99], v[56:59]\n"
    "  buffer_load_dwordx2 v[32:33], v15, s[16:19], s33 offen sc1\n"
    "  buffer_load_dwordx2 v[34:35], v15, s[16:19], s33 offen offset:256 sc1\n"
    "  buffer_load_dwordx2 v[36:37], v15, s[16:19], s33 offen offset:512 sc1\n"
    "  buffer_load_dwordx2 v[38:39], v15, s[16:19], s33 offen offset:768 sc1\n"
    "  buffer_load_dwordx2 v[40:41], v15, s[16:19], s33 offen offset:1024 sc1\n"
    "  buffer_load_dwordx2 v[42:43], v15, s[16:19], s33 offen offset:1280 sc1\n"
    "  buffer_load_dwordx2 v[44:45], v15, s[16:19], s33 offen offset:1536 sc1\n"
    "  buffer_load_dwordx2 v[46:47], v15, s[16:19], s33 offen offset:1792 sc1\n"
    "  v_cvt_pk_bf16_f32 v60, v52, v53\n"
    "  v_cvt_pk_bf16_f32 v61, v54, v55\n"
    "  v_cvt_pk_bf16_f32 v62, v56, v57\n"
    "  v_cvt_pk_bf16_f32 v63, v58, v59\n"
    "  s_mov_b32 exec_lo, 0xffffffff\n"
    "  s_mov_b32 exec_hi, 0x00000000\n"
    "  ds_write_b64 v110, v[60:61]\n"
    "  s_mov_b32 exec_lo, 0x00000000\n"
    "  s_mov_b32 exec_hi, 0x0000ffff\n"
    "  ds_write_b64 v111, v[60:61]\n"
    "  s_mov_b32 exec_lo, 0x00000000\n"
    "  s_mov_b32 exec_hi, 0xffff0000\n"
    "  ds_write2_b32 v50, v60, v61 offset0:0 offset1:32\n"
    "  s_mov_b64 exec, 0xffff\n"
    "  ds_write_b16 v51, v62\n"
    "  ds_write_b16_d16_hi v51, v62 offset:64\n"
    "  ds_write_b16 v51, v63 offset:128\n"
    "  ds_write_b16_d16_hi v51, v63 offset:192\n"
    "  s_bitcmp0_b32 s40, 0\n"
    "  s_cselect_b32 exec_lo, 0x10000, 0\n"
    "  ds_write_b16 v51, v62 offset:256\n"
    "  s_mov_b64 exec, -1\n"
    "  s_and_b32 s67, s2, 7\n"
    "  s_lshl_b32 s67, s67, 12\n"
    "  s_add_u32 s54, s67, 0x11000\n"
    "  s_cmp_eq_u32 s40, 0\n"
    "  s_cselect_b64 exec, -1, 0\n"
    "  buffer_load_dwordx4 v[84:87], v2, s[16:19], s54 offen sc1\n"
    "  s_mov_b64 exec, -1\n"
    "  s_waitcnt vmcnt(1)\n"
    "  s_mov_b32 s55, 0x00008000\n"
    "Lc_39%=:\n"
    "  v_max3_u32 v10, v33, v35, v37\n"
    "  v_max3_u32 v10, v10, v39, v41\n"
    "  v_max3_u32 v10, v10, v43, v45\n"
    "  v_max_u32 v10, v10, v47\n"
    "  v_cmp_eq_u32 vcc, -1, v10\n"
    "  s_cbranch_vccz Lo_40%=\n"
    "  s_sub_u32 s55, s55, 1\n"
    "  s_cbranch_scc1 Lo_40%=\n"
    "  s_sleep 2\n"
    "  buffer_load_dwordx2 v[32:33], v15, s[16:19], s33 offen sc1\n"
    "  buffer_load_dwordx2 v[34:35], v15, s[16:19], s33 offen offset:256 sc1\n"
    "  buffer_load_dwordx2 v[36:37], v15, s[16:19], s33 offen offset:512 sc1\n"
    "  buffer_load_dwordx2 v[38:39], v15, s[16:19], s33 offen offset:768 sc1\n"
    "  buffer_load_dwordx2 v[40:41], v15, s[16:19], s33 offen offset:1024 sc1\n"
    "  buffer_load_dwordx2 v[42:43], v15, s[16:19], s33 offen offset:1280 sc1\n"
    "  buffer_load_dwordx2 v[44:45], v15, s[16:19], s33 offen offset:1536 sc1\n"
    "  buffer_load_dwordx2 v[46:47], v15, s[16:19], s33 offen offset:1792 sc1\n"
    "  s_waitcnt vmcnt(0)\n"
    "  s_branch Lc_39%=\n"
    "Lo_40%=:\n"
    "  v_pk_add_f32 v[32:33], v[32:33], v[34:35]\n"
    "  v_pk_add_f32 v[36:37], v[36:37], v[38:39]\n"
    "  v_pk_add_f32 v[40:41], v[40:41], v[42:43]\n"
    "  v_pk_add_f32 v[44:45], v[44:45], v[46:47]\n"
    "  v_pk_add_f32 v[32:33], v[32:33], v[36:37]\n"
    "  v_pk_add_f32 v[40:41], v[40:41], v[44:45]\n"
    "  v_pk_add_f32 v[32:33], v[32:33], v[40:41]\n"
    "  v_mov_b32 v30, v32\n"
    "  v_mov_b32 v31, v33\n"
    "  s_nop 0\n"
    "  v_permlane32_swap_b32 v32, v30\n"
    "  v_permlane32_swap_b32 v33, v31\n"
    "  v_add_f32 v32, v32, v30\n"
    "  v_add_f32 v33, v33, v31\n"
    "  v_mul_f32 v32, 0x3b000000, v32\n"
    "  v_mul_f32 v33, 0x3b000000, v33\n"
    "  v_fma_f32 v33, -v32, v32, v33\n"
    "  v_max_f32 v33, 0, v33\n"
    "  v_cvt_pk_bf16_f32 v32, v32, v32\n"
    "  v_and_b32 v32, 0xffff0000, v32\n"
    "  v_cvt_pk_bf16_f32 v33, v33, v33\n"
    "  v_and_b32 v33, 0xffff0000, v33\n"
    "  v_add_f32 v33, 0x3727c5ac, v33\n"
    "  v_cvt_pk_bf16_f32 v33, v33, v33\n"
    "  v_and_b32 v33, 0xffff0000, v33\n"
    "  v_sqrt_f32 v33, v33\n"
    "  s_nop 0\n"
    "  v_cvt_pk_bf16_f32 v33, v33, v33\n"
    "  v_and_b32 v33, 0xffff0000, v33\n"
    "  v_rcp_f32 v33, v33\n"
    "  s_nop 0\n"
    "  v_mul_f32 v34, 0x3fb8aa3b, v33\n"
    "  v_mul_f32 v34, v32, v33\n"
    "  v_pk_fma_f32 v[112:113], v[112:113], v[32:33], v[34:35] op_sel:[0,1,0] op_sel_hi:[1,1,0] neg_lo:[0,0,1] neg_hi:[0,0,1]\n"
    "  v_pk_fma_f32 v[114:115], v[114:115], v[32:33], v[34:35] op_sel:[0,1,0] op_sel_hi:[1,1,0] neg_lo:[0,0,1] neg_hi:[0,0,1]\n"
    "  v_pk_fma_f32 v[116:117], v[116:117], v[32:33], v[34:35] op_sel:[0,1,0] op_sel_hi:[1,1,0] neg_lo:[0,0,1] neg_hi:[0,0,1]\n"
    "  v_pk_fma_f32 v[118:119], v[118:119], v[32:33], v[34:35] op_sel:[0,1,0] op_sel_hi:[1,1,0] neg_lo:[0,0,1] neg_hi:[0,0,1]\n"
    "  v_pk_fma_f32 v[120:121], v[120:121], v[32:33], v[34:35] op_sel:[0,1,0] op_sel_hi:[1,1,0] neg_lo:[0,0,1] neg_hi:[0,0,1]\n"
    "  v_pk_fma_f32 v[122:123], v[122:123], v[32:33], v[34:35] op_sel:[0,1,0] op_sel_hi:[1,1,0] neg_lo:[0,0,1] neg_hi:[0,0,1]\n"
    "  v_pk_fma_f32 v[124:125], v[124:125], v[32:33], v[34:35] op_sel:[0,1,0] op_sel_hi:[1,1,0] neg_lo:[0,0,1] neg_hi:[0,0,1]\n"
    "  v_pk_fma_f32 v[126:127], v[126:127], v[32:33], v[34:35] op_sel:[0,1,0] op_sel_hi:[1,1,0] neg_lo:[0,0,1] neg_hi:[0,0,1]\n"
    "  v_cvt_pk_bf16_f32 v16, v112, v113\n"
    "  v_cvt_pk_bf16_f32 v17, v114, v115\n"
    "  v_cvt_pk_bf16_f32 v18, v116, v117\n"
    "  v_cvt_pk_bf16_f32 v19, v118, v119\n"
    "  v_cvt_pk_bf16_f32 v20, v120, v121\n"
    "  v_cvt_pk_bf16_f32 v21, v122, v123\n"
    "  v_cvt_pk_bf16_f32 v22, v124, v125\n"
    "  v_cvt_pk_bf16_f32 v23, v126, v127\n"
    "  v_permlane16_swap_b32 v16, v18\n"
    "  v_permlane16_swap_b32 v17, v19\n"
    "  v_permlane16_swap_b32 v20, v22\n"
    "  v_permlane16_swap_b32 v21, v23\n"
    "  v_mov_b32 v24, v16\n"
    "  v_mov_b32 v25, v18\n"
    "  v_mov_b32 v26, v20\n"
    "  v_mov_b32 v27, v22\n"
    "  v_mov_b32_dpp v16, v17 row_ror:8 row_mask:0xf bank_mask:0xc\n"
    "  v_mov_b32_dpp v18, v19 row_ror:8 row_mask:0xf bank_mask:0xc\n"
    "  v_mov_b32_dpp v20, v21 row_ror:8 row_mask:0xf bank_mask:0xc\n"
    "  v_mov_b32_dpp v22, v23 row_ror:8 row_mask:0xf bank_mask:0xc\n"
    "  v_mov_b32_dpp v17, v24 row_ror:8 row_mask:0xf bank_mask:0x3\n"
    "  v_mov_b32_dpp v19, v25 row_ror:8 row_mask:0xf bank_mask:0x3\n"
    "  v_mov_b32_dpp v21, v26 row_ror:8 row_mask:0xf bank_mask:0x3\n"
    "  v_mov_b32_dpp v23, v27 row_ror:8 row_mask:0xf bank_mask:0x3\n"
    "  buffer_store_dwordx4 v[16:19], v14, s[16:19], s52 offen sc1\n"
    "  buffer_store_dwordx4 v[20:23], v14, s[16:19], s53 offen sc1\n"
    "  s_waitcnt lgkmcnt(0)\n"
    "  s_barrier\n"
    "  v_lshlrev_b32 v120, 4, v0\n"
    "  s_lshl_b32 s67, s2, 13\n"
    "  s_add_u32 s48, s67, 0xc00000\n"
    "  s_add_u32 s49, s48, 4096\n"
    "  v_mov_b32 v124, 0\n"
    "  v_mov_b32 v125, 0\n"
    "  v_mov_b32 v126, 0\n"
    "  v_mov_b32 v127, 0\n"
    "  v_and_b32 v122, 31, v1\n"
    "  v_lshrrev_b32 v121, 5, v1\n"
    "  v_mul_u32_u24 v121, 0x1200, v121\n"
    "  v_lshl_add_u32 v123, v122, 4, v121\n"
    "  s_cmp_eq_u32 s40, 0\n"
    "  s_cselect_b64 exec, -1, 0\n"
    "  ds_write_b128 v123, v[124:127]\n"
    "  s_mov_b64 exec, -1\n"
    "  s_lshr_b32 s67, s40, 1\n"
    "  s_mul_i32 s67, s67, 0x510\n"
    "  s_add_u32 s50, s67, 0x25ca0\n"
    "  s_add_u32 s51, s50, 256\n"
    "  s_sub_u32 s52, s50, 34048\n"
    "  s_and_b32 s67, s40, 1\n"
    "  s_lshl_b32 s67, s67, 6\n"
    "  v_add_lshl_u32 v13, s67, v1, 4\n"
    "  v_lshl_add_u32 v14, v13, 1, s50\n"
    "  v_cvt_f32_u32 v13, v13\n"
    "  s_and_b32 s67, s40, 1\n"
    "  s_lshl_b32 s67, s67, 6\n"
    "  v_add_u32 v12, s67, v1\n"
    "  s_lshr_b32 s67, s40, 1\n"
    "  s_mul_i32 s67, s67, 0x4100\n"
    "  s_add_u32 s67, s67, 0x10040\n"
    "  v_lshl_add_u32 v15, v12, 5, s67\n"
    "  v_mov_b32 v39, s52\n"
    "  v_mov_b32 v36, v13\n"
    "  v_add_f32 v37, 0x3f800000, v13\n"
    "  v_cvt_pk_bf16_f32 v38, v36, v37\n"
    "  v_add_u32 v42, 0, v14\n"
    "  v_mad_u32_u16 v40, v38, 2, v39 op_sel:[0,0,0,0]\n"
    "  v_add_u32 v43, 2, v14\n"
    "  v_mad_u32_u16 v41, v38, 2, v39 op_sel:[1,0,0,0]\n"
    "  v_med3_u32 v40, v40, v42, s51\n"
    "  v_med3_u32 v41, v41, v43, s51\n"
    "  ds_read_u16 v16, v40\n"
    "  ds_read_u16 v17, v41\n"
    "  v_add_f32 v36, 0x40000000, v13\n"
    "  v_add_f32 v37, 0x40400000, v13\n"
    "  v_cvt_pk_bf16_f32 v38, v36, v37\n"
    "  v_add_u32 v42, 4, v14\n"
    "  v_mad_u32_u16 v40, v38, 2, v39 op_sel:[0,0,0,0]\n"
    "  v_add_u32 v43, 6, v14\n"
    "  v_mad_u32_u16 v41, v38, 2, v39 op_sel:[1,0,0,0]\n"
    "  v_med3_u32 v40, v40, v42, s51\n"
    "  v_med3_u32 v41, v41, v43, s51\n"
    "  ds_read_u16 v18, v40\n"
    "  ds_read_u16 v19, v41\n"
    "  v_add_f32 v36, 0x40800000, v13\n"
    "  v_add_f32 v37, 0x40a00000, v13\n"
    "  v_cvt_pk_bf16_f32 v38, v36, v37\n"
    "  v_add_u32 v42, 8, v14\n"
    "  v_mad_u32_u16 v40, v38, 2, v39 op_sel:[0,0,0,0]\n"
    "  v_add_u32 v43, 10, v14\n"
    "  v_mad_u32_u16 v41, v38, 2, v39 op_sel:[1,0,0,0]\n"
    "  v_med3_u32 v40, v40, v42, s51\n"
    "  v_med3_u32 v41, v41, v43, s51\n"
    "  ds_read_u16 v20, v40\n"
    "  ds_read_u16 v21, v41\n"
    "  v_add_f32 v36, 0x40c00000, v13\n"
    "  v_add_f32 v37, 0x40e00000, v13\n"
    "  v_cvt_pk_bf16_f32 v38, v36, v37\n"
    "  v_add_u32 v42, 12, v14\n"
    "  v_mad_u32_u16 v40, v38, 2, v39 op_sel:[0,0,0,0]\n"
    "  v_add_u32 v43, 14, v14\n"
    "  v_mad_u32_u16 v41, v38, 2, v39 op_sel:[1,0,0,0]\n"
    "  v_med3_u32 v40, v40, v42, s51\n"
    "  v_med3_u32 v41, v41, v43, s51\n"
    "  ds_read_u16 v22, v40\n"
    "  ds_read_u16 v23, v41\n"
    "  v_add_f32 v36, 0x41000000, v13\n"
    "  v_add_f32 v37, 0x41100000, v13\n"
    "  v_cvt_pk_bf16_f32 v38, v36, v37\n"
    "  v_add_u32 v42, 16, v14\n"
    "  v_mad_u32_u16 v40, v38, 2, v39 op_sel:[0,0,0,0]\n"
    "  v_add_u32 v43, 18, v14\n"
    "  v_mad_u32_u16 v41, v38, 2, v39 op_sel:[1,0,0,0]\n"
    "  v_med3_u32 v40, v40, v42, s51\n"
    "  v_med3_u32 v41, v41, v43, s51\n"
    "  ds_read_u16 v24, v40\n"
    "  ds_read_u16 v25, v41\n"
    "  v_add_f32 v36, 0x41200000, v13\n"
    "  v_add_f32 v37, 0x41300000, v13\n"
    "  v_cvt_pk_bf16_f32 v38, v36, v37\n"
    "  v_add_u32 v42, 20, v14\n"
    "  v_mad_u32_u16 v40, v38, 2, v39 op_sel:[0,0,0,0]\n"
    "  v_add_u32 v43, 22, v14\n"
    "  v_mad_u32_u16 v41, v38, 2, v39 op_sel:[1,0,0,0]\n"
    "  v_med3_u32 v40, v40, v42, s51\n"
    "  v_med3_u32 v41, v41, v43, s51\n"
    "  ds_read_u16 v26, v40\n"
    "  ds_read_u16 v27, v41\n"
    "  v_add_f32 v36, 0x41400000, v13\n"
    "  v_add_f32 v37, 0x41500000, v13\n"
    "  v_cvt_pk_bf16_f32 v38, v36, v37\n"
    "  v_add_u32 v42, 24, v14\n"
    "  v_mad_u32_u16 v40, v38, 2, v39 op_sel:[0,0,0,0]\n"
    "  v_add_u32 v43, 26, v14\n"
    "  v_mad_u32_u16 v41, v38, 2, v39 op_sel:[1,0,0,0]\n"
    "  v_med3_u32 v40, v40, v42, s51\n"
    "  v_med3_u32 v41, v41, v43, s51\n"
    "  ds_read_u16 v28, v40\n"
    "  ds_read_u16 v29, v41\n"
    "  v_add_f32 v36, 0x41600000, v13\n"
    "  v_add_f32 v37, 0x41700000, v13\n"
    "  v_cvt_pk_bf16_f32 v38, v36, v37\n"
    "  v_add_u32 v42, 28, v14\n"
    "  v_mad_u32_u16 v40, v38, 2, v39 op_sel:[0,0,0,0]\n"
    "  v_add_u32 v43, 30, v14\n"
    "  v_mad_u32_u16 v41, v38, 2, v39 op_sel:[1,0,0,0]\n"
    "  v_med3_u32 v40, v40, v42, s51\n"
    "  v_med3_u32 v41, v41, v43, s51\n"
    "  ds_read_u16 v30, v40\n"
    "  ds_read_u16 v31, v41\n"
    "  v_add_f32 v36, 0x41800000, v13\n"
    "  v_add_f32 v37, 0x41880000, v13\n"
    "  v_cvt_pk_bf16_f32 v38, v36, v37\n"
    "  v_add_u32 v42, 32, v14\n"
    "  v_mad_u32_u16 v40, v38, 2, v39 op_sel:[0,0,0,0]\n"
    "  v_add_u32 v43, 34, v14\n"
    "  v_mad_u32_u16 v41, v38, 2, v39 op_sel:[1,0,0,0]\n"
    "  v_med3_u32 v40, v40, v42, s51\n"
    "  v_med3_u32 v41, v41, v43, s51\n"
    "  ds_read_u16 v32, v40\n"
    "  ds_read_u16 v33, v41\n"
    "  v_add_f32 v36, 0x41900000, v13\n"
    "  v_cvt_pk_bf16_f32 v38, v36, v36\n"
    "  v_add_u32 v42, 36, v14\n"
    "  v_mad_u32_u16 v40, v38, 2, v39 op_sel:[0,0,0,0]\n"
    "  v_med3_u32 v40, v40, v42, s51\n"
    "  ds_read_u16 v34, v40\n"
    "  s_cmp_lg_u32 s40, 0\n"
    "  s_cbranch_scc1 Lnr_41%=\n"
    "  s_waitcnt vmcnt(2)\n"
    "  s_mov_b32 s55, 0x00008000\n"
    "Lc_42%=:\n"
    "  v_xor_b32 v84, s46, v84\n"
    "  v_xor_b32 v85, s46, v85\n"
    "  v_xor_b32 v86, s46, v86\n"
    "  v_xor_b32 v87, s46, v87\n"
    "  v_or3_b32 v88, v84, v85, v86\n"
    "  v_or_b32 v88, v88, v87\n"
    "  v_cmp_ne_u32 vcc, 0, v88\n"
    "  s_cbranch_vccz Lo_43%=\n"
    "  s_sub_u32 s55, s55, 1\n"
    "  s_cbranch_scc1 Lo_43%=\n"
    "  s_sleep 2\n"
    "  buffer_load_dwordx4 v[84:87], v2, s[16:19], s54 offen sc1\n"
    "  s_waitcnt vmcnt(0)\n"
    "  s_branch Lc_42%=\n"
    "Lo_43%=:\n"
    "Lnr_41%=:\n"
    "  s_barrier\n"
    "  buffer_load_dwordx4 v[112:115], v120, s[16:19], s48 offen sc1\n"
    "  buffer_load_dwordx4 v[116:119], v120, s[16:19], s49 offen sc1\n"
    "  v_mov_b32 v44, 0\n"
    "  v_mov_b32 v45, 0\n"
    "  v_mov_b32 v46, 0\n"
    "  v_mov_b32 v47, 0\n"
    "  v_bfe_u32 v50, v1, 2, 2\n"
    "  v_mul_u32_u24 v50, 0x1040, v50\n"
    "  v_and_b32 v49, 3, v1\n"
    "  v_lshlrev_b32 v49, 4, v49\n"
    "  s_lshr_b32 s67, s40, 1\n"
    "  s_mul_i32 s67, s67, 0x4100\n"
    "  s_add_u32 s67, s67, 0x10000\n"
    "  v_add3_u32 v48, v50, v49, s67\n"
    "  s_bitcmp0_b32 s40, 0\n"
    "  s_cselect_b64 exec, 0xffff, 0\n"
    "  ds_write_b128 v48, v[44:47]\n"
    "  s_mov_b64 exec, -1\n"
    "  v_lshl_or_b32 v52, v17, 16, v16\n"
    "  v_lshl_or_b32 v53, v19, 16, v18\n"
    "  s_waitcnt lgkmcnt(6)\n"
    "  v_lshl_or_b32 v54, v21, 16, v20\n"
    "  v_lshl_or_b32 v55, v23, 16, v22\n"
    "  v_lshl_or_b32 v56, v25, 16, v24\n"
    "  v_lshl_or_b32 v57, v27, 16, v26\n"
    "  v_lshl_or_b32 v58, v29, 16, v28\n"
    "  s_waitcnt lgkmcnt(4)\n"
    "  v_lshl_or_b32 v59, v31, 16, v30\n"
    "  ds_write_b128 v15, v[52:55]\n"
    "  ds_write_b128 v15, v[56:59] offset:16\n"
    "  v_lshl_or_b32 v60, v18, 16, v17\n"
    "  v_lshl_or_b32 v61, v20, 16, v19\n"
    "  v_lshl_or_b32 v62, v22, 16, v21\n"
    "  v_lshl_or_b32 v63, v24, 16, v23\n"
    "  v_lshl_or_b32 v64, v26, 16, v25\n"
    "  v_lshl_or_b32 v65, v28, 16, v27\n"
    "  v_lshl_or_b32 v66, v30, 16, v29\n"
    "  s_waitcnt lgkmcnt(5)\n"
    "  v_lshl_or_b32 v67, v32, 16, v31\n"
    "  ds_write_b128 v15, v[60:63] offset:4160\n"
    "  ds_write_b128 v15, v[64:67] offset:4176\n"
    "  v_lshl_or_b32 v68, v19, 16, v18\n"
    "  v_lshl_or_b32 v69, v21, 16, v20\n"
    "  v_lshl_or_b32 v70, v23, 16, v22\n"
    "  v_lshl_or_b32 v71, v25, 16, v24\n"
    "  v_lshl_or_b32 v72, v27, 16, v26\n"
    "  v_lshl_or_b32 v73, v29, 16, v28\n"
    "  v_lshl_or_b32 v74, v31, 16, v30\n"
    "  s_waitcnt lgkmcnt(6)\n"
    "  v_lshl_or_b32 v75, v33, 16, v32\n"
    "  ds_write_b128 v15, v[68:71] offset:8320\n"
    "  ds_write_b128 v15, v[72:75] offset:8336\n"
    "  v_lshl_or_b32 v76, v20, 16, v19\n"
    "  v_lshl_or_b32 v77, v22, 16, v21\n"
    "  v_lshl_or_b32 v78, v24, 16, v23\n"
    "  v_lshl_or_b32 v79, v26, 16, v25\n"
    "  v_lshl_or_b32 v80, v28, 16, v27\n"
    "  v_lshl_or_b32 v81, v30, 16, v29\n"
    "  v_lshl_or_b32 v82, v32, 16, v31\n"
    "  s_waitcnt lgkmcnt(7)\n"
    "  v_lshl_or_b32 v83, v34, 16, v33\n"
    "  ds_write_b128 v15, v[76:79] offset:12480\n"
    "  ds_write_b128 v15, v[80:83] offset:12496\n"
    "  v_lshlrev_b32 v44, 16, v16\n"
    "  v_subrev_u32 v48, 8, v15\n"
    "  s_bitcmp0_b32 s40, 0\n"
    "  s_cselect_b64 exec, 1, 0\n"
    "  ds_write_b32 v48, v44 offset:4164\n"
    "  ds_write_b32 v48, v52 offset:8324\n"
    "  ds_write_b32 v48, v44 offset:12480\n"
    "  ds_write_b32 v48, v60 offset:12484\n"
    "  s_mov_b64 exec, -1\n"
    "  v_and_b32 v51, 15, v1\n"
    "  v_lshrrev_b32 v50, 4, v1\n"
    "  v_lshlrev_b32 v50, 4, v50\n"
    "  v_lshl_add_u32 v14, v51, 6, v50\n"
    "  s_lshl_b32 s67, s2, 13\n"
    "  s_add_u32 s50, s67, 0x800000\n"
    "  s_and_b32 s67, s40, 1\n"
    "  s_lshl_b32 s67, s67, 1\n"
    "  s_sub_u32 s67, 7, s67\n"
    "  s_lshl_b32 s67, s67, 10\n"
    "  s_add_u32 s34, s67, s50\n"
    "  s_and_b32 s67, s40, 1\n"
    "  s_lshl_b32 s67, s67, 1\n"
    "  s_sub_u32 s67, 6, s67\n"
    "  s_lshl_b32 s67, s67, 10\n"
    "  s_add_u32 s35, s67, s50\n"
    "  s_and_b32 s67, s40, 1\n"
    "  s_lshl1_add_u32 s67, s67, 1\n"
    "  s_lshl_b32 s67, s67, 10\n"
    "  s_add_u32 s36, s67, s50\n"
    "  s_and_b32 s67, s40, 1\n"
    "  s_lshl_b32 s67, s67, 1\n"
    "  s_lshl_b32 s67, s67, 10\n"
    "  s_add_u32 s37, s67, s50\n"
    "  v_and_b32 v51, 3, v1\n"
    "  v_bfe_u32 v50, v1, 2, 2\n"
    "  v_sub_u32 v49, 4, v50\n"
    "  v_and_b32 v49, 3, v49\n"
    "  v_mul_u32_u24 v48, 0x1040, v49\n"
    "  s_lshr_b32 s67, s40, 1\n"
    "  s_mul_i32 s67, s67, 0x4100\n"
    "  s_and_b32 s66, s40, 1\n"
    "  s_lshl_b32 s66, s66, 8\n"
    "  s_add_u32 s66, s67, s66\n"
    "  s_add_u32 s66, s66, 0x10058\n"
    "  v_add_u32 v48, s66, v48\n"
    "  v_lshl_add_u32 v51, v51, 3, v48\n"
    "  v_lshrrev_b32 v48, 4, v1\n"
    "  v_add_lshl_u32 v50, v50, v49, 1\n"
    "  v_lshl_add_u32 v50, v48, 4, v50\n"
    "  v_sub_u32 v10, v51, v50\n"
    "  v_and_b32 v51, 15, v1\n"
    "  v_lshlrev_b32 v51, 5, v51\n"
    "  v_lshrrev_b32 v50, 4, v1\n"
    "  v_lshlrev_b32 v50, 4, v50\n"
    "  s_lshr_b32 s67, s40, 1\n"
    "  s_mul_i32 s67, s67, 0x1200\n"
    "  s_and_b32 s66, s40, 1\n"
    "  s_sub_u32 s66, 1, s66\n"
    "  s_lshl_b32 s66, s66, 8\n"
    "  s_add_u32 s67, s67, s66\n"
    "  s_add_u32 s67, s67, 32\n"
    "  v_add3_u32 v11, v51, v50, s67\n"
    "  v_and_b32 v51, 15, v1\n"
    "  v_lshlrev_b32 v51, 5, v51\n"
    "  v_lshrrev_b32 v50, 4, v1\n"
    "  v_lshlrev_b32 v50, 3, v50\n"
    "  s_lshr_b32 s67, s40, 1\n"
    "  s_mul_i32 s67, s67, 0x1200\n"
    "  s_add_u32 s67, s67, 0x200\n"
    "  v_add3_u32 v12, v51, v50, s67\n"
    "  v_lshrrev_b32 v51, 4, v1\n"
    "  v_lshrrev_b32 v50, 1, v51\n"
    "  v_and_b32 v49, 15, v1\n"
    "  v_and_b32 v48, 3, v49\n"
    "  v_and_b32 v51, 1, v51\n"
    "  v_sub_u32 v51, 1, v51\n"
    "  v_lshrrev_b32 v49, 2, v49\n"
    "  v_lshlrev_b32 v49, 16, v49\n"
    "  v_lshl_add_u32 v49, v51, 18, v49\n"
    "  v_lshl_add_u32 v49, v48, 5, v49\n"
    "  v_lshl_add_u32 v13, v50, 4, v49\n"
    "  s_lshr_b32 s67, s40, 1\n"
    "  s_lshl_b32 s66, s67, 17\n"
    "  s_lshl_b32 s67, s67, 25\n"
    "  s_add_u32 s67, s66, s67\n"
    "  s_add_u32 s54, s67, 0x5040c0c\n"
    "  s_and_b32 s67, s40, 1\n"
    "  s_sub_u32 s67, 3, s67\n"
    "  s_lshl_b32 s67, s67, 19\n"
    "  s_lshr_b32 s66, s40, 1\n"
    "  s_lshl1_add_u32 s66, s2, s66\n"
    "  s_lshl_b32 s66, s66, 7\n"
    "  s_add_u32 s66, s67, s66\n"
    "  s_add_u32 s32, s66, 0x1000000\n"
    "  s_and_b32 s67, s40, 1\n"
    "  s_lshl_b32 s67, s67, 19\n"
    "  s_lshr_b32 s66, s40, 1\n"
    "  s_lshl1_add_u32 s66, s2, s66\n"
    "  s_lshl_b32 s66, s66, 7\n"
    "  s_add_u32 s67, s67, s66\n"
    "  s_add_u32 s33, s67, 0x1000000\n"
    "  s_mov_b32 s67, 0x200\n"
    "  v_lshl_add_u32 v15, v0, 3, s67\n"
    "  s_mov_b32 s52, 0x05040100\n"
    "  s_mov_b32 s53, 0x07060302\n"
    "  s_waitcnt vmcnt(0)\n"
    "  s_mov_b32 s55, 0x00008000\n"
    "Lc_44%=:\n"
    "  v_max_u32 v16, v112, v116\n"
    "  v_cmp_eq_u32 vcc, -1, v16\n"
    "  s_cbranch_vccz Lo_45%=\n"
    "  s_sub_u32 s55, s55, 1\n"
    "  s_cbranch_scc1 Lo_45%=\n"
    "  s_sleep 2\n"
    "  s_mov_b64 exec, vcc\n"
    "  buffer_load_dwordx4 v[112:115], v120, s[16:19], s48 offen sc1\n"
    "  buffer_load_dwordx4 v[116:119], v120, s[16:19], s49 offen sc1\n"
    "  s_mov_b64 exec, -1\n"
    "  s_waitcnt vmcnt(0)\n"
    "  s_branch Lc_44%=\n"
    "Lo_45%=:\n"
    "  v_perm_b32 v52, v113, v112, s52\n"
    "  v_perm_b32 v53, v115, v114, s52\n"
    "  v_perm_b32 v54, v113, v112, s53\n"
    "  v_perm_b32 v55, v115, v114, s53\n"
    "  ds_write_b64 v15, v[52:53]\n"
    "  ds_write_b64 v15, v[54:55] offset:4608\n"
    "  v_perm_b32 v56, v117, v116, s52\n"
    "  v_perm_b32 v57, v119, v118, s52\n"
    "  v_perm_b32 v58, v117, v116, s53\n"
    "  v_perm_b32 v59, v119, v118, s53\n"
    "  ds_write_b64 v15, v[56:57] offset:2048\n"
    "  ds_write_b64 v15, v[58:59] offset:6656\n"
    "  s_waitcnt lgkmcnt(0)\n"
    "  s_barrier\n"
    "  ds_read_b64_tr_b16 v[88:89], v10 offset:3592\n"
    "  ds_read_b64_tr_b16 v[90:91], v10 offset:3584\n"
    "  ds_read_b128 v[60:63], v11 offset:192\n"
    "  ds_read_b64_tr_b16 v[92:93], v10 offset:3080\n"
    "  ds_read_b64_tr_b16 v[94:95], v10 offset:3072\n"
    "  ds_read_b128 v[64:67], v11 offset:704\n"
    "  ds_read_b64_tr_b16 v[96:97], v10 offset:2568\n"
    "  ds_read_b64_tr_b16 v[98:99], v10 offset:2560\n"
    "  ds_read_b128 v[68:71], v11 offset:1216\n"
    "  ds_read_b64_tr_b16 v[100:101], v10 offset:2056\n"
    "  ds_read_b64_tr_b16 v[102:103], v10 offset:2048\n"
    "  ds_read_b128 v[72:75], v11 offset:1728\n"
    "  s_bitcmp1_b32 s40, 0\n"
    "  s_cbranch_scc1 Lb_46%=\n"
    "  s_waitcnt lgkmcnt(6)\n"
    "  v_mfma_f32_16x16x32_bf16 v[16:19], v[88:91], v[60:63], 0\n"
    "  ds_read_b64_tr_b16 v[48:49], v10 offset:1544\n"
    "  v_mfma_f32_16x16x32_bf16 v[20:23], v[92:95], v[60:63], 0\n"
    "  buffer_load_dwordx4 v[32:35], v14, s[16:19], s34 offen sc1\n"
    "  v_mfma_f32_16x16x32_bf16 v[16:19], v[92:95], v[64:67], v[16:19]\n"
    "  ds_read_b64_tr_b16 v[50:51], v10 offset:1536\n"
    "  s_waitcnt lgkmcnt(6)\n"
    "  v_mfma_f32_16x16x32_bf16 v[104:107], v[96:99], v[60:63], 0\n"
    "  ds_read_b128 v[52:55], v11 offset:2240\n"
    "  v_mfma_f32_16x16x32_bf16 v[20:23], v[96:99], v[64:67], v[20:23]\n"
    "  buffer_load_dwordx4 v[36:39], v14, s[16:19], s35 offen sc1\n"
    "  s_waitcnt lgkmcnt(6)\n"
    "  v_mfma_f32_16x16x32_bf16 v[16:19], v[96:99], v[68:71], v[16:19]\n"
    "  ds_read_b64_tr_b16 v[56:57], v10 offset:1032\n"
    "  s_waitcnt lgkmcnt(5)\n"
    "  v_mfma_f32_16x16x32_bf16 v[108:111], v[100:103], v[60:63], 0\n"
    "  ds_read_b64_tr_b16 v[58:59], v10 offset:1024\n"
    "  v_mfma_f32_16x16x32_bf16 v[104:107], v[100:103], v[64:67], v[104:107]\n"
    "  buffer_load_dwordx4 v[40:43], v14, s[16:19], s36 offen sc1\n"
    "  v_mfma_f32_16x16x32_bf16 v[20:23], v[100:103], v[68:71], v[20:23]\n"
    "  ds_read_b128 v[76:79], v11 offset:2752\n"
    "  s_waitcnt lgkmcnt(6)\n"
    "  v_mfma_f32_16x16x32_bf16 v[16:19], v[100:103], v[72:75], v[16:19]\n"
    "  ds_read_b64_tr_b16 v[80:81], v10 offset:520\n"
    "  s_waitcnt lgkmcnt(5)\n"
    "  v_mfma_f32_16x16x32_bf16 v[112:115], v[48:51], v[60:63], 0\n"
    "  buffer_load_dwordx4 v[44:47], v14, s[16:19], s37 offen sc1\n"
    "  v_mfma_f32_16x16x32_bf16 v[108:111], v[48:51], v[64:67], v[108:111]\n"
    "  ds_read_b64_tr_b16 v[82:83], v10 offset:512\n"
    "  s_lshl_b32 s67, s40, 12\n"
    "  v_mfma_f32_16x16x32_bf16 v[104:107], v[48:51], v[68:71], v[104:107]\n"
    "  ds_read_b128 v[84:87], v11 offset:3264\n"
    "  s_add_u32 s67, s67, 0x4000\n"
    "  v_mfma_f32_16x16x32_bf16 v[20:23], v[48:51], v[72:75], v[20:23]\n"
    "  ds_read_b64_tr_b16 v[88:89], v10 offset:8\n"
    "  v_add_u32 v14, s67, v2\n"
    "  s_waitcnt lgkmcnt(7)\n"
    "  v_mfma_f32_16x16x32_bf16 v[16:19], v[48:51], v[52:55], v[16:19]\n"
    "  ds_read_b64_tr_b16 v[90:91], v10\n"
    "  s_waitcnt lgkmcnt(6)\n"
    "  v_mfma_f32_16x16x32_bf16 v[116:119], v[56:59], v[60:63], 0\n"
    "  ds_read_b128 v[92:95], v11 offset:3776\n"
    "  v_mfma_f32_16x16x32_bf16 v[112:115], v[56:59], v[64:67], v[112:115]\n"
    "  ds_read_b64_tr_b16 v[96:97], v10 offset:3656\n"
    "  v_mov_b32 v120, -1\n"
    "  v_mfma_f32_16x16x32_bf16 v[108:111], v[56:59], v[68:71], v[108:111]\n"
    "  ds_read_b64_tr_b16 v[98:99], v10 offset:3648\n"
    "  v_mov_b32 v121, -1\n"
    "  v_mfma_f32_16x16x32_bf16 v[104:107], v[56:59], v[72:75], v[104:107]\n"
    "  ds_read_b128 v[100:103], v11 offset:128\n"
    "  v_mov_b32 v122, -1\n"
    "  v_mfma_f32_16x16x32_bf16 v[20:23], v[56:59], v[52:55], v[20:23]\n"
    "  ds_read_b64_tr_b16 v[48:49], v10 offset:3144\n"
    "  v_mov_b32 v123, -1\n"
    "  s_waitcnt lgkmcnt(8)\n"
    "  v_mfma_f32_16x16x32_bf16 v[16:19], v[56:59], v[76:79], v[16:19]\n"
    "  ds_read_b64_tr_b16 v[50:51], v10 offset:3136\n"
    "  s_lshl_b32 s67, s47, 8\n"
    "  v_mfma_f32_16x16x32_bf16 v[24:27], v[80:83], v[60:63], 0\n"
    "  ds_read_b128 v[56:59], v11 offset:640\n"
    "  s_add_u32 s67, s67, s41\n"
    "  v_mfma_f32_16x16x32_bf16 v[116:119], v[80:83], v[64:67], v[116:119]\n"
    "  s_lshl_b32 s67, s67, 7\n"
    "  s_add_u32 s38, s67, 0x300000\n"
    "  v_mfma_f32_16x16x32_bf16 v[112:115], v[80:83], v[68:71], v[112:115]\n"
    "  buffer_store_dwordx4 v[120:123], v8, s[16:19], s38 offen sc1\n"
    "  s_lshl_b32 s67, s47, 5\n"
    "  v_mfma_f32_16x16x32_bf16 v[108:111], v[80:83], v[72:75], v[108:111]\n"
    "  s_add_u32 s67, s67, s41\n"
    "  s_lshl_b32 s67, s67, 6\n"
    "  v_mfma_f32_16x16x32_bf16 v[104:107], v[80:83], v[52:55], v[104:107]\n"
    "  s_add_u32 s39, s67, 0x30000\n"
    "  s_mov_b64 exec, 0xffff\n"
    "  buffer_store_dword v120, v9, s[16:19], s39 offen sc1\n"
    "  s_mov_b64 exec, -1\n"
    "  v_mfma_f32_16x16x32_bf16 v[20:23], v[80:83], v[76:79], v[20:23]\n"
    "  s_and_b32 s67, s40, 1\n"
    "  s_lshl1_add_u32 s67, s41, s67\n"
    "  s_waitcnt lgkmcnt(7)\n"
    "  v_mfma_f32_16x16x32_bf16 v[16:19], v[80:83], v[84:87], v[16:19]\n"
    "  ds_read_b64_tr_b16 v[80:81], v10 offset:2632\n"
    "  s_lshl_b32 s67, s67, 5\n"
    "  v_mfma_f32_16x16x32_bf16 v[28:31], v[88:91], v[60:63], 0\n"
    "  ds_read_b64_tr_b16 v[82:83], v10 offset:2624\n"
    "  s_lshl2_add_u32 s67, s42, s67\n"
    "  v_mfma_f32_16x16x32_bf16 v[24:27], v[88:91], v[64:67], v[24:27]\n"
    "  ds_read_b128 v[60:63], v11 offset:1152\n"
    "  s_and_b32 s66, s40, 2\n"
    "  v_mfma_f32_16x16x32_bf16 v[116:119], v[88:91], v[68:71], v[116:119]\n"
    "  ds_read_b64_tr_b16 v[64:65], v10 offset:2120\n"
    "  s_add_u32 s67, s67, s66\n"
    "  v_mfma_f32_16x16x32_bf16 v[112:115], v[88:91], v[72:75], v[112:115]\n"
    "  ds_read_b64_tr_b16 v[66:67], v10 offset:2112\n"
    "  s_lshl_b32 s67, s67, 10\n"
    "  v_mfma_f32_16x16x32_bf16 v[108:111], v[88:91], v[52:55], v[108:111]\n"
    "  ds_read_b128 v[68:71], v11 offset:1664\n"
    "  s_add_u32 s38, s67, 0x600000\n"
    "  v_mfma_f32_16x16x32_bf16 v[104:107], v[88:91], v[76:79], v[104:107]\n"
    "  ds_read_b64_tr_b16 v[72:73], v10 offset:1608\n"
    "  buffer_store_dwordx4 v[120:123], v2, s[16:19], s38 offen sc1\n"
    "  v_mfma_f32_16x16x32_bf16 v[20:23], v[88:91], v[84:87], v[20:23]\n"
    "  ds_read_b64_tr_b16 v[74:75], v10 offset:1600\n"
    "  buffer_store_dwordx4 v[120:123], v2, s[16:19], s38 offen offset:1024 sc1\n"
    "  s_waitcnt lgkmcnt(9)\n"
    "  v_mfma_f32_16x16x32_bf16 v[16:19], v[88:91], v[92:95], v[16:19]\n"
    "  ds_read_b128 v[52:55], v11 offset:2176\n"
    "  s_lshl_b32 s67, s2, 7\n"
    "  v_mfma_f32_16x16x32_bf16 v[16:19], v[96:99], v[100:103], v[16:19]\n"
    "  ds_read_b64_tr_b16 v[76:77], v10 offset:1096\n"
    "  s_add_u32 s38, s67, 0x1c0000\n"
    "  v_mfma_f32_16x16x32_bf16 v[20:23], v[48:51], v[100:103], v[20:23]\n"
    "  ds_read_b64_tr_b16 v[78:79], v10 offset:1088\n"
    "  v_lshrrev_b32 v126, 3, v1\n"
    "  s_waitcnt lgkmcnt(9)\n"
    "  v_mfma_f32_16x16x32_bf16 v[16:19], v[48:51], v[56:59], v[16:19]\n"
    "  ds_read_b128 v[84:87], v11 offset:2688\n"
    "  v_and_b32 v125, 7, v1\n"
    "  v_mfma_f32_16x16x32_bf16 v[104:107], v[80:83], v[100:103], v[104:107]\n"
    "  ds_read_b64_tr_b16 v[88:89], v10 offset:584\n"
    "  v_lshlrev_b32 v125, 3, v125\n"
    "  v_mfma_f32_16x16x32_bf16 v[20:23], v[80:83], v[56:59], v[20:23]\n"
    "  ds_read_b64_tr_b16 v[90:91], v10 offset:576\n"
    "  v_lshl_add_u32 v124, v126, 15, v125\n"
    "  s_waitcnt lgkmcnt(9)\n"
    "  v_mfma_f32_16x16x32_bf16 v[16:19], v[80:83], v[60:63], v[16:19]\n"
    "  s_mov_b32 s60, 0x03030303\n"
    "  s_mov_b32 s61, 0x03030303\n"
    "  v_mfma_f32_16x16x32_bf16 v[108:111], v[64:67], v[100:103], v[108:111]\n"
    "  s_cmp_eq_u32 s40, 0\n"
    "  s_cselect_b64 exec, -1, 0\n"
    "  buffer_store_dwordx2 v[120:121], v124, s[16:19], s38 offen sc1\n"
    "  s_cselect_b64 exec, s[60:61], 0\n"
    "  buffer_store_dwordx2 v[120:121], v124, s[16:19], s38 offen offset:64 sc1\n"
    "  s_mov_b64 exec, -1\n"
    "  s_lshl_b32 s67, s44, 2\n"
    "  v_mfma_f32_16x16x32_bf16 v[104:107], v[64:67], v[56:59], v[104:107]\n"
    "  ds_read_b128 v[92:95], v11 offset:3200\n"
    "  s_lshl4_add_u32 s67, s43, s67\n"
    "  v_mfma_f32_16x16x32_bf16 v[20:23], v[64:67], v[60:63], v[20:23]\n"
    "  ds_read_b64_tr_b16 v[96:97], v10 offset:72\n"
    "  s_add_u32 s67, s67, s40\n"
    "  s_waitcnt lgkmcnt(8)\n"
    "  v_mfma_f32_16x16x32_bf16 v[16:19], v[64:67], v[68:71], v[16:19]\n"
    "  ds_read_b64_tr_b16 v[98:99], v10 offset:64\n"
    "  s_lshl_b32 s67, s67, 8\n"
    "  v_mfma_f32_16x16x32_bf16 v[112:115], v[72:75], v[100:103], v[112:115]\n"
    "  s_add_u32 s38, s67, 0x80000\n"
    "  buffer_store_dword v120, v9, s[16:19], s38 offen sc1\n"
    "  v_mfma_f32_16x16x32_bf16 v[108:111], v[72:75], v[56:59], v[108:111]\n"
    "  v_mfma_f32_16x16x32_bf16 v[104:107], v[72:75], v[60:63], v[104:107]\n"
    "  v_mfma_f32_16x16x32_bf16 v[20:23], v[72:75], v[68:71], v[20:23]\n"
    "  s_waitcnt lgkmcnt(6)\n"
    "  v_mfma_f32_16x16x32_bf16 v[16:19], v[72:75], v[52:55], v[16:19]\n"
    "  v_mfma_f32_16x16x32_bf16 v[116:119], v[76:79], v[100:103], v[116:119]\n"
    "  ds_read_b128 v[48:51], v11 offset:3712\n"
    "  v_mfma_f32_16x16x32_bf16 v[112:115], v[76:79], v[56:59], v[112:115]\n"
    "  ds_read_b64_tr_b16 v[80:81], v10 offset:3720\n"
    "  v_mfma_f32_16x16x32_bf16 v[108:111], v[76:79], v[60:63], v[108:111]\n"
    "  ds_read_b64_tr_b16 v[82:83], v10 offset:3712\n"
    "  v_mfma_f32_16x16x32_bf16 v[104:107], v[76:79], v[68:71], v[104:107]\n"
    "  ds_read_b128 v[64:67], v11 offset:64\n"
    "  v_mfma_f32_16x16x32_bf16 v[20:23], v[76:79], v[52:55], v[20:23]\n"
    "  ds_read_b64_tr_b16 v[72:73], v10 offset:3208\n"
    "  s_waitcnt lgkmcnt(8)\n"
    "  v_mfma_f32_16x16x32_bf16 v[16:19], v[76:79], v[84:87], v[16:19]\n"
    "  ds_read_b64_tr_b16 v[74:75], v10 offset:3200\n"
    "  v_mfma_f32_16x16x32_bf16 v[24:27], v[88:91], v[100:103], v[24:27]\n"
    "  ds_read_b128 v[76:79], v11 offset:576\n"
    "  v_mfma_f32_16x16x32_bf16 v[116:119], v[88:91], v[56:59], v[116:119]\n"
    "  v_mfma_f32_16x16x32_bf16 v[112:115], v[88:91], v[60:63], v[112:115]\n"
    "  v_mfma_f32_16x16x32_bf16 v[108:111], v[88:91], v[68:71], v[108:111]\n"
    "  v_mfma_f32_16x16x32_bf16 v[104:107], v[88:91], v[52:55], v[104:107]\n"
    "  v_mfma_f32_16x16x32_bf16 v[20:23], v[88:91], v[84:87], v[20:23]\n"
    "  s_waitcnt lgkmcnt(7)\n"
    "  v_mfma_f32_16x16x32_bf16 v[16:19], v[88:91], v[92:95], v[16:19]\n"
    "  ds_read_b64_tr_b16 v[88:89], v10 offset:2696\n"
    "  v_mfma_f32_16x16x32_bf16 v[28:31], v[96:99], v[100:103], v[28:31]\n"
    "  ds_read_b64_tr_b16 v[90:91], v10 offset:2688\n"
    "  v_mfma_f32_16x16x32_bf16 v[24:27], v[96:99], v[56:59], v[24:27]\n"
    "  ds_read_b128 v[100:103], v11 offset:1088\n"
    "  v_mfma_f32_16x16x32_bf16 v[116:119], v[96:99], v[60:63], v[116:119]\n"
    "  ds_read_b64_tr_b16 v[56:57], v10 offset:2184\n"
    "  v_mfma_f32_16x16x32_bf16 v[112:115], v[96:99], v[68:71], v[112:115]\n"
    "  ds_read_b64_tr_b16 v[58:59], v10 offset:2176\n"
    "  v_mfma_f32_16x16x32_bf16 v[108:111], v[96:99], v[52:55], v[108:111]\n"
    "  ds_read_b128 v[60:63], v11 offset:1600\n"
    "  v_mfma_f32_16x16x32_bf16 v[104:107], v[96:99], v[84:87], v[104:107]\n"
    "  ds_read_b64_tr_b16 v[68:69], v10 offset:1672\n"
    "  v_mfma_f32_16x16x32_bf16 v[20:23], v[96:99], v[92:95], v[20:23]\n"
    "  ds_read_b64_tr_b16 v[70:71], v10 offset:1664\n"
    "  s_waitcnt lgkmcnt(8)\n"
    "  v_mfma_f32_16x16x32_bf16 v[16:19], v[96:99], v[48:51], v[16:19]\n"
    "  ds_read_b128 v[52:55], v11 offset:2112\n"
    "  v_mfma_f32_16x16x32_bf16 v[16:19], v[80:83], v[64:67], v[16:19]\n"
    "  ds_read_b64_tr_b16 v[84:85], v10 offset:1160\n"
    "  v_mfma_f32_16x16x32_bf16 v[20:23], v[72:75], v[64:67], v[20:23]\n"
    "  ds_read_b64_tr_b16 v[86:87], v10 offset:1152\n"
    "  v_mfma_f32_16x16x32_bf16 v[16:19], v[72:75], v[76:79], v[16:19]\n"
    "  ds_read_b128 v[92:95], v11 offset:2624\n"
    "  s_waitcnt lgkmcnt(7)\n"
    "  v_mfma_f32_16x16x32_bf16 v[104:107], v[88:91], v[64:67], v[104:107]\n"
    "  ds_read_b64_tr_b16 v[96:97], v10 offset:648\n"
    "  v_mfma_f32_16x16x32_bf16 v[20:23], v[88:91], v[76:79], v[20:23]\n"
    "  ds_read_b64_tr_b16 v[98:99], v10 offset:640\n"
    "  v_mfma_f32_16x16x32_bf16 v[16:19], v[88:91], v[100:103], v[16:19]\n"
    "  ds_read_b64 v[124:125], v12 offset:512\n"
    "  v_mfma_f32_16x16x32_bf16 v[108:111], v[56:59], v[64:67], v[108:111]\n"
    "  ds_read_b64 v[126:127], v12\n"
    "  v_mfma_f32_16x16x32_bf16 v[104:107], v[56:59], v[76:79], v[104:107]\n"
    "  ds_read_b128 v[48:51], v11 offset:3136\n"
    "  v_mfma_f32_16x16x32_bf16 v[20:23], v[56:59], v[100:103], v[20:23]\n"
    "  ds_read_b64_tr_b16 v[80:81], v10 offset:136\n"
    "  s_waitcnt lgkmcnt(10)\n"
    "  v_mfma_f32_16x16x32_bf16 v[16:19], v[56:59], v[60:63], v[16:19]\n"
    "  ds_read_b64_tr_b16 v[82:83], v10 offset:128\n"
    "  v_mfma_f32_16x16x32_bf16 v[112:115], v[68:71], v[64:67], v[112:115]\n"
    "  ds_read_b64 v[120:121], v12 offset:3584\n"
    "  v_mfma_f32_16x16x32_bf16 v[108:111], v[68:71], v[76:79], v[108:111]\n"
    "  ds_read_b64 v[122:123], v12 offset:3072\n"
    "  s_waitcnt vmcnt(8)\n"
    "  v_perm_b32 v40, v40, v40, s54\n"
    "  v_mfma_f32_16x16x32_bf16 v[104:107], v[68:71], v[100:103], v[104:107]\n"
    "  v_perm_b32 v41, v41, v41, s54\n"
    "  v_perm_b32 v42, v42, v42, s54\n"
    "  v_mfma_f32_16x16x32_bf16 v[20:23], v[68:71], v[60:63], v[20:23]\n"
    "  v_perm_b32 v43, v43, v43, s54\n"
    "  s_waitcnt lgkmcnt(6)\n"
    "  v_lshlrev_b32 v15, 16, v124\n"
    "  v_mfma_f32_16x16x32_bf16 v[16:19], v[68:71], v[52:55], v[16:19]\n"
    "  v_fmac_f32 v40, v15, v3\n"
    "  v_and_b32 v15, 0xffff0000, v124\n"
    "  v_mfma_f32_16x16x32_bf16 v[116:119], v[84:87], v[64:67], v[116:119]\n"
    "  ds_read_b128 v[72:75], v11 offset:3648\n"
    "  v_fmac_f32 v41, v15, v3\n"
    "  v_mfma_f32_16x16x32_bf16 v[112:115], v[84:87], v[76:79], v[112:115]\n"
    "  ds_read_b64_tr_b16 v[88:89], v10 offset:2760\n"
    "  v_lshlrev_b32 v15, 16, v125\n"
    "  v_mfma_f32_16x16x32_bf16 v[108:111], v[84:87], v[100:103], v[108:111]\n"
    "  ds_read_b64_tr_b16 v[90:91], v10 offset:2752\n"
    "  v_fmac_f32 v42, v15, v3\n"
    "  v_mfma_f32_16x16x32_bf16 v[104:107], v[84:87], v[60:63], v[104:107]\n"
    "  ds_read_b128 v[56:59], v11\n"
    "  v_and_b32 v15, 0xffff0000, v125\n"
    "  v_mfma_f32_16x16x32_bf16 v[20:23], v[84:87], v[52:55], v[20:23]\n"
    "  ds_read_b64_tr_b16 v[68:69], v10 offset:2248\n"
    "  v_fmac_f32 v43, v15, v3\n"
    "  v_mfma_f32_16x16x32_bf16 v[16:19], v[84:87], v[92:95], v[16:19]\n"
    "  ds_read_b64_tr_b16 v[70:71], v10 offset:2240\n"
    "  s_waitcnt vmcnt(7)\n"
    "  v_perm_b32 v44, v44, v44, s54\n"
    "  v_mfma_f32_16x16x32_bf16 v[24:27], v[96:99], v[64:67], v[24:27]\n"
    "  ds_read_b128 v[84:87], v11 offset:512\n"
    "  v_perm_b32 v45, v45, v45, s54\n"
    "  v_mfma_f32_16x16x32_bf16 v[116:119], v[96:99], v[76:79], v[116:119]\n"
    "  v_perm_b32 v46, v46, v46, s54\n"
    "  v_perm_b32 v47, v47, v47, s54\n"
    "  v_mfma_f32_16x16x32_bf16 v[112:115], v[96:99], v[100:103], v[112:115]\n"
    "  s_waitcnt lgkmcnt(12)\n"
    "  v_lshlrev_b32 v15, 16, v126\n"
    "  v_fmac_f32 v44, v15, v3\n"
    "  v_mfma_f32_16x16x32_bf16 v[108:111], v[96:99], v[60:63], v[108:111]\n"
    "  v_and_b32 v15, 0xffff0000, v126\n"
    "  v_fmac_f32 v45, v15, v3\n"
    "  v_mfma_f32_16x16x32_bf16 v[104:107], v[96:99], v[52:55], v[104:107]\n"
    "  v_lshlrev_b32 v15, 16, v127\n"
    "  v_fmac_f32 v46, v15, v3\n"
    "  v_mfma_f32_16x16x32_bf16 v[20:23], v[96:99], v[92:95], v[20:23]\n"
    "  v_and_b32 v15, 0xffff0000, v127\n"
    "  v_fmac_f32 v47, v15, v3\n"
    "  s_waitcnt lgkmcnt(9)\n"
    "  v_mfma_f32_16x16x32_bf16 v[16:19], v[96:99], v[48:51], v[16:19]\n"
    "  ds_read_b64_tr_b16 v[96:97], v10 offset:1736\n"
    "  v_perm_b32 v32, v32, v32, s54\n"
    "  v_mfma_f32_16x16x32_bf16 v[28:31], v[80:83], v[64:67], v[28:31]\n"
    "  ds_read_b64_tr_b16 v[98:99], v10 offset:1728\n"
    "  v_perm_b32 v33, v33, v33, s54\n"
    "  v_mfma_f32_16x16x32_bf16 v[24:27], v[80:83], v[76:79], v[24:27]\n"
    "  ds_read_b128 v[64:67], v11 offset:1024\n"
    "  v_perm_b32 v34, v34, v34, s54\n"
    "  v_mfma_f32_16x16x32_bf16 v[116:119], v[80:83], v[100:103], v[116:119]\n"
    "  ds_read_b64_tr_b16 v[76:77], v10 offset:1224\n"
    "  v_perm_b32 v35, v35, v35, s54\n"
    "  v_mfma_f32_16x16x32_bf16 v[112:115], v[80:83], v[60:63], v[112:115]\n"
    "  ds_read_b64_tr_b16 v[78:79], v10 offset:1216\n"
    "  s_waitcnt lgkmcnt(13)\n"
    "  v_lshlrev_b32 v15, 16, v120\n"
    "  v_mfma_f32_16x16x32_bf16 v[108:111], v[80:83], v[52:55], v[108:111]\n"
    "  ds_read_b64_tr_b16 v[100:101], v10 offset:712\n"
    "  v_fmac_f32 v32, v15, v3\n"
    "  v_mfma_f32_16x16x32_bf16 v[104:107], v[80:83], v[92:95], v[104:107]\n"
    "  ds_read_b64_tr_b16 v[102:103], v10 offset:704\n"
    "  v_and_b32 v15, 0xffff0000, v120\n"
    "  v_mfma_f32_16x16x32_bf16 v[20:23], v[80:83], v[48:51], v[20:23]\n"
    "  ds_read_b64_tr_b16 v[60:61], v10 offset:200\n"
    "  v_fmac_f32 v33, v15, v3\n"
    "  s_waitcnt lgkmcnt(9)\n"
    "  v_mfma_f32_16x16x32_bf16 v[16:19], v[80:83], v[72:75], v[16:19]\n"
    "  ds_read_b64_tr_b16 v[62:63], v10 offset:192\n"
    "  v_lshlrev_b32 v15, 16, v121\n"
    "  v_mfma_f32_16x16x32_bf16 v[104:107], v[88:91], v[56:59], v[104:107]\n"
    "  ds_read_b128 v[52:55], v11 offset:1536\n"
    "  v_fmac_f32 v34, v15, v3\n"
    "  v_mfma_f32_16x16x32_bf16 v[108:111], v[68:71], v[56:59], v[108:111]\n"
    "  ds_read_b128 v[92:95], v11 offset:2048\n"
    "  v_and_b32 v15, 0xffff0000, v121\n"
    "  s_waitcnt lgkmcnt(9)\n"
    "  v_mfma_f32_16x16x32_bf16 v[104:107], v[68:71], v[84:87], v[104:107]\n"
    "  ds_read_b128 v[48:51], v11 offset:2560\n"
    "  v_fmac_f32 v35, v15, v3\n"
    "  v_mfma_f32_16x16x32_bf16 v[112:115], v[96:99], v[56:59], v[112:115]\n"
    "  ds_read_b64_tr_b16 v[80:81], v10 offset:3784\n"
    "  v_perm_b32 v36, v36, v36, s54\n"
    "  v_mfma_f32_16x16x32_bf16 v[108:111], v[96:99], v[84:87], v[108:111]\n"
    "  ds_read_b64_tr_b16 v[82:83], v10 offset:3776\n"
    "  v_perm_b32 v37, v37, v37, s54\n"
    "  s_waitcnt lgkmcnt(7)\n"
    "  v_mfma_f32_16x16x32_bf16 v[104:107], v[96:99], v[64:67], v[104:107]\n"
    "  ds_read_b64_tr_b16 v[72:73], v10 offset:3272\n"
    "  v_perm_b32 v38, v38, v38, s54\n"
    "  v_mfma_f32_16x16x32_bf16 v[116:119], v[76:79], v[56:59], v[116:119]\n"
    "  ds_read_b64_tr_b16 v[74:75], v10 offset:3264\n"
    "  v_perm_b32 v39, v39, v39, s54\n"
    "  v_mfma_f32_16x16x32_bf16 v[116:119], v[100:103], v[84:87], v[116:119]\n"
    "  v_lshlrev_b32 v15, 16, v122\n"
    "  v_fmac_f32 v36, v15, v3\n"
    "  s_waitcnt lgkmcnt(7)\n"
    "  v_mfma_f32_16x16x32_bf16 v[116:119], v[60:63], v[64:67], v[116:119]\n"
    "  v_and_b32 v15, 0xffff0000, v122\n"
    "  v_fmac_f32 v37, v15, v3\n"
    "  v_mfma_f32_16x16x32_bf16 v[112:115], v[76:79], v[84:87], v[112:115]\n"
    "  v_lshlrev_b32 v15, 16, v123\n"
    "  v_fmac_f32 v38, v15, v3\n"
    "  v_mfma_f32_16x16x32_bf16 v[112:115], v[100:103], v[64:67], v[112:115]\n"
    "  v_and_b32 v15, 0xffff0000, v123\n"
    "  v_fmac_f32 v39, v15, v3\n"
    "  s_waitcnt lgkmcnt(6)\n"
    "  v_mfma_f32_16x16x32_bf16 v[112:115], v[60:63], v[52:55], v[112:115]\n"
    "  v_mfma_f32_16x16x32_bf16 v[108:111], v[76:79], v[64:67], v[108:111]\n"
    "  ds_write_b128 v14, v[116:119] offset:3072\n"
    "  v_mfma_f32_16x16x32_bf16 v[108:111], v[100:103], v[52:55], v[108:111]\n"
    "  s_waitcnt lgkmcnt(6)\n"
    "  v_mfma_f32_16x16x32_bf16 v[108:111], v[60:63], v[92:95], v[108:111]\n"
    "  v_mfma_f32_16x16x32_bf16 v[104:107], v[76:79], v[52:55], v[104:107]\n"
    "  s_nop 1\n"
    "  ds_write_b128 v14, v[112:115] offset:2048\n"
    "  v_mfma_f32_16x16x32_bf16 v[104:107], v[100:103], v[92:95], v[104:107]\n"
    "  s_waitcnt lgkmcnt(6)\n"
    "  v_mfma_f32_16x16x32_bf16 v[104:107], v[60:63], v[48:51], v[104:107]\n"
    "  s_waitcnt lgkmcnt(4)\n"
    "  v_mfma_f32_16x16x32_bf16 v[16:19], v[80:83], v[56:59], v[16:19]\n"
    "  ds_write_b128 v14, v[108:111] offset:1024\n"
    "  s_waitcnt lgkmcnt(3)\n"
    "  v_mfma_f32_16x16x32_bf16 v[20:23], v[72:75], v[56:59], v[20:23]\n"
    "  ds_read_b128 v[80:83], v11 offset:3072\n"
    "  v_mfma_f32_16x16x32_bf16 v[16:19], v[72:75], v[84:87], v[16:19]\n"
    "  ds_read_b128 v[72:75], v11 offset:3584\n"
    "  v_mfma_f32_16x16x32_bf16 v[20:23], v[88:91], v[84:87], v[20:23]\n"
    "  ds_write_b128 v14, v[104:107]\n"
    "  v_mfma_f32_16x16x32_bf16 v[16:19], v[88:91], v[64:67], v[16:19]\n"
    "  v_mfma_f32_16x16x32_bf16 v[20:23], v[68:71], v[64:67], v[20:23]\n"
    "  v_mfma_f32_16x16x32_bf16 v[16:19], v[68:71], v[52:55], v[16:19]\n"
    "  v_xor_b32 v14, 4096, v14\n"
    "  s_waitcnt lgkmcnt(0)\n"
    "  s_barrier\n"
    "  ds_read_b128 v[104:107], v14\n"
    "  v_mfma_f32_16x16x32_bf16 v[20:23], v[96:99], v[52:55], v[20:23]\n"
    "  ds_read_b128 v[108:111], v14 offset:1024\n"
    "  v_mfma_f32_16x16x32_bf16 v[16:19], v[96:99], v[92:95], v[16:19]\n"
    "  ds_read_b128 v[112:115], v14 offset:2048\n"
    "  v_mfma_f32_16x16x32_bf16 v[20:23], v[76:79], v[92:95], v[20:23]\n"
    "  ds_read_b128 v[116:119], v14 offset:3072\n"
    "  v_mfma_f32_16x16x32_bf16 v[16:19], v[76:79], v[48:51], v[16:19]\n"
    "  v_mfma_f32_16x16x32_bf16 v[24:27], v[100:103], v[56:59], v[24:27]\n"
    "  s_waitcnt lgkmcnt(3)\n"
    "  v_add_f32 v32, v32, v104\n"
    "  v_add_f32 v33, v33, v105\n"
    "  v_mfma_f32_16x16x32_bf16 v[20:23], v[100:103], v[48:51], v[20:23]\n"
    "  v_add_f32 v34, v34, v106\n"
    "  v_add_f32 v35, v35, v107\n"
    "  v_mfma_f32_16x16x32_bf16 v[16:19], v[100:103], v[80:83], v[16:19]\n"
    "  s_waitcnt lgkmcnt(2)\n"
    "  v_add_f32 v36, v36, v108\n"
    "  v_add_f32 v37, v37, v109\n"
    "  v_mfma_f32_16x16x32_bf16 v[28:31], v[60:63], v[56:59], v[28:31]\n"
    "  v_add_f32 v38, v38, v110\n"
    "  v_add_f32 v39, v39, v111\n"
    "  v_mfma_f32_16x16x32_bf16 v[24:27], v[60:63], v[84:87], v[24:27]\n"
    "  s_waitcnt lgkmcnt(1)\n"
    "  v_add_f32 v40, v40, v112\n"
    "  v_add_f32 v41, v41, v113\n"
    "  v_mfma_f32_16x16x32_bf16 v[20:23], v[60:63], v[80:83], v[20:23]\n"
    "  v_add_f32 v42, v42, v114\n"
    "  v_add_f32 v43, v43, v115\n"
    "  v_mfma_f32_16x16x32_bf16 v[16:19], v[60:63], v[72:75], v[16:19]\n"
    "  s_waitcnt lgkmcnt(0)\n"
    "  v_add_f32 v44, v44, v116\n"
    "  v_add_f32 v45, v45, v117\n"
    "  v_add_f32 v46, v46, v118\n"
    "  v_add_f32 v47, v47, v119\n"
    "  s_branch Lbj_47%=\n"
    "Lb_46%=:\n"
    "  s_waitcnt lgkmcnt(6)\n"
    "  v_mfma_f32_16x16x32_bf16 v[104:107], v[88:91], v[60:63], 0\n"
    "  ds_read_b64_tr_b16 v[48:49], v10 offset:1544\n"
    "  v_mfma_f32_16x16x32_bf16 v[108:111], v[92:95], v[60:63], 0\n"
    "  buffer_load_dwordx4 v[32:35], v14, s[16:19], s34 offen sc1\n"
    "  v_mfma_f32_16x16x32_bf16 v[104:107], v[92:95], v[64:67], v[104:107]\n"
    "  ds_read_b64_tr_b16 v[50:51], v10 offset:1536\n"
    "  s_waitcnt lgkmcnt(6)\n"
    "  v_mfma_f32_16x16x32_bf16 v[16:19], v[96:99], v[60:63], 0\n"
    "  ds_read_b128 v[52:55], v11 offset:2240\n"
    "  v_mfma_f32_16x16x32_bf16 v[108:111], v[96:99], v[64:67], v[108:111]\n"
    "  buffer_load_dwordx4 v[36:39], v14, s[16:19], s35 offen sc1\n"
    "  s_waitcnt lgkmcnt(6)\n"
    "  v_mfma_f32_16x16x32_bf16 v[104:107], v[96:99], v[68:71], v[104:107]\n"
    "  ds_read_b64_tr_b16 v[56:57], v10 offset:1032\n"
    "  s_waitcnt lgkmcnt(5)\n"
    "  v_mfma_f32_16x16x32_bf16 v[20:23], v[100:103], v[60:63], 0\n"
    "  ds_read_b64_tr_b16 v[58:59], v10 offset:1024\n"
    "  v_mfma_f32_16x16x32_bf16 v[16:19], v[100:103], v[64:67], v[16:19]\n"
    "  buffer_load_dwordx4 v[40:43], v14, s[16:19], s36 offen sc1\n"
    "  v_mfma_f32_16x16x32_bf16 v[108:111], v[100:103], v[68:71], v[108:111]\n"
    "  ds_read_b128 v[76:79], v11 offset:2752\n"
    "  s_waitcnt lgkmcnt(6)\n"
    "  v_mfma_f32_16x16x32_bf16 v[104:107], v[100:103], v[72:75], v[104:107]\n"
    "  ds_read_b64_tr_b16 v[80:81], v10 offset:520\n"
    "  s_waitcnt lgkmcnt(5)\n"
    "  v_mfma_f32_16x16x32_bf16 v[24:27], v[48:51], v[60:63], 0\n"
    "  buffer_load_dwordx4 v[44:47], v14, s[16:19], s37 offen sc1\n"
    "  v_mfma_f32_16x16x32_bf16 v[20:23], v[48:51], v[64:67], v[20:23]\n"
    "  ds_read_b64_tr_b16 v[82:83], v10 offset:512\n"
    "  s_lshl_b32 s67, s40, 12\n"
    "  v_mfma_f32_16x16x32_bf16 v[16:19], v[48:51], v[68:71], v[16:19]\n"
    "  ds_read_b128 v[84:87], v11 offset:3264\n"
    "  s_add_u32 s67, s67, 0x4000\n"
    "  v_mfma_f32_16x16x32_bf16 v[108:111], v[48:51], v[72:75], v[108:111]\n"
    "  ds_read_b64_tr_b16 v[88:89], v10 offset:8\n"
    "  v_add_u32 v14, s67, v2\n"
    "  s_waitcnt lgkmcnt(7)\n"
    "  v_mfma_f32_16x16x32_bf16 v[104:107], v[48:51], v[52:55], v[104:107]\n"
    "  ds_read_b64_tr_b16 v[90:91], v10\n"
    "  s_waitcnt lgkmcnt(6)\n"
    "  v_mfma_f32_16x16x32_bf16 v[28:31], v[56:59], v[60:63], 0\n"
    "  ds_read_b128 v[92:95], v11 offset:3776\n"
    "  v_mfma_f32_16x16x32_bf16 v[24:27], v[56:59], v[64:67], v[24:27]\n"
    "  ds_read_b64_tr_b16 v[96:97], v10 offset:3656\n"
    "  v_mov_b32 v120, -1\n"
    "  v_mfma_f32_16x16x32_bf16 v[20:23], v[56:59], v[68:71], v[20:23]\n"
    "  ds_read_b64_tr_b16 v[98:99], v10 offset:3648\n"
    "  v_mov_b32 v121, -1\n"
    "  v_mfma_f32_16x16x32_bf16 v[16:19], v[56:59], v[72:75], v[16:19]\n"
    "  ds_read_b128 v[100:103], v11 offset:128\n"
    "  v_mov_b32 v122, -1\n"
    "  v_mfma_f32_16x16x32_bf16 v[108:111], v[56:59], v[52:55], v[108:111]\n"
    "  ds_read_b64_tr_b16 v[48:49], v10 offset:3144\n"
    "  v_mov_b32 v123, -1\n"
    "  s_waitcnt lgkmcnt(8)\n"
    "  v_mfma_f32_16x16x32_bf16 v[104:107], v[56:59], v[76:79], v[104:107]\n"
    "  ds_read_b64_tr_b16 v[50:51], v10 offset:3136\n"
    "  s_lshl_b32 s67, s47, 8\n"
    "  v_mfma_f32_16x16x32_bf16 v[112:115], v[80:83], v[60:63], 0\n"
    "  ds_read_b128 v[56:59], v11 offset:640\n"
    "  s_add_u32 s67, s67, s41\n"
    "  v_mfma_f32_16x16x32_bf16 v[28:31], v[80:83], v[64:67], v[28:31]\n"
    "  s_lshl_b32 s67, s67, 7\n"
    "  s_add_u32 s38, s67, 0x300000\n"
    "  v_mfma_f32_16x16x32_bf16 v[24:27], v[80:83], v[68:71], v[24:27]\n"
    "  buffer_store_dwordx4 v[120:123], v8, s[16:19], s38 offen sc1\n"
    "  s_lshl_b32 s67, s47, 5\n"
    "  v_mfma_f32_16x16x32_bf16 v[20:23], v[80:83], v[72:75], v[20:23]\n"
    "  s_add_u32 s67, s67, s41\n"
    "  s_lshl_b32 s67, s67, 6\n"
    "  v_mfma_f32_16x16x32_bf16 v[16:19], v[80:83], v[52:55], v[16:19]\n"
    "  s_add_u32 s39, s67, 0x30000\n"
    "  s_mov_b64 exec, 0xffff\n"
    "  buffer_store_dword v120, v9, s[16:19], s39 offen sc1\n"
    "  s_mov_b64 exec, -1\n"
    "  v_mfma_f32_16x16x32_bf16 v[108:111], v[80:83], v[76:79], v[108:111]\n"
    "  s_and_b32 s67, s40, 1\n"
    "  s_lshl1_add_u32 s67, s41, s67\n"
    "  s_waitcnt lgkmcnt(7)\n"
    "  v_mfma_f32_16x16x32_bf16 v[104:107], v[80:83], v[84:87], v[104:107]\n"
    "  ds_read_b64_tr_b16 v[80:81], v10 offset:2632\n"
    "  s_lshl_b32 s67, s67, 5\n"
    "  v_mfma_f32_16x16x32_bf16 v[116:119], v[88:91], v[60:63], 0\n"
    "  ds_read_b64_tr_b16 v[82:83], v10 offset:2624\n"
    "  s_lshl2_add_u32 s67, s42, s67\n"
    "  v_mfma_f32_16x16x32_bf16 v[112:115], v[88:91], v[64:67], v[112:115]\n"
    "  ds_read_b128 v[60:63], v11 offset:1152\n"
    "  s_and_b32 s66, s40, 2\n"
    "  v_mfma_f32_16x16x32_bf16 v[28:31], v[88:91], v[68:71], v[28:31]\n"
    "  ds_read_b64_tr_b16 v[64:65], v10 offset:2120\n"
    "  s_add_u32 s67, s67, s66\n"
    "  v_mfma_f32_16x16x32_bf16 v[24:27], v[88:91], v[72:75], v[24:27]\n"
    "  ds_read_b64_tr_b16 v[66:67], v10 offset:2112\n"
    "  s_lshl_b32 s67, s67, 10\n"
    "  v_mfma_f32_16x16x32_bf16 v[20:23], v[88:91], v[52:55], v[20:23]\n"
    "  ds_read_b128 v[68:71], v11 offset:1664\n"
    "  s_add_u32 s38, s67, 0x600000\n"
    "  v_mfma_f32_16x16x32_bf16 v[16:19], v[88:91], v[76:79], v[16:19]\n"
    "  ds_read_b64_tr_b16 v[72:73], v10 offset:1608\n"
    "  buffer_store_dwordx4 v[120:123], v2, s[16:19], s38 offen sc1\n"
    "  v_mfma_f32_16x16x32_bf16 v[108:111], v[88:91], v[84:87], v[108:111]\n"
    "  ds_read_b64_tr_b16 v[74:75], v10 offset:1600\n"
    "  buffer_store_dwordx4 v[120:123], v2, s[16:19], s38 offen offset:1024 sc1\n"
    "  s_waitcnt lgkmcnt(9)\n"
    "  v_mfma_f32_16x16x32_bf16 v[104:107], v[88:91], v[92:95], v[104:107]\n"
    "  ds_read_b128 v[52:55], v11 offset:2176\n"
    "  s_lshl_b32 s67, s2, 7\n"
    "  v_mfma_f32_16x16x32_bf16 v[104:107], v[96:99], v[100:103], v[104:107]\n"
    "  ds_read_b64_tr_b16 v[76:77], v10 offset:1096\n"
    "  s_add_u32 s38, s67, 0x1c0000\n"
    "  v_mfma_f32_16x16x32_bf16 v[108:111], v[48:51], v[100:103], v[108:111]\n"
    "  ds_read_b64_tr_b16 v[78:79], v10 offset:1088\n"
    "  v_lshrrev_b32 v126, 3, v1\n"
    "  s_waitcnt lgkmcnt(9)\n"
    "  v_mfma_f32_16x16x32_bf16 v[104:107], v[48:51], v[56:59], v[104:107]\n"
    "  ds_read_b128 v[84:87], v11 offset:2688\n"
    "  v_and_b32 v125, 7, v1\n"
    "  v_mfma_f32_16x16x32_bf16 v[16:19], v[80:83], v[100:103], v[16:19]\n"
    "  ds_read_b64_tr_b16 v[88:89], v10 offset:584\n"
    "  v_lshlrev_b32 v125, 3, v125\n"
    "  v_mfma_f32_16x16x32_bf16 v[108:111], v[80:83], v[56:59], v[108:111]\n"
    "  ds_read_b64_tr_b16 v[90:91], v10 offset:576\n"
    "  v_lshl_add_u32 v124, v126, 15, v125\n"
    "  s_waitcnt lgkmcnt(9)\n"
    "  v_mfma_f32_16x16x32_bf16 v[104:107], v[80:83], v[60:63], v[104:107]\n"
    "  s_mov_b32 s60, 0x03030303\n"
    "  s_mov_b32 s61, 0x03030303\n"
    "  v_mfma_f32_16x16x32_bf16 v[20:23], v[64:67], v[100:103], v[20:23]\n"
    "  s_cmp_eq_u32 s40, 0\n"
    "  s_cselect_b64 exec, -1, 0\n"
    "  buffer_store_dwordx2 v[120:121], v124, s[16:19], s38 offen sc1\n"
    "  s_cselect_b64 exec, s[60:61], 0\n"
    "  buffer_store_dwordx2 v[120:121], v124, s[16:19], s38 offen offset:64 sc1\n"
    "  s_mov_b64 exec, -1\n"
    "  s_lshl_b32 s67, s44, 2\n"
    "  v_mfma_f32_16x16x32_bf16 v[16:19], v[64:67], v[56:59], v[16:19]\n"
    "  ds_read_b128 v[92:95], v11 offset:3200\n"
    "  s_lshl4_add_u32 s67, s43, s67\n"
    "  v_mfma_f32_16x16x32_bf16 v[108:111], v[64:67], v[60:63], v[108:111]\n"
    "  ds_read_b64_tr_b16 v[96:97], v10 offset:72\n"
    "  s_add_u32 s67, s67, s40\n"
    "  s_waitcnt lgkmcnt(8)\n"
    "  v_mfma_f32_16x16x32_bf16 v[104:107], v[64:67], v[68:71], v[104:107]\n"
    "  ds_read_b64_tr_b16 v[98:99], v10 offset:64\n"
    "  s_lshl_b32 s67, s67, 8\n"
    "  v_mfma_f32_16x16x32_bf16 v[24:27], v[72:75], v[100:103], v[24:27]\n"
    "  s_add_u32 s38, s67, 0x80000\n"
    "  buffer_store_dword v120, v9, s[16:19], s38 offen sc1\n"
    "  v_mfma_f32_16x16x32_bf16 v[20:23], v[72:75], v[56:59], v[20:23]\n"
    "  v_mfma_f32_16x16x32_bf16 v[16:19], v[72:75], v[60:63], v[16:19]\n"
    "  v_mfma_f32_16x16x32_bf16 v[108:111], v[72:75], v[68:71], v[108:111]\n"
    "  s_waitcnt lgkmcnt(6)\n"
    "  v_mfma_f32_16x16x32_bf16 v[104:107], v[72:75], v[52:55], v[104:107]\n"
    "  v_mfma_f32_16x16x32_bf16 v[28:31], v[76:79], v[100:103], v[28:31]\n"
    "  ds_read_b128 v[48:51], v11 offset:3712\n"
    "  v_mfma_f32_16x16x32_bf16 v[24:27], v[76:79], v[56:59], v[24:27]\n"
    "  ds_read_b64_tr_b16 v[80:81], v10 offset:3720\n"
    "  v_mfma_f32_16x16x32_bf16 v[20:23], v[76:79], v[60:63], v[20:23]\n"
    "  ds_read_b64_tr_b16 v[82:83], v10 offset:3712\n"
    "  v_mfma_f32_16x16x32_bf16 v[16:19], v[76:79], v[68:71], v[16:19]\n"
    "  ds_read_b128 v[64:67], v11 offset:64\n"
    "  v_mfma_f32_16x16x32_bf16 v[108:111], v[76:79], v[52:55], v[108:111]\n"
    "  ds_read_b64_tr_b16 v[72:73], v10 offset:3208\n"
    "  s_waitcnt lgkmcnt(8)\n"
    "  v_mfma_f32_16x16x32_bf16 v[104:107], v[76:79], v[84:87], v[104:107]\n"
    "  ds_read_b64_tr_b16 v[74:75], v10 offset:3200\n"
    "  v_mfma_f32_16x16x32_bf16 v[112:115], v[88:91], v[100:103], v[112:115]\n"
    "  ds_read_b128 v[76:79], v11 offset:576\n"
    "  v_mfma_f32_16x16x32_bf16 v[28:31], v[88:91], v[56:59], v[28:31]\n"
    "  v_mfma_f32_16x16x32_bf16 v[24:27], v[88:91], v[60:63], v[24:27]\n"
    "  v_mfma_f32_16x16x32_bf16 v[20:23], v[88:91], v[68:71], v[20:23]\n"
    "  v_mfma_f32_16x16x32_bf16 v[16:19], v[88:91], v[52:55], v[16:19]\n"
    "  v_mfma_f32_16x16x32_bf16 v[108:111], v[88:91], v[84:87], v[108:111]\n"
    "  s_waitcnt lgkmcnt(7)\n"
    "  v_mfma_f32_16x16x32_bf16 v[104:107], v[88:91], v[92:95], v[104:107]\n"
    "  ds_read_b64_tr_b16 v[88:89], v10 offset:2696\n"
    "  v_mfma_f32_16x16x32_bf16 v[116:119], v[96:99], v[100:103], v[116:119]\n"
    "  ds_read_b64_tr_b16 v[90:91], v10 offset:2688\n"
    "  v_mfma_f32_16x16x32_bf16 v[112:115], v[96:99], v[56:59], v[112:115]\n"
    "  ds_read_b128 v[100:103], v11 offset:1088\n"
    "  v_mfma_f32_16x16x32_bf16 v[28:31], v[96:99], v[60:63], v[28:31]\n"
    "  ds_read_b64_tr_b16 v[56:57], v10 offset:2184\n"
    "  v_mfma_f32_16x16x32_bf16 v[24:27], v[96:99], v[68:71], v[24:27]\n"
    "  ds_read_b64_tr_b16 v[58:59], v10 offset:2176\n"
    "  v_mfma_f32_16x16x32_bf16 v[20:23], v[96:99], v[52:55], v[20:23]\n"
    "  ds_read_b128 v[60:63], v11 offset:1600\n"
    "  v_mfma_f32_16x16x32_bf16 v[16:19], v[96:99], v[84:87], v[16:19]\n"
    "  ds_read_b64_tr_b16 v[68:69], v10 offset:1672\n"
    "  v_mfma_f32_16x16x32_bf16 v[108:111], v[96:99], v[92:95], v[108:111]\n"
    "  ds_read_b64_tr_b16 v[70:71], v10 offset:1664\n"
    "  s_waitcnt lgkmcnt(8)\n"
    "  v_mfma_f32_16x16x32_bf16 v[104:107], v[96:99], v[48:51], v[104:107]\n"
    "  ds_read_b128 v[52:55], v11 offset:2112\n"
    "  v_mfma_f32_16x16x32_bf16 v[104:107], v[80:83], v[64:67], v[104:107]\n"
    "  ds_read_b64_tr_b16 v[84:85], v10 offset:1160\n"
    "  v_mfma_f32_16x16x32_bf16 v[108:111], v[72:75], v[64:67], v[108:111]\n"
    "  ds_read_b64_tr_b16 v[86:87], v10 offset:1152\n"
    "  v_mfma_f32_16x16x32_bf16 v[104:107], v[72:75], v[76:79], v[104:107]\n"
    "  ds_read_b128 v[92:95], v11 offset:2624\n"
    "  s_waitcnt lgkmcnt(7)\n"
    "  v_mfma_f32_16x16x32_bf16 v[16:19], v[88:91], v[64:67], v[16:19]\n"
    "  ds_read_b64_tr_b16 v[96:97], v10 offset:648\n"
    "  v_mfma_f32_16x16x32_bf16 v[108:111], v[88:91], v[76:79], v[108:111]\n"
    "  ds_read_b64_tr_b16 v[98:99], v10 offset:640\n"
    "  v_mfma_f32_16x16x32_bf16 v[104:107], v[88:91], v[100:103], v[104:107]\n"
    "  ds_read_b64 v[124:125], v12 offset:1536\n"
    "  v_mfma_f32_16x16x32_bf16 v[20:23], v[56:59], v[64:67], v[20:23]\n"
    "  ds_read_b64 v[126:127], v12 offset:1024\n"
    "  v_mfma_f32_16x16x32_bf16 v[16:19], v[56:59], v[76:79], v[16:19]\n"
    "  ds_read_b128 v[48:51], v11 offset:3136\n"
    "  v_mfma_f32_16x16x32_bf16 v[108:111], v[56:59], v[100:103], v[108:111]\n"
    "  ds_read_b64_tr_b16 v[80:81], v10 offset:136\n"
    "  s_waitcnt lgkmcnt(10)\n"
    "  v_mfma_f32_16x16x32_bf16 v[104:107], v[56:59], v[60:63], v[104:107]\n"
    "  ds_read_b64_tr_b16 v[82:83], v10 offset:128\n"
    "  v_mfma_f32_16x16x32_bf16 v[24:27], v[68:71], v[64:67], v[24:27]\n"
    "  ds_read_b64 v[120:121], v12 offset:2560\n"
    "  v_mfma_f32_16x16x32_bf16 v[20:23], v[68:71], v[76:79], v[20:23]\n"
    "  ds_read_b64 v[122:123], v12 offset:2048\n"
    "  s_waitcnt vmcnt(8)\n"
    "  v_perm_b32 v40, v40, v40, s54\n"
    "  v_mfma_f32_16x16x32_bf16 v[16:19], v[68:71], v[100:103], v[16:19]\n"
    "  v_perm_b32 v41, v41, v41, s54\n"
    "  v_perm_b32 v42, v42, v42, s54\n"
    "  v_mfma_f32_16x16x32_bf16 v[108:111], v[68:71], v[60:63], v[108:111]\n"
    "  v_perm_b32 v43, v43, v43, s54\n"
    "  s_waitcnt lgkmcnt(6)\n"
    "  v_lshlrev_b32 v15, 16, v124\n"
    "  v_mfma_f32_16x16x32_bf16 v[104:107], v[68:71], v[52:55], v[104:107]\n"
    "  v_fmac_f32 v40, v15, v3\n"
    "  v_and_b32 v15, 0xffff0000, v124\n"
    "  v_mfma_f32_16x16x32_bf16 v[28:31], v[84:87], v[64:67], v[28:31]\n"
    "  ds_read_b128 v[72:75], v11 offset:3648\n"
    "  v_fmac_f32 v41, v15, v3\n"
    "  v_mfma_f32_16x16x32_bf16 v[24:27], v[84:87], v[76:79], v[24:27]\n"
    "  ds_read_b64_tr_b16 v[88:89], v10 offset:3784\n"
    "  v_lshlrev_b32 v15, 16, v125\n"
    "  v_mfma_f32_16x16x32_bf16 v[20:23], v[84:87], v[100:103], v[20:23]\n"
    "  ds_read_b64_tr_b16 v[90:91], v10 offset:3776\n"
    "  v_fmac_f32 v42, v15, v3\n"
    "  v_mfma_f32_16x16x32_bf16 v[16:19], v[84:87], v[60:63], v[16:19]\n"
    "  ds_read_b128 v[56:59], v11\n"
    "  v_and_b32 v15, 0xffff0000, v125\n"
    "  v_mfma_f32_16x16x32_bf16 v[108:111], v[84:87], v[52:55], v[108:111]\n"
    "  ds_read_b64_tr_b16 v[68:69], v10 offset:3272\n"
    "  v_fmac_f32 v43, v15, v3\n"
    "  v_mfma_f32_16x16x32_bf16 v[104:107], v[84:87], v[92:95], v[104:107]\n"
    "  ds_read_b64_tr_b16 v[70:71], v10 offset:3264\n"
    "  s_waitcnt vmcnt(7)\n"
    "  v_perm_b32 v44, v44, v44, s54\n"
    "  v_mfma_f32_16x16x32_bf16 v[112:115], v[96:99], v[64:67], v[112:115]\n"
    "  ds_read_b128 v[84:87], v11 offset:512\n"
    "  v_perm_b32 v45, v45, v45, s54\n"
    "  v_mfma_f32_16x16x32_bf16 v[28:31], v[96:99], v[76:79], v[28:31]\n"
    "  v_perm_b32 v46, v46, v46, s54\n"
    "  v_perm_b32 v47, v47, v47, s54\n"
    "  v_mfma_f32_16x16x32_bf16 v[24:27], v[96:99], v[100:103], v[24:27]\n"
    "  s_waitcnt lgkmcnt(12)\n"
    "  v_lshlrev_b32 v15, 16, v126\n"
    "  v_fmac_f32 v44, v15, v3\n"
    "  v_mfma_f32_16x16x32_bf16 v[20:23], v[96:99], v[60:63], v[20:23]\n"
    "  v_and_b32 v15, 0xffff0000, v126\n"
    "  v_fmac_f32 v45, v15, v3\n"
    "  v_mfma_f32_16x16x32_bf16 v[16:19], v[96:99], v[52:55], v[16:19]\n"
    "  v_lshlrev_b32 v15, 16, v127\n"
    "  v_fmac_f32 v46, v15, v3\n"
    "  v_mfma_f32_16x16x32_bf16 v[108:111], v[96:99], v[92:95], v[108:111]\n"
    "  v_and_b32 v15, 0xffff0000, v127\n"
    "  v_fmac_f32 v47, v15, v3\n"
    "  s_waitcnt lgkmcnt(9)\n"
    "  v_mfma_f32_16x16x32_bf16 v[104:107], v[96:99], v[48:51], v[104:107]\n"
    "  ds_read_b64_tr_b16 v[96:97], v10 offset:2760\n"
    "  v_perm_b32 v32, v32, v32, s54\n"
    "  v_mfma_f32_16x16x32_bf16 v[116:119], v[80:83], v[64:67], v[116:119]\n"
    "  ds_read_b64_tr_b16 v[98:99], v10 offset:2752\n"
    "  v_perm_b32 v33, v33, v33, s54\n"
    "  v_mfma_f32_16x16x32_bf16 v[112:115], v[80:83], v[76:79], v[112:115]\n"
    "  ds_read_b128 v[64:67], v11 offset:1024\n"
    "  v_perm_b32 v34, v34, v34, s54\n"
    "  v_mfma_f32_16x16x32_bf16 v[28:31], v[80:83], v[100:103], v[28:31]\n"
    "  ds_read_b64_tr_b16 v[76:77], v10 offset:2248\n"
    "  v_perm_b32 v35, v35, v35, s54\n"
    "  v_mfma_f32_16x16x32_bf16 v[24:27], v[80:83], v[60:63], v[24:27]\n"
    "  ds_read_b64_tr_b16 v[78:79], v10 offset:2240\n"
    "  s_waitcnt lgkmcnt(13)\n"
    "  v_lshlrev_b32 v15, 16, v120\n"
    "  v_mfma_f32_16x16x32_bf16 v[20:23], v[80:83], v[52:55], v[20:23]\n"
    "  ds_read_b128 v[100:103], v11 offset:1536\n"
    "  v_fmac_f32 v32, v15, v3\n"
    "  v_mfma_f32_16x16x32_bf16 v[16:19], v[80:83], v[92:95], v[16:19]\n"
    "  ds_read_b64_tr_b16 v[60:61], v10 offset:1736\n"
    "  v_and_b32 v15, 0xffff0000, v120\n"
    "  v_mfma_f32_16x16x32_bf16 v[108:111], v[80:83], v[48:51], v[108:111]\n"
    "  ds_read_b64_tr_b16 v[62:63], v10 offset:1728\n"
    "  v_fmac_f32 v33, v15, v3\n"
    "  s_waitcnt lgkmcnt(9)\n"
    "  v_mfma_f32_16x16x32_bf16 v[104:107], v[80:83], v[72:75], v[104:107]\n"
    "  ds_read_b128 v[52:55], v11 offset:2048\n"
    "  v_lshlrev_b32 v15, 16, v121\n"
    "  v_mfma_f32_16x16x32_bf16 v[104:107], v[88:91], v[56:59], v[104:107]\n"
    "  ds_read_b64_tr_b16 v[92:93], v10 offset:200\n"
    "  v_fmac_f32 v34, v15, v3\n"
    "  v_mfma_f32_16x16x32_bf16 v[108:111], v[68:71], v[56:59], v[108:111]\n"
    "  ds_read_b64_tr_b16 v[94:95], v10 offset:192\n"
    "  v_and_b32 v15, 0xffff0000, v121\n"
    "  s_waitcnt lgkmcnt(8)\n"
    "  v_mfma_f32_16x16x32_bf16 v[104:107], v[68:71], v[84:87], v[104:107]\n"
    "  ds_read_b64_tr_b16 v[48:49], v10 offset:712\n"
    "  v_fmac_f32 v35, v15, v3\n"
    "  v_mfma_f32_16x16x32_bf16 v[108:111], v[96:99], v[84:87], v[108:111]\n"
    "  ds_read_b64_tr_b16 v[50:51], v10 offset:704\n"
    "  v_perm_b32 v36, v36, v36, s54\n"
    "  v_mfma_f32_16x16x32_bf16 v[104:107], v[96:99], v[64:67], v[104:107]\n"
    "  ds_read_b64_tr_b16 v[80:81], v10 offset:1224\n"
    "  v_perm_b32 v37, v37, v37, s54\n"
    "  s_waitcnt lgkmcnt(6)\n"
    "  v_mfma_f32_16x16x32_bf16 v[108:111], v[76:79], v[64:67], v[108:111]\n"
    "  ds_read_b64_tr_b16 v[82:83], v10 offset:1216\n"
    "  v_perm_b32 v38, v38, v38, s54\n"
    "  v_mfma_f32_16x16x32_bf16 v[104:107], v[76:79], v[100:103], v[104:107]\n"
    "  ds_read_b128 v[72:75], v11 offset:2560\n"
    "  v_perm_b32 v39, v39, v39, s54\n"
    "  v_mfma_f32_16x16x32_bf16 v[108:111], v[60:63], v[100:103], v[108:111]\n"
    "  ds_read_b128 v[88:91], v11 offset:3072\n"
    "  v_lshlrev_b32 v15, 16, v122\n"
    "  s_waitcnt lgkmcnt(6)\n"
    "  v_mfma_f32_16x16x32_bf16 v[104:107], v[60:63], v[52:55], v[104:107]\n"
    "  ds_read_b128 v[68:71], v11 offset:3584\n"
    "  v_fmac_f32 v36, v15, v3\n"
    "  v_mfma_f32_16x16x32_bf16 v[116:119], v[92:95], v[56:59], v[116:119]\n"
    "  v_and_b32 v15, 0xffff0000, v122\n"
    "  v_fmac_f32 v37, v15, v3\n"
    "  s_waitcnt lgkmcnt(5)\n"
    "  v_mfma_f32_16x16x32_bf16 v[112:115], v[48:51], v[56:59], v[112:115]\n"
    "  v_lshlrev_b32 v15, 16, v123\n"
    "  v_fmac_f32 v38, v15, v3\n"
    "  v_mfma_f32_16x16x32_bf16 v[112:115], v[92:95], v[84:87], v[112:115]\n"
    "  v_and_b32 v15, 0xffff0000, v123\n"
    "  v_fmac_f32 v39, v15, v3\n"
    "  s_waitcnt lgkmcnt(3)\n"
    "  v_mfma_f32_16x16x32_bf16 v[108:111], v[80:83], v[52:55], v[108:111]\n"
    "  s_waitcnt lgkmcnt(2)\n"
    "  v_mfma_f32_16x16x32_bf16 v[108:111], v[48:51], v[72:75], v[108:111]\n"
    "  ds_write_b128 v14, v[116:119] offset:3072\n"
    "  s_waitcnt lgkmcnt(2)\n"
    "  v_mfma_f32_16x16x32_bf16 v[108:111], v[92:95], v[88:91], v[108:111]\n"
    "  v_mfma_f32_16x16x32_bf16 v[104:107], v[80:83], v[72:75], v[104:107]\n"
    "  ds_write_b128 v14, v[112:115] offset:2048\n"
    "  v_mfma_f32_16x16x32_bf16 v[104:107], v[48:51], v[88:91], v[104:107]\n"
    "  s_waitcnt lgkmcnt(2)\n"
    "  v_mfma_f32_16x16x32_bf16 v[104:107], v[92:95], v[68:71], v[104:107]\n"
    "  v_mfma_f32_16x16x32_bf16 v[16:19], v[96:99], v[56:59], v[16:19]\n"
    "  s_nop 1\n"
    "  ds_write_b128 v14, v[108:111] offset:1024\n"
    "  v_mfma_f32_16x16x32_bf16 v[20:23], v[76:79], v[56:59], v[20:23]\n"
    "  v_mfma_f32_16x16x32_bf16 v[16:19], v[76:79], v[84:87], v[16:19]\n"
    "  v_mfma_f32_16x16x32_bf16 v[24:27], v[60:63], v[56:59], v[24:27]\n"
    "  s_nop 0\n"
    "  ds_write_b128 v14, v[104:107]\n"
    "  v_mfma_f32_16x16x32_bf16 v[20:23], v[60:63], v[84:87], v[20:23]\n"
    "  v_mfma_f32_16x16x32_bf16 v[16:19], v[60:63], v[64:67], v[16:19]\n"
    "  v_mfma_f32_16x16x32_bf16 v[28:31], v[80:83], v[56:59], v[28:31]\n"
    "  v_xor_b32 v14, 4096, v14\n"
    "  s_waitcnt lgkmcnt(0)\n"
    "  s_barrier\n"
    "  ds_read_b128 v[104:107], v14\n"
    "  v_mfma_f32_16x16x32_bf16 v[24:27], v[80:83], v[84:87], v[24:27]\n"
    "  ds_read_b128 v[108:111], v14 offset:1024\n"
    "  v_mfma_f32_16x16x32_bf16 v[20:23], v[80:83], v[64:67], v[20:23]\n"
    "  ds_read_b128 v[112:115], v14 offset:2048\n"
    "  v_mfma_f32_16x16x32_bf16 v[16:19], v[80:83], v[100:103], v[16:19]\n"
    "  ds_read_b128 v[116:119], v14 offset:3072\n"
    "  v_mfma_f32_16x16x32_bf16 v[28:31], v[48:51], v[84:87], v[28:31]\n"
    "  v_mfma_f32_16x16x32_bf16 v[24:27], v[48:51], v[64:67], v[24:27]\n"
    "  s_waitcnt lgkmcnt(3)\n"
    "  v_add_f32 v32, v32, v104\n"
    "  v_add_f32 v33, v33, v105\n"
    "  v_mfma_f32_16x16x32_bf16 v[20:23], v[48:51], v[100:103], v[20:23]\n"
    "  v_add_f32 v34, v34, v106\n"
    "  v_add_f32 v35, v35, v107\n"
    "  v_mfma_f32_16x16x32_bf16 v[16:19], v[48:51], v[52:55], v[16:19]\n"
    "  s_waitcnt lgkmcnt(2)\n"
    "  v_add_f32 v36, v36, v108\n"
    "  v_add_f32 v37, v37, v109\n"
    "  v_mfma_f32_16x16x32_bf16 v[28:31], v[92:95], v[64:67], v[28:31]\n"
    "  v_add_f32 v38, v38, v110\n"
    "  v_add_f32 v39, v39, v111\n"
    "  v_mfma_f32_16x16x32_bf16 v[24:27], v[92:95], v[100:103], v[24:27]\n"
    "  s_waitcnt lgkmcnt(1)\n"
    "  v_add_f32 v40, v40, v112\n"
    "  v_add_f32 v41, v41, v113\n"
    "  v_mfma_f32_16x16x32_bf16 v[20:23], v[92:95], v[52:55], v[20:23]\n"
    "  v_add_f32 v42, v42, v114\n"
    "  v_add_f32 v43, v43, v115\n"
    "  v_mfma_f32_16x16x32_bf16 v[16:19], v[92:95], v[72:75], v[16:19]\n"
    "  s_waitcnt lgkmcnt(0)\n"
    "  v_add_f32 v44, v44, v116\n"
    "  v_add_f32 v45, v45, v117\n"
    "  v_add_f32 v46, v46, v118\n"
    "  v_add_f32 v47, v47, v119\n"
    "Lbj_47%=:\n"
    "  s_waitcnt vmcnt(0)\n"
    "  s_barrier\n"
    "  v_pk_add_f32 v[24:25], v[24:25], v[40:41]\n"
    "  v_pk_add_f32 v[26:27], v[26:27], v[42:43]\n"
    "  v_pk_add_f32 v[28:29], v[28:29], v[44:45]\n"
    "  v_pk_add_f32 v[30:31], v[30:31], v[46:47]\n"
    "  v_cvt_pk_bf16_f32 v52, v24, v25\n"
    "  v_cvt_pk_bf16_f32 v53, v26, v27\n"
    "  v_cvt_pk_bf16_f32 v54, v28, v29\n"
    "  v_cvt_pk_bf16_f32 v55, v30, v31\n"
    "  s_nop 0\n"
    "  v_permlane16_swap_b32 v52, v54\n"
    "  v_permlane16_swap_b32 v53, v55\n"
    "  buffer_store_dwordx4 v[52:55], v13, s[16:19], s33 offen sc1\n"
    "  v_pk_add_f32 v[16:17], v[16:17], v[32:33]\n"
    "  v_pk_add_f32 v[18:19], v[18:19], v[34:35]\n"
    "  v_pk_add_f32 v[20:21], v[20:21], v[36:37]\n"
    "  v_pk_add_f32 v[22:23], v[22:23], v[38:39]\n"
    "  v_cvt_pk_bf16_f32 v48, v16, v17\n"
    "  v_cvt_pk_bf16_f32 v49, v18, v19\n"
    "  v_cvt_pk_bf16_f32 v50, v20, v21\n"
    "  v_cvt_pk_bf16_f32 v51, v22, v23\n"
    "  s_nop 0\n"
    "  v_permlane16_swap_b32 v48, v50\n"
    "  v_permlane16_swap_b32 v49, v51\n"
    "  buffer_store_dwordx4 v[48:51], v13, s[16:19], s32 offen sc1\n"
    "  s_waitcnt lgkmcnt(0)\n"
    "  s_barrier\n"
    "  v_lshrrev_b32 v14, 3, v1\n"
    "  v_and_b32 v13, 7, v1\n"
    "  v_bfe_u32 v12, v1, 4, 1\n"
    "  v_lshlrev_b32 v12, 2, v12\n"
    "  v_xor_b32 v12, v13, v12\n"
    "  v_lshlrev_b32 v12, 4, v12\n"
    "  v_lshl_add_u32 v10, v14, 7, v12\n"
    "  v_xor_b32 v15, 32, v10\n"
    "  v_lshrrev_b32 v14, 4, v1\n"
    "  v_lshlrev_b32 v14, 13, v14\n"
    "  v_and_b32 v13, 15, v1\n"
    "  v_lshlrev_b32 v13, 7, v13\n"
    "  s_mov_b32 s67, 0x70\n"
    "  v_add3_u32 v11, v14, v13, s67\n"
    "  s_lshl_b32 s67, s41, 16\n"
    "  s_lshl_b32 s66, s40, 11\n"
    "  s_add_u32 s66, s67, s66\n"
    "  s_add_u32 s48, s66, 0x1000000\n"
    "  s_lshl_b32 s49, s40, 11\n"
    "  s_sleep 20\n"
    "  s_add_u32 s52, s45, 0\n"
    "  s_and_b32 s52, s52, 7\n"
    "  s_lshl_b32 s52, s52, 13\n"
    "  s_add_u32 m0, s49, 0x00000000\n"
    "  s_add_u32 s52, s52, s48\n"
    "  buffer_load_dwordx4 v10, s[16:19], s52 offen sc1 lds\n"
    "  buffer_load_dwordx4 v15, s[16:19], s52 offen offset:1024 sc1 lds\n"
    "  s_add_u32 s52, s45, 1\n"
    "  s_and_b32 s52, s52, 7\n"
    "  s_lshl_b32 s52, s52, 13\n"
    "  s_add_u32 m0, s49, 0x00002000\n"
    "  s_add_u32 s52, s52, s48\n"
    "  buffer_load_dwordx4 v10, s[16:19], s52 offen sc1 lds\n"
    "  buffer_load_dwordx4 v15, s[16:19], s52 offen offset:1024 sc1 lds\n"
    "  s_add_u32 s52, s45, 2\n"
    "  s_and_b32 s52, s52, 7\n"
    "  s_lshl_b32 s52, s52, 13\n"
    "  s_add_u32 m0, s49, 0x00004000\n"
    "  s_add_u32 s52, s52, s48\n"
    "  buffer_load_dwordx4 v10, s[16:19], s52 offen sc1 lds\n"
    "  buffer_load_dwordx4 v15, s[16:19], s52 offen offset:1024 sc1 lds\n"
    "  s_add_u32 s52, s45, 3\n"
    "  s_and_b32 s52, s52, 7\n"
    "  s_lshl_b32 s52, s52, 13\n"
    "  s_add_u32 m0, s49, 0x00006000\n"
    "  s_add_u32 s52, s52, s48\n"
    "  buffer_load_dwordx4 v10, s[16:19], s52 offen sc1 lds\n"
    "  buffer_load_dwordx4 v15, s[16:19], s52 offen offset:1024 sc1 lds\n"
    "  s_add_u32 s52, s45, 4\n"
    "  s_and_b32 s52, s52, 7\n"
    "  s_lshl_b32 s52, s52, 13\n"
    "  s_add_u32 m0, s49, 0x00008000\n"
    "  s_add_u32 s52, s52, s48\n"
    "  buffer_load_dwordx4 v10, s[16:19], s52 offen sc1 lds\n"
    "  buffer_load_dwordx4 v15, s[16:19], s52 offen offset:1024 sc1 lds\n"
    "  s_add_u32 s52, s45, 5\n"
    "  s_and_b32 s52, s52, 7\n"
    "  s_lshl_b32 s52, s52, 13\n"
    "  s_add_u32 m0, s49, 0x0000a000\n"
    "  s_add_u32 s52, s52, s48\n"
    "  buffer_load_dwordx4 v10, s[16:19], s52 offen sc1 lds\n"
    "  buffer_load_dwordx4 v15, s[16:19], s52 offen offset:1024 sc1 lds\n"
    "  s_add_u32 s52, s45, 6\n"
    "  s_and_b32 s52, s52, 7\n"
    "  s_lshl_b32 s52, s52, 13\n"
    "  s_add_u32 m0, s49, 0x0000c000\n"
    "  s_add_u32 s52, s52, s48\n"
    "  buffer_load_dwordx4 v10, s[16:19], s52 offen sc1 lds\n"
    "  buffer_load_dwordx4 v15, s[16:19], s52 offen offset:1024 sc1 lds\n"
    "  s_add_u32 s52, s45, 7\n"
    "  s_and_b32 s52, s52, 7\n"
    "  s_lshl_b32 s52, s52, 13\n"
    "  s_add_u32 m0, s49, 0x0000e000\n"
    "  s_add_u32 s52, s52, s48\n"
    "  buffer_load_dwordx4 v10, s[16:19], s52 offen sc1 lds\n"
    "  buffer_load_dwordx4 v15, s[16:19], s52 offen offset:1024 sc1 lds\n"
    "  v_and_b32 v126, 3, v1\n"
    "  v_lshrrev_b32 v119, 4, v1\n"
    "  v_bfe_u32 v118, v1, 2, 2\n"
    "  v_lshl_add_u32 v117, v119, 3, v118\n"
    "  s_and_b32 s67, s40, 3\n"
    "  v_lshrrev_b32 v118, 1, v118\n"
    "  v_and_b32 v119, 1, v119\n"
    "  v_lshl_add_u32 v118, v118, 1, v119\n"
    "  v_xor_b32 v118, s67, v118\n"
    "  v_lshlrev_b32 v118, 5, v118\n"
    "  v_lshl_add_u32 v118, v117, 7, v118\n"
    "  v_lshl_add_u32 v120, v126, 3, v118\n"
    "  v_and_b32 v126, 3, v1\n"
    "  v_lshrrev_b32 v119, 4, v1\n"
    "  v_bfe_u32 v118, v1, 2, 2\n"
    "  v_lshl_add_u32 v117, v119, 3, v118\n"
    "  s_add_u32 s67, s40, 1\n"
    "  s_and_b32 s67, s67, 3\n"
    "  v_lshrrev_b32 v118, 1, v118\n"
    "  v_and_b32 v119, 1, v119\n"
    "  v_lshl_add_u32 v118, v118, 1, v119\n"
    "  v_xor_b32 v118, s67, v118\n"
    "  v_lshlrev_b32 v118, 5, v118\n"
    "  v_lshl_add_u32 v117, v117, 7, v118\n"
    "  v_lshl_add_u32 v121, v126, 3, v117\n"
    "  v_and_b32 v126, 3, v1\n"
    "  v_lshrrev_b32 v119, 4, v1\n"
    "  v_bfe_u32 v118, v1, 2, 2\n"
    "  v_lshl_add_u32 v117, v119, 3, v118\n"
    "  s_add_u32 s67, s40, 2\n"
    "  s_and_b32 s67, s67, 3\n"
    "  v_lshrrev_b32 v118, 1, v118\n"
    "  v_and_b32 v119, 1, v119\n"
    "  v_lshl_add_u32 v118, v118, 1, v119\n"
    "  v_xor_b32 v118, s67, v118\n"
    "  v_lshlrev_b32 v118, 5, v118\n"
    "  v_lshl_add_u32 v117, v117, 7, v118\n"
    "  v_lshl_add_u32 v122, v126, 3, v117\n"
    "  v_and_b32 v126, 3, v1\n"
    "  v_lshrrev_b32 v119, 4, v1\n"
    "  v_bfe_u32 v118, v1, 2, 2\n"
    "  v_lshl_add_u32 v117, v119, 3, v118\n"
    "  s_add_u32 s67, s40, 3\n"
    "  s_and_b32 s67, s67, 3\n"
    "  v_lshrrev_b32 v118, 1, v118\n"
    "  v_and_b32 v119, 1, v119\n"
    "  v_lshl_add_u32 v119, v118, 1, v119\n"
    "  v_xor_b32 v119, s67, v119\n"
    "  v_lshlrev_b32 v119, 5, v119\n"
    "  v_lshl_add_u32 v119, v117, 7, v119\n"
    "  v_lshl_add_u32 v123, v126, 3, v119\n"
    "  s_lshl_b32 s67, s40, 11\n"
    "  v_add_u32 v124, s67, v2\n"
    "  v_mov_b32 v112, 0x3f803f80\n"
    "  v_mov_b32 v113, 0x3f803f80\n"
    "  v_mov_b32 v114, 0x3f803f80\n"
    "  v_mov_b32 v115, 0x3f803f80\n"
    "  s_waitcnt vmcnt(14)\n"
    "  ds_read_b128 v[116:119], v124\n"
    "  ds_read_b128 v[56:59], v124 offset:1024\n"
    "  s_waitcnt lgkmcnt(0)\n"
    "  v_max_u32 v116, v116, v56\n"
    "  v_cmp_eq_u32 vcc, -1, v116\n"
    "  s_cbranch_vccz Lxk_48%=\n"
    "  s_mov_b32 s55, 0x00008000\n"
    "Lxr_49%=:\n"
    "  s_add_u32 s52, s45, 0\n"
    "  s_and_b32 s52, s52, 7\n"
    "  s_lshl_b32 s52, s52, 13\n"
    "  s_add_u32 m0, s49, 0x00000000\n"
    "  s_add_u32 s52, s52, s48\n"
    "  buffer_load_dwordx4 v10, s[16:19], s52 offen sc1 lds\n"
    "  buffer_load_dwordx4 v15, s[16:19], s52 offen offset:1024 sc1 lds\n"
    "  s_waitcnt vmcnt(0)\n"
    "  ds_read_b128 v[116:119], v124\n"
    "  ds_read_b128 v[56:59], v124 offset:1024\n"
    "  s_waitcnt lgkmcnt(0)\n"
    "  v_max_u32 v116, v116, v56\n"
    "  v_cmp_eq_u32 vcc, -1, v116\n"
    "  s_cbranch_vccz Lxk_48%=\n"
    "  s_sub_u32 s55, s55, 1\n"
    "  s_cbranch_scc0 Lxr_49%=\n"
    "Lxk_48%=:\n"
    "  v_mov_b32 v60, -1\n"
    "  v_mov_b32 v61, -1\n"
    "  v_mov_b32 v62, -1\n"
    "  v_mov_b32 v63, -1\n"
    "  s_waitcnt lgkmcnt(0)\n"
    "  s_barrier\n"
    "  ds_read_b64_tr_b16 v[64:65], v120\n"
    "  ds_read_b64_tr_b16 v[66:67], v120 offset:512\n"
    "  ds_read_b64_tr_b16 v[68:69], v121\n"
    "  ds_read_b64_tr_b16 v[70:71], v121 offset:512\n"
    "  ds_read_b64_tr_b16 v[72:73], v122\n"
    "  ds_read_b64_tr_b16 v[74:75], v122 offset:512\n"
    "  ds_read_b64_tr_b16 v[76:77], v123\n"
    "  ds_read_b64_tr_b16 v[78:79], v123 offset:512\n"
    "  s_waitcnt vmcnt(12)\n"
    "  ds_read_b128 v[116:119], v124 offset:8192\n"
    "  ds_read_b128 v[56:59], v124 offset:9216\n"
    "  ds_read_b64_tr_b16 v[80:81], v120 offset:4096\n"
    "  ds_read_b64_tr_b16 v[82:83], v120 offset:4608\n"
    "  ds_read_b64_tr_b16 v[84:85], v121 offset:4096\n"
    "  ds_read_b64_tr_b16 v[86:87], v121 offset:4608\n"
    "  ds_read_b64_tr_b16 v[88:89], v122 offset:4096\n"
    "  ds_read_b64_tr_b16 v[90:91], v122 offset:4608\n"
    "  ds_read_b64_tr_b16 v[92:93], v123 offset:4096\n"
    "  ds_read_b64_tr_b16 v[94:95], v123 offset:4608\n"
    "  s_waitcnt lgkmcnt(8)\n"
    "  v_max_u32 v116, v116, v56\n"
    "  v_cmp_eq_u32 vcc, -1, v116\n"
    "  s_cbranch_vccz Lxk_50%=\n"
    "  s_mov_b32 s55, 0x00008000\n"
    "Lxr_51%=:\n"
    "  s_add_u32 s52, s45, 1\n"
    "  s_and_b32 s52, s52, 7\n"
    "  s_lshl_b32 s52, s52, 13\n"
    "  s_add_u32 m0, s49, 0x00002000\n"
    "  s_add_u32 s52, s52, s48\n"
    "  buffer_load_dwordx4 v10, s[16:19], s52 offen sc1 lds\n"
    "  buffer_load_dwordx4 v15, s[16:19], s52 offen offset:1024 sc1 lds\n"
    "  s_waitcnt vmcnt(0)\n"
    "  ds_read_b128 v[116:119], v124 offset:8192\n"
    "  ds_read_b128 v[56:59], v124 offset:9216\n"
    "  s_waitcnt lgkmcnt(0)\n"
    "  v_max_u32 v116, v116, v56\n"
    "  v_cmp_eq_u32 vcc, -1, v116\n"
    "  s_cbranch_vccz Lxk_50%=\n"
    "  s_sub_u32 s55, s55, 1\n"
    "  s_cbranch_scc0 Lxr_51%=\n"
    "Lxk_50%=:\n"
    "  s_waitcnt lgkmcnt(0)\n"
    "  s_barrier\n"
    "  v_mfma_f32_16x16x32_bf16 v[16:19], v[64:67], a[0:3], 0\n"
    "  ds_read_b64_tr_b16 v[96:97], v120 offset:8192\n"
    "  v_mfma_f32_16x16x32_bf16 v[32:35], v[64:67], a[4:7], 0\n"
    "  ds_read_b64_tr_b16 v[98:99], v120 offset:8704\n"
    "  v_mfma_f32_16x16x32_bf16 v[20:23], v[68:71], a[0:3], 0\n"
    "  ds_read_b64_tr_b16 v[100:101], v121 offset:8192\n"
    "  v_mfma_f32_16x16x32_bf16 v[36:39], v[68:71], a[4:7], 0\n"
    "  ds_read_b64_tr_b16 v[102:103], v121 offset:8704\n"
    "  v_mfma_f32_16x16x32_bf16 v[24:27], v[72:75], a[0:3], 0\n"
    "  ds_read_b64_tr_b16 v[104:105], v122 offset:8192\n"
    "  v_mfma_f32_16x16x32_bf16 v[40:43], v[72:75], a[4:7], 0\n"
    "  ds_read_b64_tr_b16 v[106:107], v122 offset:8704\n"
    "  v_mfma_f32_16x16x32_bf16 v[28:31], v[76:79], a[0:3], 0\n"
    "  ds_read_b64_tr_b16 v[108:109], v123 offset:8192\n"
    "  v_mfma_f32_16x16x32_bf16 v[44:47], v[76:79], a[4:7], 0\n"
    "  ds_read_b64_tr_b16 v[110:111], v123 offset:8704\n"
    "  v_mfma_f32_16x16x32_bf16 v[48:51], v[64:67], v[64:67], 0\n"
    "  s_waitcnt vmcnt(10)\n"
    "  ds_read_b128 v[116:119], v124 offset:16384\n"
    "  ds_read_b128 v[56:59], v124 offset:17408\n"
    "  v_mfma_f32_16x16x32_bf16 v[52:55], v[64:67], v[112:115], 0\n"
    "  v_mfma_f32_16x16x32_bf16 v[16:19], v[80:83], a[8:11], v[16:19]\n"
    "  ds_read_b64_tr_b16 v[64:65], v120 offset:12288\n"
    "  v_mfma_f32_16x16x32_bf16 v[32:35], v[80:83], a[12:15], v[32:35]\n"
    "  ds_read_b64_tr_b16 v[66:67], v120 offset:12800\n"
    "  v_mfma_f32_16x16x32_bf16 v[20:23], v[84:87], a[8:11], v[20:23]\n"
    "  ds_read_b64_tr_b16 v[68:69], v121 offset:12288\n"
    "  v_mfma_f32_16x16x32_bf16 v[36:39], v[84:87], a[12:15], v[36:39]\n"
    "  ds_read_b64_tr_b16 v[70:71], v121 offset:12800\n"
    "  v_mfma_f32_16x16x32_bf16 v[24:27], v[88:91], a[8:11], v[24:27]\n"
    "  ds_read_b64_tr_b16 v[72:73], v122 offset:12288\n"
    "  v_mfma_f32_16x16x32_bf16 v[40:43], v[88:91], a[12:15], v[40:43]\n"
    "  ds_read_b64_tr_b16 v[74:75], v122 offset:12800\n"
    "  v_mfma_f32_16x16x32_bf16 v[28:31], v[92:95], a[8:11], v[28:31]\n"
    "  ds_read_b64_tr_b16 v[76:77], v123 offset:12288\n"
    "  v_mfma_f32_16x16x32_bf16 v[44:47], v[92:95], a[12:15], v[44:47]\n"
    "  ds_read_b64_tr_b16 v[78:79], v123 offset:12800\n"
    "  v_mfma_f32_16x16x32_bf16 v[48:51], v[80:83], v[80:83], v[48:51]\n"
    "  v_mfma_f32_16x16x32_bf16 v[52:55], v[80:83], v[112:115], v[52:55]\n"
    "  s_waitcnt lgkmcnt(8)\n"
    "  v_max_u32 v116, v116, v56\n"
    "  v_cmp_eq_u32 vcc, -1, v116\n"
    "  s_cbranch_vccz Lxk_52%=\n"
    "  s_mov_b32 s55, 0x00008000\n"
    "Lxr_53%=:\n"
    "  s_add_u32 s52, s45, 2\n"
    "  s_and_b32 s52, s52, 7\n"
    "  s_lshl_b32 s52, s52, 13\n"
    "  s_add_u32 m0, s49, 0x00004000\n"
    "  s_add_u32 s52, s52, s48\n"
    "  buffer_load_dwordx4 v10, s[16:19], s52 offen sc1 lds\n"
    "  buffer_load_dwordx4 v15, s[16:19], s52 offen offset:1024 sc1 lds\n"
    "  s_waitcnt vmcnt(0)\n"
    "  ds_read_b128 v[116:119], v124 offset:16384\n"
    "  ds_read_b128 v[56:59], v124 offset:17408\n"
    "  s_waitcnt lgkmcnt(0)\n"
    "  v_max_u32 v116, v116, v56\n"
    "  v_cmp_eq_u32 vcc, -1, v116\n"
    "  s_cbranch_vccz Lxk_52%=\n"
    "  s_sub_u32 s55, s55, 1\n"
    "  s_cbranch_scc0 Lxr_53%=\n"
    "Lxk_52%=:\n"
    "  s_waitcnt lgkmcnt(0)\n"
    "  s_barrier\n"
    "  v_mfma_f32_16x16x32_bf16 v[16:19], v[96:99], a[16:19], v[16:19]\n"
    "  ds_read_b64_tr_b16 v[80:81], v120 offset:16384\n"
    "  v_mfma_f32_16x16x32_bf16 v[32:35], v[96:99], a[20:23], v[32:35]\n"
    "  ds_read_b64_tr_b16 v[82:83], v120 offset:16896\n"
    "  v_mfma_f32_16x16x32_bf16 v[20:23], v[100:103], a[16:19], v[20:23]\n"
    "  ds_read_b64_tr_b16 v[84:85], v121 offset:16384\n"
    "  v_mfma_f32_16x16x32_bf16 v[36:39], v[100:103], a[20:23], v[36:39]\n"
    "  ds_read_b64_tr_b16 v[86:87], v121 offset:16896\n"
    "  v_mfma_f32_16x16x32_bf16 v[24:27], v[104:107], a[16:19], v[24:27]\n"
    "  ds_read_b64_tr_b16 v[88:89], v122 offset:16384\n"
    "  v_mfma_f32_16x16x32_bf16 v[40:43], v[104:107], a[20:23], v[40:43]\n"
    "  ds_read_b64_tr_b16 v[90:91], v122 offset:16896\n"
    "  v_mfma_f32_16x16x32_bf16 v[28:31], v[108:111], a[16:19], v[28:31]\n"
    "  ds_read_b64_tr_b16 v[92:93], v123 offset:16384\n"
    "  v_mfma_f32_16x16x32_bf16 v[44:47], v[108:111], a[20:23], v[44:47]\n"
    "  ds_read_b64_tr_b16 v[94:95], v123 offset:16896\n"
    "  v_mfma_f32_16x16x32_bf16 v[48:51], v[96:99], v[96:99], v[48:51]\n"
    "  s_waitcnt vmcnt(8)\n"
    "  ds_read_b128 v[116:119], v124 offset:24576\n"
    "  ds_read_b128 v[56:59], v124 offset:25600\n"
    "  v_mfma_f32_16x16x32_bf16 v[52:55], v[96:99], v[112:115], v[52:55]\n"
    "  v_mfma_f32_16x16x32_bf16 v[16:19], v[64:67], a[24:27], v[16:19]\n"
    "  ds_read_b64_tr_b16 v[96:97], v120 offset:20480\n"
    "  v_mfma_f32_16x16x32_bf16 v[32:35], v[64:67], a[28:31], v[32:35]\n"
    "  ds_read_b64_tr_b16 v[98:99], v120 offset:20992\n"
    "  v_mfma_f32_16x16x32_bf16 v[20:23], v[68:71], a[24:27], v[20:23]\n"
    "  ds_read_b64_tr_b16 v[100:101], v121 offset:20480\n"
    "  v_mfma_f32_16x16x32_bf16 v[36:39], v[68:71], a[28:31], v[36:39]\n"
    "  ds_read_b64_tr_b16 v[102:103], v121 offset:20992\n"
    "  v_mfma_f32_16x16x32_bf16 v[24:27], v[72:75], a[24:27], v[24:27]\n"
    "  ds_read_b64_tr_b16 v[104:105], v122 offset:20480\n"
    "  v_mfma_f32_16x16x32_bf16 v[40:43], v[72:75], a[28:31], v[40:43]\n"
    "  ds_read_b64_tr_b16 v[106:107], v122 offset:20992\n"
    "  v_mfma_f32_16x16x32_bf16 v[28:31], v[76:79], a[24:27], v[28:31]\n"
    "  ds_read_b64_tr_b16 v[108:109], v123 offset:20480\n"
    "  v_mfma_f32_16x16x32_bf16 v[44:47], v[76:79], a[28:31], v[44:47]\n"
    "  ds_read_b64_tr_b16 v[110:111], v123 offset:20992\n"
    "  v_mfma_f32_16x16x32_bf16 v[48:51], v[64:67], v[64:67], v[48:51]\n"
    "  v_mfma_f32_16x16x32_bf16 v[52:55], v[64:67], v[112:115], v[52:55]\n"
    "  s_waitcnt lgkmcnt(8)\n"
    "  v_max_u32 v116, v116, v56\n"
    "  v_cmp_eq_u32 vcc, -1, v116\n"
    "  s_cbranch_vccz Lxk_54%=\n"
    "  s_mov_b32 s55, 0x00008000\n"
    "Lxr_55%=:\n"
    "  s_add_u32 s52, s45, 3\n"
    "  s_and_b32 s52, s52, 7\n"
    "  s_lshl_b32 s52, s52, 13\n"
    "  s_add_u32 m0, s49, 0x00006000\n"
    "  s_add_u32 s52, s52, s48\n"
    "  buffer_load_dwordx4 v10, s[16:19], s52 offen sc1 lds\n"
    "  buffer_load_dwordx4 v15, s[16:19], s52 offen offset:1024 sc1 lds\n"
    "  s_waitcnt vmcnt(0)\n"
    "  ds_read_b128 v[116:119], v124 offset:24576\n"
    "  ds_read_b128 v[56:59], v124 offset:25600\n"
    "  s_waitcnt lgkmcnt(0)\n"
    "  v_max_u32 v116, v116, v56\n"
    "  v_cmp_eq_u32 vcc, -1, v116\n"
    "  s_cbranch_vccz Lxk_54%=\n"
    "  s_sub_u32 s55, s55, 1\n"
    "  s_cbranch_scc0 Lxr_55%=\n"
    "Lxk_54%=:\n"
    "  s_waitcnt lgkmcnt(0)\n"
    "  s_barrier\n"
    "  v_mfma_f32_16x16x32_bf16 v[16:19], v[80:83], a[32:35], v[16:19]\n"
    "  ds_read_b64_tr_b16 v[64:65], v120 offset:24576\n"
    "  v_mfma_f32_16x16x32_bf16 v[32:35], v[80:83], a[36:39], v[32:35]\n"
    "  ds_read_b64_tr_b16 v[66:67], v120 offset:25088\n"
    "  v_mfma_f32_16x16x32_bf16 v[20:23], v[84:87], a[32:35], v[20:23]\n"
    "  ds_read_b64_tr_b16 v[68:69], v121 offset:24576\n"
    "  v_mfma_f32_16x16x32_bf16 v[36:39], v[84:87], a[36:39], v[36:39]\n"
    "  ds_read_b64_tr_b16 v[70:71], v121 offset:25088\n"
    "  v_mfma_f32_16x16x32_bf16 v[24:27], v[88:91], a[32:35], v[24:27]\n"
    "  ds_read_b64_tr_b16 v[72:73], v122 offset:24576\n"
    "  v_mfma_f32_16x16x32_bf16 v[40:43], v[88:91], a[36:39], v[40:43]\n"
    "  ds_read_b64_tr_b16 v[74:75], v122 offset:25088\n"
    "  v_mfma_f32_16x16x32_bf16 v[28:31], v[92:95], a[32:35], v[28:31]\n"
    "  ds_read_b64_tr_b16 v[76:77], v123 offset:24576\n"
    "  v_mfma_f32_16x16x32_bf16 v[44:47], v[92:95], a[36:39], v[44:47]\n"
    "  ds_read_b64_tr_b16 v[78:79], v123 offset:25088\n"
    "  v_mfma_f32_16x16x32_bf16 v[48:51], v[80:83], v[80:83], v[48:51]\n"
    "  s_waitcnt vmcnt(6)\n"
    "  ds_read_b128 v[116:119], v124 offset:32768\n"
    "  ds_read_b128 v[56:59], v124 offset:33792\n"
    "  v_mfma_f32_16x16x32_bf16 v[52:55], v[80:83], v[112:115], v[52:55]\n"
    "  v_mfma_f32_16x16x32_bf16 v[16:19], v[96:99], a[40:43], v[16:19]\n"
    "  ds_read_b64_tr_b16 v[80:81], v120 offset:28672\n"
    "  ds_read_b64_tr_b16 v[82:83], v120 offset:29184\n"
    "  v_mfma_f32_16x16x32_bf16 v[32:35], v[96:99], a[44:47], v[32:35]\n"
    "  ds_read_b64_tr_b16 v[84:85], v121 offset:28672\n"
    "  ds_read_b64_tr_b16 v[86:87], v121 offset:29184\n"
    "  v_mfma_f32_16x16x32_bf16 v[20:23], v[100:103], a[40:43], v[20:23]\n"
    "  ds_read_b64_tr_b16 v[88:89], v122 offset:28672\n"
    "  ds_read_b64_tr_b16 v[90:91], v122 offset:29184\n"
    "  v_mfma_f32_16x16x32_bf16 v[36:39], v[100:103], a[44:47], v[36:39]\n"
    "  ds_read_b64_tr_b16 v[92:93], v123 offset:28672\n"
    "  ds_read_b64_tr_b16 v[94:95], v123 offset:29184\n"
    "  v_mfma_f32_16x16x32_bf16 v[24:27], v[104:107], a[40:43], v[24:27]\n"
    "  s_lshl_b32 s67, s44, 6\n"
    "  s_lshl4_add_u32 s67, s40, s67\n"
    "  v_mfma_f32_16x16x32_bf16 v[40:43], v[104:107], a[44:47], v[40:43]\n"
    "  s_lshl_b32 s67, s67, 13\n"
    "  s_lshl_b32 s66, s43, 7\n"
    "  v_mfma_f32_16x16x32_bf16 v[28:31], v[108:111], a[40:43], v[28:31]\n"
    "  s_add_u32 s66, s67, s66\n"
    "  s_add_u32 s34, s66, 0xe00000\n"
    "  v_mfma_f32_16x16x32_bf16 v[44:47], v[108:111], a[44:47], v[44:47]\n"
    "  v_lshrrev_b32 v11, 3, v1\n"
    "  v_bfe_u32 v125, v11, 1, 1\n"
    "  v_mfma_f32_16x16x32_bf16 v[48:51], v[96:99], v[96:99], v[48:51]\n"
    "  v_lshlrev_b32 v125, 2, v125\n"
    "  v_lshrrev_b32 v14, 5, v1\n"
    "  v_mfma_f32_16x16x32_bf16 v[52:55], v[96:99], v[112:115], v[52:55]\n"
    "  v_lshlrev_b32 v14, 1, v14\n"
    "  v_and_b32 v11, 3, v11\n"
    "  v_and_b32 v11, 1, v11\n"
    "  v_add3_u32 v125, v125, v14, v11\n"
    "  v_and_b32 v14, 7, v1\n"
    "  v_lshlrev_b32 v14, 4, v14\n"
    "  v_lshl_add_u32 v12, v125, 13, v14\n"
    "  buffer_store_dwordx4 v[60:63], v12, s[16:19], s34 offen sc1\n"
    "  s_waitcnt lgkmcnt(8)\n"
    "  v_max_u32 v116, v116, v56\n"
    "  v_cmp_eq_u32 vcc, -1, v116\n"
    "  s_cbranch_vccz Lxk_56%=\n"
    "  s_mov_b32 s55, 0x00008000\n"
    "Lxr_57%=:\n"
    "  s_add_u32 s52, s45, 4\n"
    "  s_and_b32 s52, s52, 7\n"
    "  s_lshl_b32 s52, s52, 13\n"
    "  s_add_u32 m0, s49, 0x00008000\n"
    "  s_add_u32 s52, s52, s48\n"
    "  buffer_load_dwordx4 v10, s[16:19], s52 offen sc1 lds\n"
    "  buffer_load_dwordx4 v15, s[16:19], s52 offen offset:1024 sc1 lds\n"
    "  s_waitcnt vmcnt(0)\n"
    "  ds_read_b128 v[116:119], v124 offset:32768\n"
    "  ds_read_b128 v[56:59], v124 offset:33792\n"
    "  s_waitcnt lgkmcnt(0)\n"
    "  v_max_u32 v116, v116, v56\n"
    "  v_cmp_eq_u32 vcc, -1, v116\n"
    "  s_cbranch_vccz Lxk_56%=\n"
    "  s_sub_u32 s55, s55, 1\n"
    "  s_cbranch_scc0 Lxr_57%=\n"
    "Lxk_56%=:\n"
    "  s_waitcnt lgkmcnt(0)\n"
    "  s_barrier\n"
    "  v_mfma_f32_16x16x32_bf16 v[16:19], v[64:67], a[48:51], v[16:19]\n"
    "  ds_read_b64_tr_b16 v[96:97], v120 offset:32768\n"
    "  v_mfma_f32_16x16x32_bf16 v[32:35], v[64:67], a[52:55], v[32:35]\n"
    "  ds_read_b64_tr_b16 v[98:99], v120 offset:33280\n"
    "  v_mfma_f32_16x16x32_bf16 v[20:23], v[68:71], a[48:51], v[20:23]\n"
    "  ds_read_b64_tr_b16 v[100:101], v121 offset:32768\n"
    "  v_mfma_f32_16x16x32_bf16 v[36:39], v[68:71], a[52:55], v[36:39]\n"
    "  ds_read_b64_tr_b16 v[102:103], v121 offset:33280\n"
    "  v_mfma_f32_16x16x32_bf16 v[24:27], v[72:75], a[48:51], v[24:27]\n"
    "  ds_read_b64_tr_b16 v[104:105], v122 offset:32768\n"
    "  v_mfma_f32_16x16x32_bf16 v[40:43], v[72:75], a[52:55], v[40:43]\n"
    "  ds_read_b64_tr_b16 v[106:107], v122 offset:33280\n"
    "  v_mfma_f32_16x16x32_bf16 v[28:31], v[76:79], a[48:51], v[28:31]\n"
    "  ds_read_b64_tr_b16 v[108:109], v123 offset:32768\n"
    "  v_mfma_f32_16x16x32_bf16 v[44:47], v[76:79], a[52:55], v[44:47]\n"
    "  ds_read_b64_tr_b16 v[110:111], v123 offset:33280\n"
    "  v_mfma_f32_16x16x32_bf16 v[48:51], v[64:67], v[64:67], v[48:51]\n"
    "  s_waitcnt vmcnt(5)\n"
    "  ds_read_b128 v[116:119], v124 offset:40960\n"
    "  ds_read_b128 v[56:59], v124 offset:41984\n"
    "  v_mfma_f32_16x16x32_bf16 v[52:55], v[64:67], v[112:115], v[52:55]\n"
    "  v_mfma_f32_16x16x32_bf16 v[16:19], v[80:83], a[56:59], v[16:19]\n"
    "  ds_read_b64_tr_b16 v[64:65], v120 offset:36864\n"
    "  ds_read_b64_tr_b16 v[66:67], v120 offset:37376\n"
    "  v_mfma_f32_16x16x32_bf16 v[32:35], v[80:83], a[60:63], v[32:35]\n"
    "  ds_read_b64_tr_b16 v[68:69], v121 offset:36864\n"
    "  ds_read_b64_tr_b16 v[70:71], v121 offset:37376\n"
    "  v_mfma_f32_16x16x32_bf16 v[20:23], v[84:87], a[56:59], v[20:23]\n"
    "  ds_read_b64_tr_b16 v[72:73], v122 offset:36864\n"
    "  ds_read_b64_tr_b16 v[74:75], v122 offset:37376\n"
    "  v_mfma_f32_16x16x32_bf16 v[36:39], v[84:87], a[60:63], v[36:39]\n"
    "  ds_read_b64_tr_b16 v[76:77], v123 offset:36864\n"
    "  ds_read_b64_tr_b16 v[78:79], v123 offset:37376\n"
    "  v_mfma_f32_16x16x32_bf16 v[24:27], v[88:91], a[56:59], v[24:27]\n"
    "  s_add_u32 s34, s34, 0x00010000\n"
    "  buffer_store_dwordx4 v[60:63], v12, s[16:19], s34 offen sc1\n"
    "  v_mfma_f32_16x16x32_bf16 v[40:43], v[88:91], a[60:63], v[40:43]\n"
    "  v_mfma_f32_16x16x32_bf16 v[28:31], v[92:95], a[56:59], v[28:31]\n"
    "  v_mfma_f32_16x16x32_bf16 v[44:47], v[92:95], a[60:63], v[44:47]\n"
    "  v_mfma_f32_16x16x32_bf16 v[48:51], v[80:83], v[80:83], v[48:51]\n"
    "  v_mfma_f32_16x16x32_bf16 v[52:55], v[80:83], v[112:115], v[52:55]\n"
    "  s_waitcnt lgkmcnt(8)\n"
    "  v_max_u32 v116, v116, v56\n"
    "  v_cmp_eq_u32 vcc, -1, v116\n"
    "  s_cbranch_vccz Lxk_58%=\n"
    "  s_mov_b32 s55, 0x00008000\n"
    "Lxr_59%=:\n"
    "  s_add_u32 s52, s45, 5\n"
    "  s_and_b32 s52, s52, 7\n"
    "  s_lshl_b32 s52, s52, 13\n"
    "  s_add_u32 m0, s49, 0x0000a000\n"
    "  s_add_u32 s52, s52, s48\n"
    "  buffer_load_dwordx4 v10, s[16:19], s52 offen sc1 lds\n"
    "  buffer_load_dwordx4 v15, s[16:19], s52 offen offset:1024 sc1 lds\n"
    "  s_waitcnt vmcnt(0)\n"
    "  ds_read_b128 v[116:119], v124 offset:40960\n"
    "  ds_read_b128 v[56:59], v124 offset:41984\n"
    "  s_waitcnt lgkmcnt(0)\n"
    "  v_max_u32 v116, v116, v56\n"
    "  v_cmp_eq_u32 vcc, -1, v116\n"
    "  s_cbranch_vccz Lxk_58%=\n"
    "  s_sub_u32 s55, s55, 1\n"
    "  s_cbranch_scc0 Lxr_59%=\n"
    "Lxk_58%=:\n"
    "  s_waitcnt lgkmcnt(0)\n"
    "  s_barrier\n"
    "  v_mfma_f32_16x16x32_bf16 v[16:19], v[96:99], a[64:67], v[16:19]\n"
    "  ds_read_b64_tr_b16 v[80:81], v120 offset:40960\n"
    "  v_mfma_f32_16x16x32_bf16 v[32:35], v[96:99], a[68:71], v[32:35]\n"
    "  ds_read_b64_tr_b16 v[82:83], v120 offset:41472\n"
    "  v_mfma_f32_16x16x32_bf16 v[20:23], v[100:103], a[64:67], v[20:23]\n"
    "  ds_read_b64_tr_b16 v[84:85], v121 offset:40960\n"
    "  v_mfma_f32_16x16x32_bf16 v[36:39], v[100:103], a[68:71], v[36:39]\n"
    "  ds_read_b64_tr_b16 v[86:87], v121 offset:41472\n"
    "  v_mfma_f32_16x16x32_bf16 v[24:27], v[104:107], a[64:67], v[24:27]\n"
    "  ds_read_b64_tr_b16 v[88:89], v122 offset:40960\n"
    "  v_mfma_f32_16x16x32_bf16 v[40:43], v[104:107], a[68:71], v[40:43]\n"
    "  ds_read_b64_tr_b16 v[90:91], v122 offset:41472\n"
    "  v_mfma_f32_16x16x32_bf16 v[28:31], v[108:111], a[64:67], v[28:31]\n"
    "  ds_read_b64_tr_b16 v[92:93], v123 offset:40960\n"
    "  v_mfma_f32_16x16x32_bf16 v[44:47], v[108:111], a[68:71], v[44:47]\n"
    "  ds_read_b64_tr_b16 v[94:95], v123 offset:41472\n"
    "  v_mfma_f32_16x16x32_bf16 v[48:51], v[96:99], v[96:99], v[48:51]\n"
    "  s_waitcnt vmcnt(4)\n"
    "  ds_read_b128 v[116:119], v124 offset:49152\n"
    "  ds_read_b128 v[56:59], v124 offset:50176\n"
    "  v_mfma_f32_16x16x32_bf16 v[52:55], v[96:99], v[112:115], v[52:55]\n"
    "  v_mfma_f32_16x16x32_bf16 v[16:19], v[64:67], a[72:75], v[16:19]\n"
    "  ds_read_b64_tr_b16 v[96:97], v120 offset:45056\n"
    "  ds_read_b64_tr_b16 v[98:99], v120 offset:45568\n"
    "  v_mfma_f32_16x16x32_bf16 v[32:35], v[64:67], a[76:79], v[32:35]\n"
    "  ds_read_b64_tr_b16 v[100:101], v121 offset:45056\n"
    "  ds_read_b64_tr_b16 v[102:103], v121 offset:45568\n"
    "  v_mfma_f32_16x16x32_bf16 v[20:23], v[68:71], a[72:75], v[20:23]\n"
    "  ds_read_b64_tr_b16 v[104:105], v122 offset:45056\n"
    "  ds_read_b64_tr_b16 v[106:107], v122 offset:45568\n"
    "  v_mfma_f32_16x16x32_bf16 v[36:39], v[68:71], a[76:79], v[36:39]\n"
    "  ds_read_b64_tr_b16 v[108:109], v123 offset:45056\n"
    "  ds_read_b64_tr_b16 v[110:111], v123 offset:45568\n"
    "  v_mfma_f32_16x16x32_bf16 v[24:27], v[72:75], a[72:75], v[24:27]\n"
    "  s_lshl_b32 s67, s2, 8\n"
    "  s_add_u32 s34, s67, 0x1200000\n"
    "  v_mfma_f32_16x16x32_bf16 v[40:43], v[72:75], a[76:79], v[40:43]\n"
    "  v_lshrrev_b32 v11, 3, v0\n"
    "  v_and_b32 v125, 7, v0\n"
    "  v_mfma_f32_16x16x32_bf16 v[28:31], v[76:79], a[72:75], v[28:31]\n"
    "  v_lshlrev_b32 v125, 4, v125\n"
    "  v_lshl_add_u32 v13, v11, 16, v125\n"
    "  v_mfma_f32_16x16x32_bf16 v[44:47], v[76:79], a[76:79], v[44:47]\n"
    "  buffer_store_dwordx4 v[60:63], v13, s[16:19], s34 offen sc1\n"
    "  v_mfma_f32_16x16x32_bf16 v[48:51], v[64:67], v[64:67], v[48:51]\n"
    "  v_mfma_f32_16x16x32_bf16 v[52:55], v[64:67], v[112:115], v[52:55]\n"
    "  s_waitcnt lgkmcnt(8)\n"
    "  v_max_u32 v116, v116, v56\n"
    "  v_cmp_eq_u32 vcc, -1, v116\n"
    "  s_cbranch_vccz Lxk_60%=\n"
    "  s_mov_b32 s55, 0x00008000\n"
    "Lxr_61%=:\n"
    "  s_add_u32 s52, s45, 6\n"
    "  s_and_b32 s52, s52, 7\n"
    "  s_lshl_b32 s52, s52, 13\n"
    "  s_add_u32 m0, s49, 0x0000c000\n"
    "  s_add_u32 s52, s52, s48\n"
    "  buffer_load_dwordx4 v10, s[16:19], s52 offen sc1 lds\n"
    "  buffer_load_dwordx4 v15, s[16:19], s52 offen offset:1024 sc1 lds\n"
    "  s_waitcnt vmcnt(0)\n"
    "  ds_read_b128 v[116:119], v124 offset:49152\n"
    "  ds_read_b128 v[56:59], v124 offset:50176\n"
    "  s_waitcnt lgkmcnt(0)\n"
    "  v_max_u32 v116, v116, v56\n"
    "  v_cmp_eq_u32 vcc, -1, v116\n"
    "  s_cbranch_vccz Lxk_60%=\n"
    "  s_sub_u32 s55, s55, 1\n"
    "  s_cbranch_scc0 Lxr_61%=\n"
    "Lxk_60%=:\n"
    "  s_waitcnt lgkmcnt(0)\n"
    "  s_barrier\n"
    "  v_mfma_f32_16x16x32_bf16 v[16:19], v[80:83], a[80:83], v[16:19]\n"
    "  ds_read_b64_tr_b16 v[64:65], v120 offset:49152\n"
    "  v_mfma_f32_16x16x32_bf16 v[32:35], v[80:83], a[84:87], v[32:35]\n"
    "  ds_read_b64_tr_b16 v[66:67], v120 offset:49664\n"
    "  v_mfma_f32_16x16x32_bf16 v[20:23], v[84:87], a[80:83], v[20:23]\n"
    "  ds_read_b64_tr_b16 v[68:69], v121 offset:49152\n"
    "  v_mfma_f32_16x16x32_bf16 v[36:39], v[84:87], a[84:87], v[36:39]\n"
    "  ds_read_b64_tr_b16 v[70:71], v121 offset:49664\n"
    "  v_mfma_f32_16x16x32_bf16 v[24:27], v[88:91], a[80:83], v[24:27]\n"
    "  ds_read_b64_tr_b16 v[72:73], v122 offset:49152\n"
    "  v_mfma_f32_16x16x32_bf16 v[40:43], v[88:91], a[84:87], v[40:43]\n"
    "  ds_read_b64_tr_b16 v[74:75], v122 offset:49664\n"
    "  v_mfma_f32_16x16x32_bf16 v[28:31], v[92:95], a[80:83], v[28:31]\n"
    "  ds_read_b64_tr_b16 v[76:77], v123 offset:49152\n"
    "  v_mfma_f32_16x16x32_bf16 v[44:47], v[92:95], a[84:87], v[44:47]\n"
    "  ds_read_b64_tr_b16 v[78:79], v123 offset:49664\n"
    "  v_mfma_f32_16x16x32_bf16 v[48:51], v[80:83], v[80:83], v[48:51]\n"
    "  s_waitcnt vmcnt(3)\n"
    "  ds_read_b128 v[116:119], v124 offset:57344\n"
    "  ds_read_b128 v[56:59], v124 offset:58368\n"
    "  v_mfma_f32_16x16x32_bf16 v[52:55], v[80:83], v[112:115], v[52:55]\n"
    "  v_mfma_f32_16x16x32_bf16 v[16:19], v[96:99], a[88:91], v[16:19]\n"
    "  ds_read_b64_tr_b16 v[80:81], v120 offset:53248\n"
    "  ds_read_b64_tr_b16 v[82:83], v120 offset:53760\n"
    "  v_mfma_f32_16x16x32_bf16 v[32:35], v[96:99], a[92:95], v[32:35]\n"
    "  ds_read_b64_tr_b16 v[84:85], v121 offset:53248\n"
    "  ds_read_b64_tr_b16 v[86:87], v121 offset:53760\n"
    "  v_mfma_f32_16x16x32_bf16 v[20:23], v[100:103], a[88:91], v[20:23]\n"
    "  ds_read_b64_tr_b16 v[88:89], v122 offset:53248\n"
    "  ds_read_b64_tr_b16 v[90:91], v122 offset:53760\n"
    "  v_mfma_f32_16x16x32_bf16 v[36:39], v[100:103], a[92:95], v[36:39]\n"
    "  ds_read_b64_tr_b16 v[92:93], v123 offset:53248\n"
    "  ds_read_b64_tr_b16 v[94:95], v123 offset:53760\n"
    "  v_mfma_f32_16x16x32_bf16 v[24:27], v[104:107], a[88:91], v[24:27]\n"
    "  buffer_store_dwordx4 v[60:63], v13, s[16:19], s34 offen offset:128 sc1\n"
    "  v_mfma_f32_16x16x32_bf16 v[40:43], v[104:107], a[92:95], v[40:43]\n"
    "  v_mfma_f32_16x16x32_bf16 v[28:31], v[108:111], a[88:91], v[28:31]\n"
    "  v_mfma_f32_16x16x32_bf16 v[44:47], v[108:111], a[92:95], v[44:47]\n"
    "  v_mfma_f32_16x16x32_bf16 v[48:51], v[96:99], v[96:99], v[48:51]\n"
    "  v_mfma_f32_16x16x32_bf16 v[52:55], v[96:99], v[112:115], v[52:55]\n"
    "  s_waitcnt lgkmcnt(8)\n"
    "  v_max_u32 v116, v116, v56\n"
    "  v_cmp_eq_u32 vcc, -1, v116\n"
    "  s_cbranch_vccz Lxk_62%=\n"
    "  s_mov_b32 s55, 0x00008000\n"
    "Lxr_63%=:\n"
    "  s_add_u32 s52, s45, 7\n"
    "  s_and_b32 s52, s52, 7\n"
    "  s_lshl_b32 s52, s52, 13\n"
    "  s_add_u32 m0, s49, 0x0000e000\n"
    "  s_add_u32 s52, s52, s48\n"
    "  buffer_load_dwordx4 v10, s[16:19], s52 offen sc1 lds\n"
    "  buffer_load_dwordx4 v15, s[16:19], s52 offen offset:1024 sc1 lds\n"
    "  s_waitcnt vmcnt(0)\n"
    "  ds_read_b128 v[116:119], v124 offset:57344\n"
    "  ds_read_b128 v[56:59], v124 offset:58368\n"
    "  s_waitcnt lgkmcnt(0)\n"
    "  v_max_u32 v116, v116, v56\n"
    "  v_cmp_eq_u32 vcc, -1, v116\n"
    "  s_cbranch_vccz Lxk_62%=\n"
    "  s_sub_u32 s55, s55, 1\n"
    "  s_cbranch_scc0 Lxr_63%=\n"
    "Lxk_62%=:\n"
    "  s_waitcnt lgkmcnt(0)\n"
    "  s_barrier\n"
    "  v_mfma_f32_16x16x32_bf16 v[48:51], v[64:67], v[64:67], v[48:51]\n"
    "  ds_read_b64_tr_b16 v[96:97], v120 offset:57344\n"
    "  v_mfma_f32_16x16x32_bf16 v[52:55], v[64:67], v[112:115], v[52:55]\n"
    "  ds_read_b64_tr_b16 v[98:99], v120 offset:57856\n"
    "  v_mfma_f32_16x16x32_bf16 v[16:19], v[64:67], a[96:99], v[16:19]\n"
    "  ds_read_b64_tr_b16 v[100:101], v121 offset:57344\n"
    "  v_mfma_f32_16x16x32_bf16 v[32:35], v[64:67], a[100:103], v[32:35]\n"
    "  ds_read_b64_tr_b16 v[102:103], v121 offset:57856\n"
    "  v_mfma_f32_16x16x32_bf16 v[20:23], v[68:71], a[96:99], v[20:23]\n"
    "  ds_read_b64_tr_b16 v[64:65], v120 offset:61440\n"
    "  v_mfma_f32_16x16x32_bf16 v[36:39], v[68:71], a[100:103], v[36:39]\n"
    "  ds_read_b64_tr_b16 v[66:67], v120 offset:61952\n"
    "  v_mfma_f32_16x16x32_bf16 v[24:27], v[72:75], a[96:99], v[24:27]\n"
    "  ds_read_b64_tr_b16 v[104:105], v122 offset:57344\n"
    "  v_mfma_f32_16x16x32_bf16 v[40:43], v[72:75], a[100:103], v[40:43]\n"
    "  ds_read_b64_tr_b16 v[106:107], v122 offset:57856\n"
    "  v_mfma_f32_16x16x32_bf16 v[28:31], v[76:79], a[96:99], v[28:31]\n"
    "  ds_read_b64_tr_b16 v[108:109], v123 offset:57344\n"
    "  v_mfma_f32_16x16x32_bf16 v[44:47], v[76:79], a[100:103], v[44:47]\n"
    "  ds_read_b64_tr_b16 v[110:111], v123 offset:57856\n"
    "  v_mfma_f32_16x16x32_bf16 v[48:51], v[80:83], v[80:83], v[48:51]\n"
    "  ds_read_b64_tr_b16 v[68:69], v121 offset:61440\n"
    "  ds_read_b64_tr_b16 v[70:71], v121 offset:61952\n"
    "  v_mfma_f32_16x16x32_bf16 v[52:55], v[80:83], v[112:115], v[52:55]\n"
    "  ds_read_b64_tr_b16 v[72:73], v122 offset:61440\n"
    "  ds_read_b64_tr_b16 v[74:75], v122 offset:61952\n"
    "  s_waitcnt lgkmcnt(8)\n"
    "  v_mfma_f32_16x16x32_bf16 v[48:51], v[96:99], v[96:99], v[48:51]\n"
    "  ds_read_b64_tr_b16 v[76:77], v123 offset:61440\n"
    "  ds_read_b64_tr_b16 v[78:79], v123 offset:61952\n"
    "  v_mfma_f32_16x16x32_bf16 v[52:55], v[96:99], v[112:115], v[52:55]\n"
    "  v_mfma_f32_16x16x32_bf16 v[48:51], v[64:67], v[64:67], v[48:51]\n"
    "  v_mfma_f32_16x16x32_bf16 v[52:55], v[64:67], v[112:115], v[52:55]\n"
    "  v_mfma_f32_16x16x32_bf16 v[16:19], v[80:83], a[104:107], v[16:19]\n"
    "  s_mov_b32 s58, 0xaaaaaaaa\n"
    "  s_mov_b32 s59, 0xaaaaaaaa\n"
    "  s_mov_b32 s60, 0xcccccccc\n"
    "  s_mov_b32 s61, 0xcccccccc\n"
    "  v_mfma_f32_16x16x32_bf16 v[32:35], v[80:83], a[108:111], v[32:35]\n"
    "  s_nop 1\n"
    "  v_cndmask_b32_e64 v56, v52, v53, s[58:59]\n"
    "  v_cndmask_b32_e64 v57, v54, v55, s[58:59]\n"
    "  v_cndmask_b32_e64 v126, v56, v57, s[60:61]\n"
    "  v_cndmask_b32_e64 v56, v48, v49, s[58:59]\n"
    "  v_mfma_f32_16x16x32_bf16 v[20:23], v[84:87], a[104:107], v[20:23]\n"
    "  v_cndmask_b32_e64 v57, v50, v51, s[58:59]\n"
    "  v_cndmask_b32_e64 v127, v56, v57, s[60:61]\n"
    "  v_mul_f32 v126, 0x3b000000, v126\n"
    "  v_mul_f32 v127, 0x3b000000, v127\n"
    "  v_mfma_f32_16x16x32_bf16 v[36:39], v[84:87], a[108:111], v[36:39]\n"
    "  v_fma_f32 v127, -v126, v126, v127\n"
    "  v_max_f32 v127, 0, v127\n"
    "  v_cvt_pk_bf16_f32 v126, v126, v126\n"
    "  v_and_b32 v126, 0xffff0000, v126\n"
    "  v_mfma_f32_16x16x32_bf16 v[24:27], v[88:91], a[104:107], v[24:27]\n"
    "  v_cvt_pk_bf16_f32 v127, v127, v127\n"
    "  v_and_b32 v127, 0xffff0000, v127\n"
    "  v_add_f32 v127, 0x3727c5ac, v127\n"
    "  v_cvt_pk_bf16_f32 v127, v127, v127\n"
    "  v_mfma_f32_16x16x32_bf16 v[40:43], v[88:91], a[108:111], v[40:43]\n"
    "  v_and_b32 v127, 0xffff0000, v127\n"
    "  v_sqrt_f32 v127, v127\n"
    "  s_nop 0\n"
    "  v_cvt_pk_bf16_f32 v127, v127, v127\n"
    "  v_and_b32 v127, 0xffff0000, v127\n"
    "  v_mfma_f32_16x16x32_bf16 v[28:31], v[92:95], a[104:107], v[28:31]\n"
    "  v_rcp_f32 v127, v127\n"
    "  s_nop 0\n"
    "  v_mul_f32 v56, 0x3fb8aa3b, v127\n"
    "  s_lshl_b32 s67, s40, 4\n"
    "  v_and_b32 v59, 15, v1\n"
    "  v_add_lshl_u32 v57, s67, v59, 2\n"
    "  v_add_u32 v57, 0x00018200, v57\n"
    "  v_mfma_f32_16x16x32_bf16 v[44:47], v[92:95], a[108:111], v[44:47]\n"
    "  s_mov_b32 exec_lo, 0x00f0000f\n"
    "  s_mov_b32 exec_hi, 0xf0000f00\n"
    "  ds_write2st64_b32 v57, v126, v126 offset0:0 offset1:1\n"
    "  ds_write2st64_b32 v57, v127, v127 offset0:4 offset1:5\n"
    "  ds_write2st64_b32 v57, v56, v56 offset0:2 offset1:3\n"
    "  s_mov_b64 exec, -1\n"
    "  s_waitcnt vmcnt(0) lgkmcnt(0)\n"
    "  s_barrier\n"
    "  v_lshrrev_b32 v127, 4, v1\n"
    "  s_lshl_b32 s67, s40, 6\n"
    "  s_add_u32 s67, s67, 0x18200\n"
    "  v_lshl_add_u32 v126, v127, 4, s67\n"
    "  v_mfma_f32_16x16x32_bf16 v[16:19], v[96:99], a[112:115], v[16:19]\n"
    "  ds_read_b128 v[80:83], v126\n"
    "  v_mfma_f32_16x16x32_bf16 v[32:35], v[96:99], a[116:119], v[32:35]\n"
    "  ds_read_b128 v[84:87], v126 offset:64\n"
    "  v_mfma_f32_16x16x32_bf16 v[20:23], v[100:103], a[112:115], v[20:23]\n"
    "  ds_read_b128 v[88:91], v126 offset:128\n"
    "  v_mfma_f32_16x16x32_bf16 v[36:39], v[100:103], a[116:119], v[36:39]\n"
    "  ds_read_b128 v[92:95], v126 offset:192\n"
    "  v_mfma_f32_16x16x32_bf16 v[24:27], v[104:107], a[112:115], v[24:27]\n"
    "  v_mfma_f32_16x16x32_bf16 v[40:43], v[104:107], a[116:119], v[40:43]\n"
    "  v_mfma_f32_16x16x32_bf16 v[28:31], v[108:111], a[112:115], v[28:31]\n"
    "  v_mfma_f32_16x16x32_bf16 v[44:47], v[108:111], a[116:119], v[44:47]\n"
    "  v_mfma_f32_16x16x32_bf16 v[16:19], v[64:67], a[120:123], v[16:19]\n"
    "  ds_read_b128 v[96:99], v126 offset:512\n"
    "  v_mfma_f32_16x16x32_bf16 v[32:35], v[64:67], a[124:127], v[32:35]\n"
    "  ds_read_b128 v[100:103], v126 offset:576\n"
    "  v_mfma_f32_16x16x32_bf16 v[20:23], v[68:71], a[120:123], v[20:23]\n"
    "  ds_read_b128 v[104:107], v126 offset:640\n"
    "  v_mfma_f32_16x16x32_bf16 v[36:39], v[68:71], a[124:127], v[36:39]\n"
    "  ds_read_b128 v[108:111], v126 offset:704\n"
    "  v_mfma_f32_16x16x32_bf16 v[24:27], v[72:75], a[120:123], v[24:27]\n"
    "  v_mfma_f32_16x16x32_bf16 v[40:43], v[72:75], a[124:127], v[40:43]\n"
    "  v_mfma_f32_16x16x32_bf16 v[28:31], v[76:79], a[120:123], v[28:31]\n"
    "  v_mfma_f32_16x16x32_bf16 v[44:47], v[76:79], a[124:127], v[44:47]\n"
    "  ds_read_b128 v[64:67], v126 offset:1024\n"
    "  ds_read_b128 v[68:71], v126 offset:1088\n"
    "  ds_read_b128 v[72:75], v126 offset:1152\n"
    "  ds_read_b128 v[76:79], v126 offset:1216\n"
    "  s_mov_b32 s62, 0x3f317218\n"
    "  s_mov_b32 s63, 0x3f317218\n"
    "  s_mov_b32 s54, 0x35c1a2a0\n"
    "  s_waitcnt lgkmcnt(7)\n"
    "  v_pk_fma_f32 v[16:17], v[80:81], v[4:5], v[16:17] op_sel_hi:[1,0,1] neg_lo:[1,0,0] neg_hi:[1,0,0]\n"
    "  v_pk_fma_f32 v[18:19], v[82:83], v[4:5], v[18:19] op_sel_hi:[1,0,1] neg_lo:[1,0,0] neg_hi:[1,0,0]\n"
    "  v_pk_mul_f32 v[16:17], v[16:17], v[96:97]\n"
    "  v_pk_mul_f32 v[18:19], v[18:19], v[98:99]\n"
    "  v_exp_f32_e64 v116, v16 clamp\n"
    "  v_exp_f32_e64 v117, v17 clamp\n"
    "  v_exp_f32_e64 v118, v18 clamp\n"
    "  v_exp_f32_e64 v119, v19 clamp\n"
    "  v_pk_fma_f32 v[32:33], v[80:81], v[4:5], v[32:33] op_sel:[0,1,0] op_sel_hi:[1,1,1] neg_lo:[1,0,0] neg_hi:[1,0,0]\n"
    "  v_pk_fma_f32 v[34:35], v[82:83], v[4:5], v[34:35] op_sel:[0,1,0] op_sel_hi:[1,1,1] neg_lo:[1,0,0] neg_hi:[1,0,0]\n"
    "  v_max_f32 v16, s54, v16\n"
    "  v_max_f32 v17, s54, v17\n"
    "  v_max_f32 v18, s54, v18\n"
    "  v_max_f32 v19, s54, v19\n"
    "  s_waitcnt lgkmcnt(3)\n"
    "  v_pk_mul_f32 v[32:33], v[32:33], v[64:65]\n"
    "  v_pk_mul_f32 v[34:35], v[34:35], v[66:67]\n"
    "  v_pk_fma_f32 v[16:17], v[16:17], s[62:63], v[116:117]\n"
    "  v_pk_fma_f32 v[18:19], v[18:19], s[62:63], v[118:119]\n"
    "  v_cvt_pk_bf16_f32 v56, v32, v33\n"
    "  v_cvt_pk_bf16_f32 v57, v34, v35\n"
    "  v_cvt_pk_bf16_f32 v48, v16, v17\n"
    "  v_cvt_pk_bf16_f32 v49, v18, v19\n"
    "  v_pk_fma_f32 v[20:21], v[84:85], v[4:5], v[20:21] op_sel_hi:[1,0,1] neg_lo:[1,0,0] neg_hi:[1,0,0]\n"
    "  v_pk_fma_f32 v[22:23], v[86:87], v[4:5], v[22:23] op_sel_hi:[1,0,1] neg_lo:[1,0,0] neg_hi:[1,0,0]\n"
    "  v_pk_mul_f32 v[20:21], v[20:21], v[100:101]\n"
    "  v_pk_mul_f32 v[22:23], v[22:23], v[102:103]\n"
    "  v_exp_f32_e64 v116, v20 clamp\n"
    "  v_exp_f32_e64 v117, v21 clamp\n"
    "  v_exp_f32_e64 v118, v22 clamp\n"
    "  v_exp_f32_e64 v119, v23 clamp\n"
    "  v_pk_fma_f32 v[36:37], v[84:85], v[4:5], v[36:37] op_sel:[0,1,0] op_sel_hi:[1,1,1] neg_lo:[1,0,0] neg_hi:[1,0,0]\n"
    "  v_pk_fma_f32 v[38:39], v[86:87], v[4:5], v[38:39] op_sel:[0,1,0] op_sel_hi:[1,1,1] neg_lo:[1,0,0] neg_hi:[1,0,0]\n"
    "  v_max_f32 v20, s54, v20\n"
    "  v_max_f32 v21, s54, v21\n"
    "  v_max_f32 v22, s54, v22\n"
    "  v_max_f32 v23, s54, v23\n"
    "  s_waitcnt lgkmcnt(2)\n"
    "  v_pk_mul_f32 v[36:37], v[36:37], v[68:69]\n"
    "  v_pk_mul_f32 v[38:39], v[38:39], v[70:71]\n"
    "  v_pk_fma_f32 v[20:21], v[20:21], s[62:63], v[116:117]\n"
    "  v_pk_fma_f32 v[22:23], v[22:23], s[62:63], v[118:119]\n"
    "  v_cvt_pk_bf16_f32 v58, v36, v37\n"
    "  v_cvt_pk_bf16_f32 v59, v38, v39\n"
    "  v_cvt_pk_bf16_f32 v50, v20, v21\n"
    "  v_cvt_pk_bf16_f32 v51, v22, v23\n"
    "  s_nop 1\n"
    "  v_mfma_f32_16x16x32_bf16 v[120:123], v[48:51], v[56:59], 0\n"
    "  v_mfma_f32_16x16x32_bf16 v[124:127], v[112:115], v[48:51], 0\n"
    "  v_pk_fma_f32 v[24:25], v[88:89], v[4:5], v[24:25] op_sel_hi:[1,0,1] neg_lo:[1,0,0] neg_hi:[1,0,0]\n"
    "  v_pk_fma_f32 v[26:27], v[90:91], v[4:5], v[26:27] op_sel_hi:[1,0,1] neg_lo:[1,0,0] neg_hi:[1,0,0]\n"
    "  v_pk_mul_f32 v[24:25], v[24:25], v[104:105]\n"
    "  v_pk_mul_f32 v[26:27], v[26:27], v[106:107]\n"
    "  v_exp_f32_e64 v116, v24 clamp\n"
    "  v_exp_f32_e64 v117, v25 clamp\n"
    "  v_exp_f32_e64 v118, v26 clamp\n"
    "  v_exp_f32_e64 v119, v27 clamp\n"
    "  v_pk_fma_f32 v[40:41], v[88:89], v[4:5], v[40:41] op_sel:[0,1,0] op_sel_hi:[1,1,1] neg_lo:[1,0,0] neg_hi:[1,0,0]\n"
    "  v_pk_fma_f32 v[42:43], v[90:91], v[4:5], v[42:43] op_sel:[0,1,0] op_sel_hi:[1,1,1] neg_lo:[1,0,0] neg_hi:[1,0,0]\n"
    "  v_max_f32 v24, s54, v24\n"
    "  v_max_f32 v25, s54, v25\n"
    "  v_max_f32 v26, s54, v26\n"
    "  v_max_f32 v27, s54, v27\n"
    "  s_waitcnt lgkmcnt(1)\n"
    "  v_pk_mul_f32 v[40:41], v[40:41], v[72:73]\n"
    "  v_pk_mul_f32 v[42:43], v[42:43], v[74:75]\n"
    "  v_pk_fma_f32 v[24:25], v[24:25], s[62:63], v[116:117]\n"
    "  v_pk_fma_f32 v[26:27], v[26:27], s[62:63], v[118:119]\n"
    "  v_cvt_pk_bf16_f32 v60, v40, v41\n"
    "  v_cvt_pk_bf16_f32 v61, v42, v43\n"
    "  v_cvt_pk_bf16_f32 v52, v24, v25\n"
    "  v_cvt_pk_bf16_f32 v53, v26, v27\n"
    "  v_pk_fma_f32 v[28:29], v[92:93], v[4:5], v[28:29] op_sel_hi:[1,0,1] neg_lo:[1,0,0] neg_hi:[1,0,0]\n"
    "  v_pk_fma_f32 v[30:31], v[94:95], v[4:5], v[30:31] op_sel_hi:[1,0,1] neg_lo:[1,0,0] neg_hi:[1,0,0]\n"
    "  v_pk_mul_f32 v[28:29], v[28:29], v[108:109]\n"
    "  v_pk_mul_f32 v[30:31], v[30:31], v[110:111]\n"
    "  v_exp_f32_e64 v116, v28 clamp\n"
    "  v_exp_f32_e64 v117, v29 clamp\n"
    "  v_exp_f32_e64 v118, v30 clamp\n"
    "  v_exp_f32_e64 v119, v31 clamp\n"
    "  v_pk_fma_f32 v[44:45], v[92:93], v[4:5], v[44:45] op_sel:[0,1,0] op_sel_hi:[1,1,1] neg_lo:[1,0,0] neg_hi:[1,0,0]\n"
    "  v_pk_fma_f32 v[46:47], v[94:95], v[4:5], v[46:47] op_sel:[0,1,0] op_sel_hi:[1,1,1] neg_lo:[1,0,0] neg_hi:[1,0,0]\n"
    "  v_max_f32 v28, s54, v28\n"
    "  v_max_f32 v29, s54, v29\n"
    "  v_max_f32 v30, s54, v30\n"
    "  v_max_f32 v31, s54, v31\n"
    "  s_waitcnt lgkmcnt(0)\n"
    "  v_pk_mul_f32 v[44:45], v[44:45], v[76:77]\n"
    "  v_pk_mul_f32 v[46:47], v[46:47], v[78:79]\n"
    "  v_pk_fma_f32 v[28:29], v[28:29], s[62:63], v[116:117]\n"
    "  v_pk_fma_f32 v[30:31], v[30:31], s[62:63], v[118:119]\n"
    "  v_cvt_pk_bf16_f32 v62, v44, v45\n"
    "  v_cvt_pk_bf16_f32 v63, v46, v47\n"
    "  v_cvt_pk_bf16_f32 v54, v28, v29\n"
    "  v_cvt_pk_bf16_f32 v55, v30, v31\n"
    "  s_nop 1\n"
    "  v_mfma_f32_16x16x32_bf16 v[120:123], v[52:55], v[60:63], v[120:123]\n"
    "  v_mfma_f32_16x16x32_bf16 v[124:127], v[112:115], v[52:55], v[124:127]\n"
    "  s_lshl_b32 s67, s47, 8\n"
    "  s_add_u32 s67, s67, s41\n"
    "  s_lshl_b32 s67, s67, 7\n"
    "  s_add_u32 s52, s67, 0x300000\n"
    "  s_lshl_b32 s67, s47, 5\n"
    "  s_add_u32 s67, s67, s41\n"
    "  s_lshl_b32 s67, s67, 6\n"
    "  s_add_u32 s53, s67, 0x30000\n"
    "  buffer_store_dwordx4 v[120:123], v8, s[16:19], s52 offen sc1\n"
    "  s_mov_b64 exec, 0xffff\n"
    "  buffer_store_dword v124, v9, s[16:19], s53 offen sc1\n"
    "  s_mov_b64 exec, -1\n"
    "  v_lshrrev_b32 v111, 4, v1\n"
    "  v_lshrrev_b32 v110, 1, v111\n"
    "  v_bfe_u32 v109, v1, 2, 2\n"
    "  v_lshl_add_u32 v108, v110, 3, v109\n"
    "  s_and_b32 s67, s40, 1\n"
    "  s_lshl_b32 s66, s67, 1\n"
    "  v_and_b32 v111, 1, v111\n"
    "  v_add_u32 v111, s66, v111\n"
    "  v_lshrrev_b32 v109, 1, v109\n"
    "  v_lshl_add_u32 v109, v109, 1, v110\n"
    "  v_xor_b32 v109, v111, v109\n"
    "  v_lshlrev_b32 v109, 5, v109\n"
    "  v_lshl_add_u32 v108, v108, 7, v109\n"
    "  v_and_b32 v109, 3, v1\n"
    "  v_lshlrev_b32 v109, 3, v109\n"
    "  s_lshl_b32 s67, s67, 15\n"
    "  v_add3_u32 v112, v108, v109, s67\n"
    "  ds_read_b64_tr_b16 v[64:65], v112\n"
    "  ds_read_b64_tr_b16 v[66:67], v112 offset:512\n"
    "  ds_read_b64_tr_b16 v[68:69], v112 offset:2048\n"
    "  ds_read_b64_tr_b16 v[70:71], v112 offset:2560\n"
    "  ds_read_b64_tr_b16 v[72:73], v112 offset:4096\n"
    "  ds_read_b64_tr_b16 v[74:75], v112 offset:4608\n"
    "  ds_read_b64_tr_b16 v[76:77], v112 offset:6144\n"
    "  ds_read_b64_tr_b16 v[78:79], v112 offset:6656\n"
    "  ds_read_b64_tr_b16 v[80:81], v112 offset:8192\n"
    "  ds_read_b64_tr_b16 v[82:83], v112 offset:8704\n"
    "  s_waitcnt lgkmcnt(8)\n"
    "  v_mfma_f32_32x32x16_bf16 v[16:31], a[128:131], v[64:67], 0\n"
    "  ds_read_b64_tr_b16 v[84:85], v112 offset:10240\n"
    "  ds_read_b64_tr_b16 v[86:87], v112 offset:10752\n"
    "  v_and_b32 v59, 15, v1\n"
    "  s_lshl_b32 s67, s40, 7\n"
    "  s_add_u32 s67, s67, 0x18800\n"
    "  v_lshl_add_u32 v56, v59, 2, s67\n"
    "  s_waitcnt lgkmcnt(6)\n"
    "  v_mfma_f32_32x32x16_bf16 v[16:31], a[132:135], v[68:71], v[16:31]\n"
    "  ds_read_b64_tr_b16 v[64:65], v112 offset:12288\n"
    "  ds_read_b64_tr_b16 v[66:67], v112 offset:12800\n"
    "  ds_write2_b32 v56, v6, v7 offset0:0 offset1:16\n"
    "  v_mfma_f32_32x32x16_bf16 v[16:31], a[136:139], v[72:75], v[16:31]\n"
    "  ds_read_b64_tr_b16 v[68:69], v112 offset:14336\n"
    "  ds_read_b64_tr_b16 v[70:71], v112 offset:14848\n"
    "  v_lshrrev_b32 v59, 5, v1\n"
    "  s_lshl_b32 s67, s40, 7\n"
    "  s_add_u32 s67, s67, 0x18800\n"
    "  v_lshl_add_u32 v56, v59, 4, s67\n"
    "  s_waitcnt lgkmcnt(7)\n"
    "  v_mfma_f32_32x32x16_bf16 v[16:31], a[140:143], v[76:79], v[16:31]\n"
    "  ds_read_b64_tr_b16 v[72:73], v112 offset:16384\n"
    "  ds_read_b64_tr_b16 v[74:75], v112 offset:16896\n"
    "  ds_read_b128 v[32:35], v56\n"
    "  v_mfma_f32_32x32x16_bf16 v[16:31], a[144:147], v[80:83], v[16:31]\n"
    "  ds_read_b64_tr_b16 v[76:77], v112 offset:18432\n"
    "  ds_read_b64_tr_b16 v[78:79], v112 offset:18944\n"
    "  ds_read_b128 v[36:39], v56 offset:32\n"
    "  s_waitcnt lgkmcnt(9)\n"
    "  v_mfma_f32_32x32x16_bf16 v[16:31], a[148:151], v[84:87], v[16:31]\n"
    "  ds_read_b64_tr_b16 v[80:81], v112 offset:20480\n"
    "  ds_read_b64_tr_b16 v[82:83], v112 offset:20992\n"
    "  ds_read_b128 v[40:43], v56 offset:64\n"
    "  v_mfma_f32_32x32x16_bf16 v[16:31], a[152:155], v[64:67], v[16:31]\n"
    "  ds_read_b64_tr_b16 v[84:85], v112 offset:22528\n"
    "  ds_read_b64_tr_b16 v[86:87], v112 offset:23040\n"
    "  ds_read_b128 v[44:47], v56 offset:96\n"
    "  s_waitcnt lgkmcnt(12)\n"
    "  v_mfma_f32_32x32x16_bf16 v[16:31], a[156:159], v[68:71], v[16:31]\n"
    "  ds_read_b64_tr_b16 v[64:65], v112 offset:24576\n"
    "  ds_read_b64_tr_b16 v[66:67], v112 offset:25088\n"
    "  v_and_b32 v59, 31, v1\n"
    "  s_and_b32 s67, s40, 1\n"
    "  s_lshl_b32 s67, s67, 7\n"
    "  s_add_u32 s67, s67, 0x18200\n"
    "  v_lshl_add_u32 v56, v59, 2, s67\n"
    "  s_waitcnt lgkmcnt(9)\n"
    "  v_mfma_f32_32x32x16_bf16 v[16:31], a[160:163], v[72:75], v[16:31]\n"
    "  ds_read_b64_tr_b16 v[68:69], v112 offset:26624\n"
    "  ds_read_b64_tr_b16 v[70:71], v112 offset:27136\n"
    "  ds_read_b32 v57, v56\n"
    "  v_mfma_f32_32x32x16_bf16 v[16:31], a[164:167], v[76:79], v[16:31]\n"
    "  ds_read_b64_tr_b16 v[72:73], v112 offset:28672\n"
    "  ds_read_b64_tr_b16 v[74:75], v112 offset:29184\n"
    "  ds_read_b32 v58, v56 offset:512\n"
    "  s_waitcnt lgkmcnt(9)\n"
    "  v_mfma_f32_32x32x16_bf16 v[16:31], a[168:171], v[80:83], v[16:31]\n"
    "  ds_read_b64_tr_b16 v[76:77], v112 offset:30720\n"
    "  ds_read_b64_tr_b16 v[78:79], v112 offset:31232\n"
    "  s_mov_b32 s62, 0x3f317218\n"
    "  v_mfma_f32_32x32x16_bf16 v[16:31], a[172:175], v[84:87], v[16:31]\n"
    "  s_and_b32 s51, s40, 1\n"
    "  s_lshl_b32 s51, s51, 16\n"
    "  s_sub_u32 s51, 0x00008000, s51\n"
    "  v_add_u32 v112, s51, v112\n"
    "  ds_read_b64_tr_b16 v[80:81], v112\n"
    "  ds_read_b64_tr_b16 v[82:83], v112 offset:512\n"
    "  s_mov_b32 s63, 0x3f317218\n"
    "  s_waitcnt lgkmcnt(8)\n"
    "  v_mfma_f32_32x32x16_bf16 v[16:31], a[176:179], v[64:67], v[16:31]\n"
    "  ds_read_b64_tr_b16 v[84:85], v112 offset:2048\n"
    "  ds_read_b64_tr_b16 v[86:87], v112 offset:2560\n"
    "  s_mov_b32 s54, 0x35c1a2a0\n"
    "  v_mfma_f32_32x32x16_bf16 v[16:31], a[180:183], v[68:71], v[16:31]\n"
    "  ds_read_b64_tr_b16 v[64:65], v112 offset:4096\n"
    "  ds_read_b64_tr_b16 v[66:67], v112 offset:4608\n"
    "  s_waitcnt lgkmcnt(6)\n"
    "  v_mfma_f32_32x32x16_bf16 v[16:31], a[184:187], v[72:75], v[16:31]\n"
    "  ds_read_b64_tr_b16 v[68:69], v112 offset:6144\n"
    "  ds_read_b64_tr_b16 v[70:71], v112 offset:6656\n"
    "  v_mfma_f32_32x32x16_bf16 v[16:31], a[188:191], v[76:79], v[16:31]\n"
    "  ds_read_b64_tr_b16 v[72:73], v112 offset:8192\n"
    "  ds_read_b64_tr_b16 v[74:75], v112 offset:8704\n"
    "  s_waitcnt lgkmcnt(6)\n"
    "  v_mfma_f32_32x32x16_bf16 v[16:31], a[192:195], v[80:83], v[16:31]\n"
    "  ds_read_b64_tr_b16 v[76:77], v112 offset:10240\n"
    "  ds_read_b64_tr_b16 v[78:79], v112 offset:10752\n"
    "  v_mfma_f32_32x32x16_bf16 v[16:31], a[196:199], v[84:87], v[16:31]\n"
    "  ds_read_b64_tr_b16 v[80:81], v112 offset:12288\n"
    "  ds_read_b64_tr_b16 v[82:83], v112 offset:12800\n"
    "  s_waitcnt lgkmcnt(6)\n"
    "  v_mfma_f32_32x32x16_bf16 v[16:31], a[200:203], v[64:67], v[16:31]\n"
    "  ds_read_b64_tr_b16 v[84:85], v112 offset:14336\n"
    "  ds_read_b64_tr_b16 v[86:87], v112 offset:14848\n"
    "  v_mfma_f32_32x32x16_bf16 v[16:31], a[204:207], v[68:71], v[16:31]\n"
    "  ds_read_b64_tr_b16 v[64:65], v112 offset:16384\n"
    "  ds_read_b64_tr_b16 v[66:67], v112 offset:16896\n"
    "  s_waitcnt lgkmcnt(6)\n"
    "  v_mfma_f32_32x32x16_bf16 v[16:31], a[208:211], v[72:75], v[16:31]\n"
    "  ds_read_b64_tr_b16 v[68:69], v112 offset:18432\n"
    "  ds_read_b64_tr_b16 v[70:71], v112 offset:18944\n"
    "  v_mfma_f32_32x32x16_bf16 v[16:31], a[212:215], v[76:79], v[16:31]\n"
    "  ds_read_b64_tr_b16 v[72:73], v112 offset:20480\n"
    "  ds_read_b64_tr_b16 v[74:75], v112 offset:20992\n"
    "  s_waitcnt lgkmcnt(6)\n"
    "  v_mfma_f32_32x32x16_bf16 v[16:31], a[216:219], v[80:83], v[16:31]\n"
    "  ds_read_b64_tr_b16 v[76:77], v112 offset:22528\n"
    "  ds_read_b64_tr_b16 v[78:79], v112 offset:23040\n"
    "  v_mfma_f32_32x32x16_bf16 v[16:31], a[220:223], v[84:87], v[16:31]\n"
    "  ds_read_b64_tr_b16 v[80:81], v112 offset:24576\n"
    "  ds_read_b64_tr_b16 v[82:83], v112 offset:25088\n"
    "  s_waitcnt lgkmcnt(6)\n"
    "  v_mfma_f32_32x32x16_bf16 v[16:31], a[224:227], v[64:67], v[16:31]\n"
    "  ds_read_b64_tr_b16 v[84:85], v112 offset:26624\n"
    "  ds_read_b64_tr_b16 v[86:87], v112 offset:27136\n"
    "  v_mfma_f32_32x32x16_bf16 v[16:31], a[228:231], v[68:71], v[16:31]\n"
    "  ds_read_b64_tr_b16 v[64:65], v112 offset:28672\n"
    "  ds_read_b64_tr_b16 v[66:67], v112 offset:29184\n"
    "  s_waitcnt lgkmcnt(6)\n"
    "  v_mfma_f32_32x32x16_bf16 v[16:31], a[232:235], v[72:75], v[16:31]\n"
    "  ds_read_b64_tr_b16 v[68:69], v112 offset:30720\n"
    "  ds_read_b64_tr_b16 v[70:71], v112 offset:31232\n"
    "  v_mfma_f32_32x32x16_bf16 v[16:31], a[236:239], v[76:79], v[16:31]\n"
    "  s_waitcnt lgkmcnt(6)\n"
    "  v_mfma_f32_32x32x16_bf16 v[16:31], a[240:243], v[80:83], v[16:31]\n"
    "  s_waitcnt lgkmcnt(4)\n"
    "  v_mfma_f32_32x32x16_bf16 v[16:31], a[244:247], v[84:87], v[16:31]\n"
    "  s_waitcnt lgkmcnt(2)\n"
    "  v_mfma_f32_32x32x16_bf16 v[16:31], a[248:251], v[64:67], v[16:31]\n"
    "  s_waitcnt lgkmcnt(0)\n"
    "  v_mfma_f32_32x32x16_bf16 v[16:31], a[252:255], v[68:71], v[16:31]\n"
    "  s_cmp_lg_u32 s40, 0\n"
    "  s_cbranch_scc1 Lnr_64%=\n"
    "  s_lshl_b32 s67, s2, 12\n"
    "  s_add_u32 s32, s67, 0x300000\n"
    "  s_and_b32 s67, s2, 7\n"
    "  s_lshr_b32 s67, s67, 2\n"
    "  s_lshr_b32 s66, s2, 3\n"
    "  s_lshl_b32 s66, s66, 11\n"
    "  s_lshl4_add_u32 s67, s67, s66\n"
    "  s_add_u32 s33, s67, 0x30000\n"
    "  v_lshrrev_b32 v111, 1, v1\n"
    "  v_and_b32 v110, 1, v1\n"
    "  v_lshlrev_b32 v110, 5, v110\n"
    "  v_lshl_add_u32 v108, v111, 6, v110\n"
    "  buffer_load_dwordx4 v[88:91], v2, s[16:19], s32 offen sc1\n"
    "  buffer_load_dwordx4 v[92:95], v2, s[16:19], s32 offen offset:1024 sc1\n"
    "  buffer_load_dwordx4 v[96:99], v2, s[16:19], s32 offen offset:2048 sc1\n"
    "  buffer_load_dwordx4 v[100:103], v2, s[16:19], s32 offen offset:3072 sc1\n"
    "  buffer_load_dwordx4 v[104:107], v108, s[16:19], s33 offen sc1\n"
    "Lnr_64%=:\n"
    "  s_nop 9\n"
    "  v_pk_fma_f32 v[16:17], v[32:33], v[56:57], v[16:17] op_sel:[0,1,0] op_sel_hi:[1,1,1] neg_lo:[1,0,0] neg_hi:[1,0,0]\n"
    "  v_pk_fma_f32 v[18:19], v[34:35], v[56:57], v[18:19] op_sel:[0,1,0] op_sel_hi:[1,1,1] neg_lo:[1,0,0] neg_hi:[1,0,0]\n"
    "  v_pk_mul_f32 v[16:17], v[16:17], v[58:59] op_sel_hi:[1,0]\n"
    "  v_pk_mul_f32 v[18:19], v[18:19], v[58:59] op_sel_hi:[1,0]\n"
    "  v_exp_f32_e64 v60, v16 clamp\n"
    "  v_exp_f32_e64 v61, v17 clamp\n"
    "  v_exp_f32_e64 v62, v18 clamp\n"
    "  v_exp_f32_e64 v63, v19 clamp\n"
    "  v_max_f32 v16, s54, v16\n"
    "  v_max_f32 v17, s54, v17\n"
    "  v_max_f32 v18, s54, v18\n"
    "  v_max_f32 v19, s54, v19\n"
    "  v_pk_fma_f32 v[16:17], v[16:17], s[62:63], v[60:61]\n"
    "  v_pk_fma_f32 v[18:19], v[18:19], s[62:63], v[62:63]\n"
    "  v_cvt_pk_bf16_f32 v48, v16, v17\n"
    "  v_cvt_pk_bf16_f32 v49, v18, v19\n"
    "  v_pk_fma_f32 v[20:21], v[36:37], v[56:57], v[20:21] op_sel:[0,1,0] op_sel_hi:[1,1,1] neg_lo:[1,0,0] neg_hi:[1,0,0]\n"
    "  v_pk_fma_f32 v[22:23], v[38:39], v[56:57], v[22:23] op_sel:[0,1,0] op_sel_hi:[1,1,1] neg_lo:[1,0,0] neg_hi:[1,0,0]\n"
    "  v_pk_mul_f32 v[20:21], v[20:21], v[58:59] op_sel_hi:[1,0]\n"
    "  v_pk_mul_f32 v[22:23], v[22:23], v[58:59] op_sel_hi:[1,0]\n"
    "  v_exp_f32_e64 v60, v20 clamp\n"
    "  v_exp_f32_e64 v61, v21 clamp\n"
    "  v_exp_f32_e64 v62, v22 clamp\n"
    "  v_exp_f32_e64 v63, v23 clamp\n"
    "  v_max_f32 v20, s54, v20\n"
    "  v_max_f32 v21, s54, v21\n"
    "  v_max_f32 v22, s54, v22\n"
    "  v_max_f32 v23, s54, v23\n"
    "  v_pk_fma_f32 v[20:21], v[20:21], s[62:63], v[60:61]\n"
    "  v_pk_fma_f32 v[22:23], v[22:23], s[62:63], v[62:63]\n"
    "  v_cvt_pk_bf16_f32 v50, v20, v21\n"
    "  v_cvt_pk_bf16_f32 v51, v22, v23\n"
    "  v_pk_fma_f32 v[24:25], v[40:41], v[56:57], v[24:25] op_sel:[0,1,0] op_sel_hi:[1,1,1] neg_lo:[1,0,0] neg_hi:[1,0,0]\n"
    "  v_pk_fma_f32 v[26:27], v[42:43], v[56:57], v[26:27] op_sel:[0,1,0] op_sel_hi:[1,1,1] neg_lo:[1,0,0] neg_hi:[1,0,0]\n"
    "  v_pk_mul_f32 v[24:25], v[24:25], v[58:59] op_sel_hi:[1,0]\n"
    "  v_pk_mul_f32 v[26:27], v[26:27], v[58:59] op_sel_hi:[1,0]\n"
    "  v_exp_f32_e64 v60, v24 clamp\n"
    "  v_exp_f32_e64 v61, v25 clamp\n"
    "  v_exp_f32_e64 v62, v26 clamp\n"
    "  v_exp_f32_e64 v63, v27 clamp\n"
    "  v_max_f32 v24, s54, v24\n"
    "  v_max_f32 v25, s54, v25\n"
    "  v_max_f32 v26, s54, v26\n"
    "  v_max_f32 v27, s54, v27\n"
    "  v_pk_fma_f32 v[24:25], v[24:25], s[62:63], v[60:61]\n"
    "  v_pk_fma_f32 v[26:27], v[26:27], s[62:63], v[62:63]\n"
    "  v_cvt_pk_bf16_f32 v52, v24, v25\n"
    "  v_cvt_pk_bf16_f32 v53, v26, v27\n"
    "  v_pk_fma_f32 v[28:29], v[44:45], v[56:57], v[28:29] op_sel:[0,1,0] op_sel_hi:[1,1,1] neg_lo:[1,0,0] neg_hi:[1,0,0]\n"
    "  v_pk_fma_f32 v[30:31], v[46:47], v[56:57], v[30:31] op_sel:[0,1,0] op_sel_hi:[1,1,1] neg_lo:[1,0,0] neg_hi:[1,0,0]\n"
    "  v_pk_mul_f32 v[28:29], v[28:29], v[58:59] op_sel_hi:[1,0]\n"
    "  v_pk_mul_f32 v[30:31], v[30:31], v[58:59] op_sel_hi:[1,0]\n"
    "  v_exp_f32_e64 v60, v28 clamp\n"
    "  v_exp_f32_e64 v61, v29 clamp\n"
    "  v_exp_f32_e64 v62, v30 clamp\n"
    "  v_exp_f32_e64 v63, v31 clamp\n"
    "  v_max_f32 v28, s54, v28\n"
    "  v_max_f32 v29, s54, v29\n"
    "  v_max_f32 v30, s54, v30\n"
    "  v_max_f32 v31, s54, v31\n"
    "  v_pk_fma_f32 v[28:29], v[28:29], s[62:63], v[60:61]\n"
    "  v_pk_fma_f32 v[30:31], v[30:31], s[62:63], v[62:63]\n"
    "  v_cvt_pk_bf16_f32 v54, v28, v29\n"
    "  v_cvt_pk_bf16_f32 v55, v30, v31\n"
    "  s_and_b32 s67, s40, 1\n"
    "  s_lshl1_add_u32 s67, s41, s67\n"
    "  s_lshl_b32 s67, s67, 5\n"
    "  s_lshl2_add_u32 s67, s42, s67\n"
    "  s_and_b32 s66, s40, 2\n"
    "  s_add_u32 s66, s67, s66\n"
    "  s_lshl_b32 s66, s66, 10\n"
    "  s_add_u32 s52, s66, 0x600000\n"
    "  buffer_store_dwordx4 v[48:51], v2, s[16:19], s52 offen sc1\n"
    "  buffer_store_dwordx4 v[52:55], v2, s[16:19], s52 offen offset:1024 sc1\n"
    "  s_cmp_lg_u32 s40, 0\n"
    "  s_cbranch_scc1 Lnr_65%=\n"
    "  s_lshl_b32 s67, s2, 7\n"
    "  s_add_u32 s39, s67, 0x1c0000\n"
    "  v_lshrrev_b32 v111, 3, v1\n"
    "  v_and_b32 v110, 7, v1\n"
    "  v_lshlrev_b32 v110, 3, v110\n"
    "  v_lshl_add_u32 v109, v111, 15, v110\n"
    "  s_waitcnt vmcnt(2)\n"
    "  s_mov_b32 s55, 0x00008000\n"
    "Lc_66%=:\n"
    "  v_max3_u32 v111, v88, v92, v96\n"
    "  v_max3_u32 v111, v111, v100, v104\n"
    "  v_max3_u32 v111, v111, v105, v106\n"
    "  v_max_u32 v111, v111, v107\n"
    "  v_cmp_eq_u32 vcc, -1, v111\n"
    "  s_cbranch_vccz Lo_67%=\n"
    "  s_sub_u32 s55, s55, 1\n"
    "  s_cbranch_scc1 Lo_67%=\n"
    "  s_sleep 2\n"
    "  s_mov_b64 exec, vcc\n"
    "  buffer_load_dwordx4 v[88:91], v2, s[16:19], s32 offen sc1\n"
    "  buffer_load_dwordx4 v[92:95], v2, s[16:19], s32 offen offset:1024 sc1\n"
    "  buffer_load_dwordx4 v[96:99], v2, s[16:19], s32 offen offset:2048 sc1\n"
    "  buffer_load_dwordx4 v[100:103], v2, s[16:19], s32 offen offset:3072 sc1\n"
    "  buffer_load_dwordx4 v[104:107], v108, s[16:19], s33 offen sc1\n"
    "  s_mov_b64 exec, -1\n"
    "  s_waitcnt vmcnt(0)\n"
    "  s_branch Lc_66%=\n"
    "Lo_67%=:\n"
    "  v_pk_add_f32 v[88:89], v[88:89], v[92:93]\n"
    "  v_pk_add_f32 v[96:97], v[96:97], v[100:101]\n"
    "  v_pk_add_f32 v[90:91], v[90:91], v[94:95]\n"
    "  v_pk_add_f32 v[98:99], v[98:99], v[102:103]\n"
    "  v_pk_add_f32 v[88:89], v[88:89], v[96:97]\n"
    "  v_pk_add_f32 v[90:91], v[90:91], v[98:99]\n"
    "  s_nop 0\n"
    "  v_add_f32_dpp v88, v88, v88 row_ror:8 row_mask:0xf bank_mask:0xf bound_ctrl:1\n"
    "  v_add_f32_dpp v89, v89, v89 row_ror:8 row_mask:0xf bank_mask:0xf bound_ctrl:1\n"
    "  v_add_f32_dpp v90, v90, v90 row_ror:8 row_mask:0xf bank_mask:0xf bound_ctrl:1\n"
    "  v_add_f32_dpp v91, v91, v91 row_ror:8 row_mask:0xf bank_mask:0xf bound_ctrl:1\n"
    "  v_add_f32_dpp v104, v104, v104 quad_perm:[2,3,0,1] row_mask:0xf bank_mask:0xf bound_ctrl:1\n"
    "  v_add_f32_dpp v105, v105, v105 quad_perm:[2,3,0,1] row_mask:0xf bank_mask:0xf bound_ctrl:1\n"
    "  v_add_f32_dpp v106, v106, v106 quad_perm:[2,3,0,1] row_mask:0xf bank_mask:0xf bound_ctrl:1\n"
    "  v_add_f32_dpp v107, v107, v107 quad_perm:[2,3,0,1] row_mask:0xf bank_mask:0xf bound_ctrl:1\n"
    "  v_mov_b32 v92, v88\n"
    "  v_mov_b32 v93, v89\n"
    "  v_mov_b32 v94, v90\n"
    "  v_mov_b32 v95, v91\n"
    "  v_permlane16_swap_b32 v88, v92\n"
    "  v_permlane16_swap_b32 v89, v93\n"
    "  v_permlane16_swap_b32 v90, v94\n"
    "  v_permlane16_swap_b32 v91, v95\n"
    "  v_add_f32 v88, v88, v92\n"
    "  v_add_f32 v89, v89, v93\n"
    "  v_add_f32 v90, v90, v94\n"
    "  v_add_f32 v91, v91, v95\n"
    "  v_mov_b32 v92, v88\n"
    "  v_mov_b32 v93, v89\n"
    "  v_mov_b32 v94, v90\n"
    "  v_mov_b32 v95, v91\n"
    "  v_permlane32_swap_b32 v88, v92\n"
    "  v_permlane32_swap_b32 v89, v93\n"
    "  v_permlane32_swap_b32 v90, v94\n"
    "  v_permlane32_swap_b32 v91, v95\n"
    "  v_add_f32 v88, v88, v92\n"
    "  v_add_f32 v89, v89, v93\n"
    "  v_add_f32 v90, v90, v94\n"
    "  v_add_f32 v91, v91, v95\n"
    "  v_cvt_pk_bf16_f32 v100, v88, v89\n"
    "  v_cvt_pk_bf16_f32 v101, v90, v91\n"
    "  buffer_store_dwordx2 v[100:101], v109, s[16:19], s39 offen sc1\n"
    "  s_and_b32 s53, s2, 3\n"
    "  s_cmp_lg_u32 s53, 0\n"
    "  s_cbranch_scc1 Lrz_68%=\n"
    "  v_add_f32_dpp v104, v104, v104 row_ror:4 row_mask:0xf bank_mask:0xf bound_ctrl:1\n"
    "  v_add_f32_dpp v105, v105, v105 row_ror:4 row_mask:0xf bank_mask:0xf bound_ctrl:1\n"
    "  v_add_f32_dpp v106, v106, v106 row_ror:4 row_mask:0xf bank_mask:0xf bound_ctrl:1\n"
    "  v_add_f32_dpp v107, v107, v107 row_ror:4 row_mask:0xf bank_mask:0xf bound_ctrl:1\n"
    "  v_add_f32_dpp v104, v104, v104 row_ror:8 row_mask:0xf bank_mask:0xf bound_ctrl:1\n"
    "  v_add_f32_dpp v105, v105, v105 row_ror:8 row_mask:0xf bank_mask:0xf bound_ctrl:1\n"
    "  v_add_f32_dpp v106, v106, v106 row_ror:8 row_mask:0xf bank_mask:0xf bound_ctrl:1\n"
    "  v_add_f32_dpp v107, v107, v107 row_ror:8 row_mask:0xf bank_mask:0xf bound_ctrl:1\n"
    "  v_mov_b32 v92, v104\n"
    "  v_mov_b32 v93, v105\n"
    "  v_mov_b32 v94, v106\n"
    "  v_mov_b32 v95, v107\n"
    "  v_permlane16_swap_b32 v104, v92\n"
    "  v_permlane16_swap_b32 v105, v93\n"
    "  v_permlane16_swap_b32 v106, v94\n"
    "  v_permlane16_swap_b32 v107, v95\n"
    "  v_add_f32 v104, v104, v92\n"
    "  v_add_f32 v105, v105, v93\n"
    "  v_add_f32 v106, v106, v94\n"
    "  v_add_f32 v107, v107, v95\n"
    "  v_mov_b32 v92, v104\n"
    "  v_mov_b32 v93, v105\n"
    "  v_mov_b32 v94, v106\n"
    "  v_mov_b32 v95, v107\n"
    "  v_permlane32_swap_b32 v104, v92\n"
    "  v_permlane32_swap_b32 v105, v93\n"
    "  v_permlane32_swap_b32 v106, v94\n"
    "  v_permlane32_swap_b32 v107, v95\n"
    "  v_add_f32 v104, v104, v92\n"
    "  v_add_f32 v105, v105, v93\n"
    "  v_add_f32 v106, v106, v94\n"
    "  v_add_f32 v107, v107, v95\n"
    "  v_cvt_pk_bf16_f32 v102, v104, v105\n"
    "  v_cvt_pk_bf16_f32 v103, v106, v107\n"
    "  s_mov_b32 exec_lo, 0x03030303\n"
    "  s_mov_b32 exec_hi, 0x03030303\n"
    "  buffer_store_dwordx2 v[102:103], v109, s[16:19], s39 offen offset:64 sc1\n"
    "  s_mov_b64 exec, -1\n"
    "Lrz_68%=:\n"
    "Lnr_65%=:\n"
    "  s_lshl_b32 s67, s43, 5\n"
    "  s_lshl3_add_u32 s67, s40, s67\n"
    "  s_lshl_b32 s67, s67, 10\n"
    "  s_add_u32 s24, s67, 0x600000\n"
    "  s_and_b32 s67, s2, 7\n"
    "  s_lshl_b32 s67, s67, 15\n"
    "  s_lshl_b32 s66, s40, 13\n"
    "  s_add_u32 s67, s67, s66\n"
    "  s_add_u32 s26, s67, 0x1c0000\n"
    "  v_lshrrev_b32 v12, 5, v1\n"
    "  v_and_b32 v13, 7, v1\n"
    "  v_lshlrev_b32 v13, 2, v13\n"
    "  v_bfe_u32 v14, v1, 3, 2\n"
    "  v_add_lshl_u32 v13, v13, v14, 4\n"
    "  v_lshl_add_u32 v15, v12, 9, v13\n"
    "  v_and_b32 v13, 15, v1\n"
    "  v_and_b32 v14, 3, v13\n"
    "  v_lshrrev_b32 v13, 2, v13\n"
    "  v_lshlrev_b32 v13, 7, v13\n"
    "  v_lshl_add_u32 v13, v12, 9, v13\n"
    "  v_lshl_add_u32 v10, v14, 4, v13\n"
    "  v_lshl_add_u32 v11, v12, 9, 64\n"
    "  s_add_u32 s25, s24, 4096\n"
    "  s_add_u32 s27, s26, 4096\n"
    "  s_mov_b32 s31, 0xffff0000\n"
    "  v_mov_b32 v13, s31\n"
    "  v_bfe_u32 v13, v13, v1, 1\n"
    "  v_cmp_ne_u32 vcc, 0, v13\n"
    "  s_nop 1\n"
    "  v_cndmask_b32 v12, v10, v11\n"
    "  v_and_b32 v97, 7, v1\n"
    "  v_lshlrev_b32 v97, 2, v97\n"
    "  v_bfe_u32 v96, v1, 3, 2\n"
    "  s_and_b32 s67, s43, 1\n"
    "  s_lshl_b32 s67, s67, 5\n"
    "  v_add3_u32 v102, v97, v96, s67\n"
    "  v_lshrrev_b32 v97, 5, v1\n"
    "  v_lshlrev_b32 v97, 9, v97\n"
    "  v_and_b32 v96, 15, v102\n"
    "  v_lshlrev_b32 v96, 1, v96\n"
    "  s_lshr_b32 s67, s40, 1\n"
    "  s_lshl1_add_u32 s67, s44, s67\n"
    "  s_sub_u32 s67, s67, s45\n"
    "  s_and_b32 s67, s67, 7\n"
    "  s_lshl_b32 s67, s67, 13\n"
    "  s_and_b32 s66, s40, 1\n"
    "  s_lshl_b32 s66, s66, 12\n"
    "  s_add_u32 s66, s67, s66\n"
    "  v_add3_u32 v103, v97, v96, s66\n"
    "  v_lshrrev_b32 v97, 4, v102\n"
    "  v_lshl_add_u32 v98, v97, 5, v103\n"
    "  v_lshrrev_b32 v97, 4, v102\n"
    "  v_xor_b32 v97, 1, v97\n"
    "  v_lshl_add_u32 v99, v97, 5, v103\n"
    "  v_lshrrev_b32 v97, 4, v102\n"
    "  v_xor_b32 v97, 2, v97\n"
    "  v_lshl_add_u32 v100, v97, 5, v103\n"
    "  v_lshrrev_b32 v97, 4, v102\n"
    "  v_xor_b32 v97, 3, v97\n"
    "  v_lshl_add_u32 v101, v97, 5, v103\n"
    "  ds_read_u16 v80, v98\n"
    "  ds_read_u16 v81, v98 offset:128\n"
    "  ds_read_u16 v82, v100 offset:256\n"
    "  ds_read_u16 v83, v100 offset:384\n"
    "  ds_read_u16 v84, v99 offset:1024\n"
    "  ds_read_u16 v85, v99 offset:1152\n"
    "  ds_read_u16 v86, v101 offset:1280\n"
    "  ds_read_u16 v87, v101 offset:1408\n"
    "  ds_read_u16 v88, v98 offset:2048\n"
    "  ds_read_u16 v89, v98 offset:2176\n"
    "  ds_read_u16 v90, v100 offset:2304\n"
    "  ds_read_u16 v91, v100 offset:2432\n"
    "  ds_read_u16 v92, v99 offset:3072\n"
    "  ds_read_u16 v93, v99 offset:3200\n"
    "  ds_read_u16 v94, v101 offset:3328\n"
    "  ds_read_u16 v95, v101 offset:3456\n"
    "  v_lshlrev_b32 v112, 16, v80\n"
    "  s_waitcnt lgkmcnt(8)\n"
    "  v_lshlrev_b32 v113, 16, v81\n"
    "  v_lshlrev_b32 v114, 16, v82\n"
    "  v_lshlrev_b32 v115, 16, v83\n"
    "  v_lshlrev_b32 v116, 16, v84\n"
    "  v_lshlrev_b32 v117, 16, v85\n"
    "  v_lshlrev_b32 v118, 16, v86\n"
    "  v_lshlrev_b32 v119, 16, v87\n"
    "  s_waitcnt lgkmcnt(6)\n"
    "  v_lshlrev_b32 v120, 16, v88\n"
    "  v_lshlrev_b32 v121, 16, v89\n"
    "  s_waitcnt lgkmcnt(5)\n"
    "  v_lshlrev_b32 v122, 16, v90\n"
    "  s_waitcnt lgkmcnt(4)\n"
    "  v_lshlrev_b32 v123, 16, v91\n"
    "  s_waitcnt lgkmcnt(3)\n"
    "  v_lshlrev_b32 v124, 16, v92\n"
    "  s_waitcnt lgkmcnt(2)\n"
    "  v_lshlrev_b32 v125, 16, v93\n"
    "  s_waitcnt lgkmcnt(1)\n"
    "  v_lshlrev_b32 v126, 16, v94\n"
    "  s_waitcnt lgkmcnt(0)\n"
    "  v_lshlrev_b32 v127, 16, v95\n"
    "  s_sleep 24\n"
    "  buffer_load_dwordx4 v[16:19], v15, s[16:19], s24 offen sc1\n"
    "  buffer_load_dwordx4 v[20:23], v15, s[16:19], s24 offen offset:1024 sc1\n"
    "  buffer_load_dwordx4 v[24:27], v15, s[16:19], s24 offen offset:2048 sc1\n"
    "  buffer_load_dwordx4 v[28:31], v15, s[16:19], s24 offen offset:3072 sc1\n"
    "  buffer_load_dwordx4 v[32:35], v15, s[16:19], s25 offen sc1\n"
    "  buffer_load_dwordx4 v[36:39], v15, s[16:19], s25 offen offset:1024 sc1\n"
    "  buffer_load_dwordx4 v[40:43], v15, s[16:19], s25 offen offset:2048 sc1\n"
    "  buffer_load_dwordx4 v[44:47], v15, s[16:19], s25 offen offset:3072 sc1\n"
    "  s_mov_b32 exec_lo, 0x0011ffff\n"
    "  s_mov_b32 exec_hi, 0x0011ffff\n"
    "  buffer_load_dwordx4 v[48:51], v12, s[16:19], s26 offen sc1\n"
    "  buffer_load_dwordx4 v[52:55], v12, s[16:19], s26 offen offset:1024 sc1\n"
    "  buffer_load_dwordx4 v[56:59], v12, s[16:19], s26 offen offset:2048 sc1\n"
    "  buffer_load_dwordx4 v[60:63], v12, s[16:19], s26 offen offset:3072 sc1\n"
    "  buffer_load_dwordx4 v[64:67], v12, s[16:19], s27 offen sc1\n"
    "  buffer_load_dwordx4 v[68:71], v12, s[16:19], s27 offen offset:1024 sc1\n"
    "  buffer_load_dwordx4 v[72:75], v12, s[16:19], s27 offen offset:2048 sc1\n"
    "  buffer_load_dwordx4 v[76:79], v12, s[16:19], s27 offen offset:3072 sc1\n"
    "  s_mov_b64 exec, -1\n"
    "  s_lshl_b32 s67, s40, 13\n"
    "  s_add_u32 s67, s67, 0x1a200\n"
    "  v_add_u32 v11, s67, v2\n"
    "  s_add_u32 s67, s40, 1\n"
    "  s_and_b32 s67, s67, 3\n"
    "  s_lshl_b32 s67, s67, 13\n"
    "  s_add_u32 s67, s67, 0x1a200\n"
    "  v_add_u32 v13, s67, v2\n"
    "  s_add_u32 s67, s40, 1\n"
    "  s_add_u32 s67, s67, 1\n"
    "  s_and_b32 s67, s67, 3\n"
    "  s_lshl_b32 s67, s67, 13\n"
    "  s_add_u32 s67, s67, 0x1a200\n"
    "  v_add_u32 v14, s67, v2\n"
    "  s_add_u32 s67, s40, 3\n"
    "  s_and_b32 s67, s67, 3\n"
    "  s_lshl_b32 s67, s67, 13\n"
    "  s_add_u32 s34, s67, 0x1a200\n"
    "  s_waitcnt vmcnt(0)\n"
    "  s_mov_b32 s55, 0x00008000\n"
    "Lac_69%=:\n"
    "  s_mov_b32 s54, 0\n"
    "  v_max3_u32 v10, v19, v23, v27\n"
    "  v_max3_u32 v10, v10, v31, v35\n"
    "  v_max3_u32 v10, v10, v39, v43\n"
    "  v_max_u32 v10, v10, v47\n"
    "  v_cmp_eq_u32 vcc, -1, v10\n"
    "  s_cbranch_vccz Laq_70%=\n"
    "  s_mov_b32 s54, 1\n"
    "Laq_70%=:\n"
    "  v_max3_u32 v10, v48, v49, v50\n"
    "  v_max3_u32 v10, v10, v51, v52\n"
    "  v_max3_u32 v10, v10, v53, v54\n"
    "  v_max3_u32 v10, v10, v55, v56\n"
    "  v_max3_u32 v10, v10, v57, v58\n"
    "  v_max3_u32 v10, v10, v59, v60\n"
    "  v_max3_u32 v10, v10, v61, v62\n"
    "  v_max3_u32 v10, v10, v63, v64\n"
    "  v_max3_u32 v10, v10, v65, v66\n"
    "  v_max3_u32 v10, v10, v67, v68\n"
    "  v_max3_u32 v10, v10, v69, v70\n"
    "  v_max3_u32 v10, v10, v71, v72\n"
    "  v_max3_u32 v10, v10, v73, v74\n"
    "  v_max3_u32 v10, v10, v75, v76\n"
    "  v_max3_u32 v10, v10, v77, v78\n"
    "  v_max_u32 v10, v10, v79\n"
    "  s_mov_b32 exec_lo, 0x0011ffff\n"
    "  s_mov_b32 exec_hi, 0x0011ffff\n"
    "  v_cmp_eq_u32 vcc, -1, v10\n"
    "  s_mov_b64 exec, -1\n"
    "  s_cbranch_vccz Lak_71%=\n"
    "  s_or_b32 s54, s54, 2\n"
    "Lak_71%=:\n"
    "  s_cmp_eq_u32 s54, 0\n"
    "  s_cbranch_scc1 Lao_72%=\n"
    "  s_sub_u32 s55, s55, 1\n"
    "  s_cbranch_scc1 Lao_72%=\n"
    "  s_sleep 2\n"
    "  s_bitcmp1_b32 s54, 1\n"
    "  s_cbranch_scc0 Laq2_73%=\n"
    "  s_mov_b32 exec_lo, 0x0011ffff\n"
    "  s_mov_b32 exec_hi, 0x0011ffff\n"
    "  v_max3_u32 v10, v48, v49, v50\n"
    "  v_max_u32 v10, v10, v51\n"
    "  v_cmp_eq_u32 vcc, -1, v10\n"
    "  s_cbranch_vccz Las_75%=\n"
    "  buffer_load_dwordx4 v[48:51], v12, s[16:19], s26 offen sc1\n"
    "Las_75%=:\n"
    "  v_max3_u32 v10, v52, v53, v54\n"
    "  v_max_u32 v10, v10, v55\n"
    "  v_cmp_eq_u32 vcc, -1, v10\n"
    "  s_cbranch_vccz Las_76%=\n"
    "  buffer_load_dwordx4 v[52:55], v12, s[16:19], s26 offen offset:1024 sc1\n"
    "Las_76%=:\n"
    "  v_max3_u32 v10, v56, v57, v58\n"
    "  v_max_u32 v10, v10, v59\n"
    "  v_cmp_eq_u32 vcc, -1, v10\n"
    "  s_cbranch_vccz Las_77%=\n"
    "  buffer_load_dwordx4 v[56:59], v12, s[16:19], s26 offen offset:2048 sc1\n"
    "Las_77%=:\n"
    "  v_max3_u32 v10, v60, v61, v62\n"
    "  v_max_u32 v10, v10, v63\n"
    "  v_cmp_eq_u32 vcc, -1, v10\n"
    "  s_cbranch_vccz Las_78%=\n"
    "  buffer_load_dwordx4 v[60:63], v12, s[16:19], s26 offen offset:3072 sc1\n"
    "Las_78%=:\n"
    "  v_max3_u32 v10, v64, v65, v66\n"
    "  v_max_u32 v10, v10, v67\n"
    "  v_cmp_eq_u32 vcc, -1, v10\n"
    "  s_cbranch_vccz Las_79%=\n"
    "  buffer_load_dwordx4 v[64:67], v12, s[16:19], s27 offen sc1\n"
    "Las_79%=:\n"
    "  v_max3_u32 v10, v68, v69, v70\n"
    "  v_max_u32 v10, v10, v71\n"
    "  v_cmp_eq_u32 vcc, -1, v10\n"
    "  s_cbranch_vccz Las_80%=\n"
    "  buffer_load_dwordx4 v[68:71], v12, s[16:19], s27 offen offset:1024 sc1\n"
    "Las_80%=:\n"
    "  v_max3_u32 v10, v72, v73, v74\n"
    "  v_max_u32 v10, v10, v75\n"
    "  v_cmp_eq_u32 vcc, -1, v10\n"
    "  s_cbranch_vccz Las_81%=\n"
    "  buffer_load_dwordx4 v[72:75], v12, s[16:19], s27 offen offset:2048 sc1\n"
    "Las_81%=:\n"
    "  v_max3_u32 v10, v76, v77, v78\n"
    "  v_max_u32 v10, v10, v79\n"
    "  v_cmp_eq_u32 vcc, -1, v10\n"
    "  s_cbranch_vccz Las_82%=\n"
    "  buffer_load_dwordx4 v[76:79], v12, s[16:19], s27 offen offset:3072 sc1\n"
    "Las_82%=:\n"
    "  s_mov_b64 exec, -1\n"
    "Laq2_73%=:\n"
    "  s_bitcmp1_b32 s54, 0\n"
    "  s_cbranch_scc0 Lak2_74%=\n"
    "  v_cmp_eq_u32 vcc, -1, v19\n"
    "  s_cbranch_vccz Las_83%=\n"
    "  buffer_load_dwordx4 v[16:19], v15, s[16:19], s24 offen sc1\n"
    "Las_83%=:\n"
    "  v_cmp_eq_u32 vcc, -1, v23\n"
    "  s_cbranch_vccz Las_84%=\n"
    "  buffer_load_dwordx4 v[20:23], v15, s[16:19], s24 offen offset:1024 sc1\n"
    "Las_84%=:\n"
    "  v_cmp_eq_u32 vcc, -1, v27\n"
    "  s_cbranch_vccz Las_85%=\n"
    "  buffer_load_dwordx4 v[24:27], v15, s[16:19], s24 offen offset:2048 sc1\n"
    "Las_85%=:\n"
    "  v_cmp_eq_u32 vcc, -1, v31\n"
    "  s_cbranch_vccz Las_86%=\n"
    "  buffer_load_dwordx4 v[28:31], v15, s[16:19], s24 offen offset:3072 sc1\n"
    "Las_86%=:\n"
    "  v_cmp_eq_u32 vcc, -1, v35\n"
    "  s_cbranch_vccz Las_87%=\n"
    "  buffer_load_dwordx4 v[32:35], v15, s[16:19], s25 offen sc1\n"
    "Las_87%=:\n"
    "  v_cmp_eq_u32 vcc, -1, v39\n"
    "  s_cbranch_vccz Las_88%=\n"
    "  buffer_load_dwordx4 v[36:39], v15, s[16:19], s25 offen offset:1024 sc1\n"
    "Las_88%=:\n"
    "  v_cmp_eq_u32 vcc, -1, v43\n"
    "  s_cbranch_vccz Las_89%=\n"
    "  buffer_load_dwordx4 v[40:43], v15, s[16:19], s25 offen offset:2048 sc1\n"
    "Las_89%=:\n"
    "  v_cmp_eq_u32 vcc, -1, v47\n"
    "  s_cbranch_vccz Las_90%=\n"
    "  buffer_load_dwordx4 v[44:47], v15, s[16:19], s25 offen offset:3072 sc1\n"
    "Las_90%=:\n"
    "Lak2_74%=:\n"
    "  s_waitcnt vmcnt(0)\n"
    "  s_branch Lac_69%=\n"
    "Lao_72%=:\n"
    "  v_add_u32 v10, s34, v2\n"
    "  v_mfma_f32_32x32x16_bf16 v[80:95], v[48:51], v[16:19], 0\n"
    "  v_mfma_f32_32x32x16_bf16 v[96:111], v[52:55], v[20:23], 0\n"
    "  s_nop 10\n"
    "  v_add_f32 v88, 0x358637bd, v88\n"
    "  v_rcp_f32 v88, v88\n"
    "  s_nop 0\n"
    "  v_pk_mul_f32 v[80:81], v[80:81], v[88:89] op_sel_hi:[1,0]\n"
    "  v_pk_mul_f32 v[82:83], v[82:83], v[88:89] op_sel_hi:[1,0]\n"
    "  v_pk_mul_f32 v[84:85], v[84:85], v[88:89] op_sel_hi:[1,0]\n"
    "  v_pk_mul_f32 v[86:87], v[86:87], v[88:89] op_sel_hi:[1,0]\n"
    "  v_cvt_pk_bf16_f32 v16, v80, v81\n"
    "  v_cvt_pk_bf16_f32 v17, v82, v83\n"
    "  v_cvt_pk_bf16_f32 v18, v84, v85\n"
    "  v_cvt_pk_bf16_f32 v19, v86, v87\n"
    "  v_mfma_f32_32x32x16_bf16 v[80:95], v[56:59], v[24:27], 0\n"
    "  ds_write_b128 v11, v[16:19]\n"
    "  v_mfma_f32_32x32x16_bf16 v[112:127], v[128:131], v[16:19], v[112:127]\n"
    "  v_add_f32 v104, 0x358637bd, v104\n"
    "  v_rcp_f32 v104, v104\n"
    "  s_nop 0\n"
    "  v_pk_mul_f32 v[96:97], v[96:97], v[104:105] op_sel_hi:[1,0]\n"
    "  v_pk_mul_f32 v[98:99], v[98:99], v[104:105] op_sel_hi:[1,0]\n"
    "  v_pk_mul_f32 v[100:101], v[100:101], v[104:105] op_sel_hi:[1,0]\n"
    "  v_pk_mul_f32 v[102:103], v[102:103], v[104:105] op_sel_hi:[1,0]\n"
    "  v_cvt_pk_bf16_f32 v20, v96, v97\n"
    "  v_cvt_pk_bf16_f32 v21, v98, v99\n"
    "  v_cvt_pk_bf16_f32 v22, v100, v101\n"
    "  v_cvt_pk_bf16_f32 v23, v102, v103\n"
    "  v_mfma_f32_32x32x16_bf16 v[96:111], v[60:63], v[28:31], 0\n"
    "  ds_write_b128 v11, v[20:23] offset:1024\n"
    "  v_mfma_f32_32x32x16_bf16 v[112:127], v[132:135], v[20:23], v[112:127]\n"
    "  v_add_f32 v88, 0x358637bd, v88\n"
    "  v_rcp_f32 v88, v88\n"
    "  s_nop 0\n"
    "  v_pk_mul_f32 v[80:81], v[80:81], v[88:89] op_sel_hi:[1,0]\n"
    "  v_pk_mul_f32 v[82:83], v[82:83], v[88:89] op_sel_hi:[1,0]\n"
    "  v_pk_mul_f32 v[84:85], v[84:85], v[88:89] op_sel_hi:[1,0]\n"
    "  v_pk_mul_f32 v[86:87], v[86:87], v[88:89] op_sel_hi:[1,0]\n"
    "  v_cvt_pk_bf16_f32 v24, v80, v81\n"
    "  v_cvt_pk_bf16_f32 v25, v82, v83\n"
    "  v_cvt_pk_bf16_f32 v26, v84, v85\n"
    "  v_cvt_pk_bf16_f32 v27, v86, v87\n"
    "  v_mfma_f32_32x32x16_bf16 v[80:95], v[64:67], v[32:35], 0\n"
    "  ds_write_b128 v11, v[24:27] offset:2048\n"
    "  v_mfma_f32_32x32x16_bf16 v[112:127], v[136:139], v[24:27], v[112:127]\n"
    "  v_add_f32 v104, 0x358637bd, v104\n"
    "  v_rcp_f32 v104, v104\n"
    "  s_nop 0\n"
    "  v_pk_mul_f32 v[96:97], v[96:97], v[104:105] op_sel_hi:[1,0]\n"
    "  v_pk_mul_f32 v[98:99], v[98:99], v[104:105] op_sel_hi:[1,0]\n"
    "  v_pk_mul_f32 v[100:101], v[100:101], v[104:105] op_sel_hi:[1,0]\n"
    "  v_pk_mul_f32 v[102:103], v[102:103], v[104:105] op_sel_hi:[1,0]\n"
    "  v_cvt_pk_bf16_f32 v28, v96, v97\n"
    "  v_cvt_pk_bf16_f32 v29, v98, v99\n"
    "  v_cvt_pk_bf16_f32 v30, v100, v101\n"
    "  v_cvt_pk_bf16_f32 v31, v102, v103\n"
    "  v_mfma_f32_32x32x16_bf16 v[96:111], v[68:71], v[36:39], 0\n"
    "  ds_write_b128 v11, v[28:31] offset:3072\n"
    "  v_mfma_f32_32x32x16_bf16 v[112:127], v[140:143], v[28:31], v[112:127]\n"
    "  v_add_f32 v88, 0x358637bd, v88\n"
    "  v_rcp_f32 v88, v88\n"
    "  s_nop 0\n"
    "  v_pk_mul_f32 v[80:81], v[80:81], v[88:89] op_sel_hi:[1,0]\n"
    "  v_pk_mul_f32 v[82:83], v[82:83], v[88:89] op_sel_hi:[1,0]\n"
    "  v_pk_mul_f32 v[84:85], v[84:85], v[88:89] op_sel_hi:[1,0]\n"
    "  v_pk_mul_f32 v[86:87], v[86:87], v[88:89] op_sel_hi:[1,0]\n"
    "  v_cvt_pk_bf16_f32 v32, v80, v81\n"
    "  v_cvt_pk_bf16_f32 v33, v82, v83\n"
    "  v_cvt_pk_bf16_f32 v34, v84, v85\n"
    "  v_cvt_pk_bf16_f32 v35, v86, v87\n"
    "  v_mfma_f32_32x32x16_bf16 v[80:95], v[72:75], v[40:43], 0\n"
    "  ds_write_b128 v11, v[32:35] offset:4096\n"
    "  v_mfma_f32_32x32x16_bf16 v[112:127], v[144:147], v[32:35], v[112:127]\n"
    "  v_add_f32 v104, 0x358637bd, v104\n"
    "  v_rcp_f32 v104, v104\n"
    "  s_nop 0\n"
    "  v_pk_mul_f32 v[96:97], v[96:97], v[104:105] op_sel_hi:[1,0]\n"
    "  v_pk_mul_f32 v[98:99], v[98:99], v[104:105] op_sel_hi:[1,0]\n"
    "  v_pk_mul_f32 v[100:101], v[100:101], v[104:105] op_sel_hi:[1,0]\n"
    "  v_pk_mul_f32 v[102:103], v[102:103], v[104:105] op_sel_hi:[1,0]\n"
    "  v_cvt_pk_bf16_f32 v36, v96, v97\n"
    "  v_cvt_pk_bf16_f32 v37, v98, v99\n"
    "  v_cvt_pk_bf16_f32 v38, v100, v101\n"
    "  v_cvt_pk_bf16_f32 v39, v102, v103\n"
    "  v_mfma_f32_32x32x16_bf16 v[96:111], v[76:79], v[44:47], 0\n"
    "  ds_write_b128 v11, v[36:39] offset:5120\n"
    "  v_mfma_f32_32x32x16_bf16 v[112:127], v[148:151], v[36:39], v[112:127]\n"
    "  v_add_f32 v88, 0x358637bd, v88\n"
    "  v_rcp_f32 v88, v88\n"
    "  s_nop 0\n"
    "  v_pk_mul_f32 v[80:81], v[80:81], v[88:89] op_sel_hi:[1,0]\n"
    "  v_pk_mul_f32 v[82:83], v[82:83], v[88:89] op_sel_hi:[1,0]\n"
    "  v_pk_mul_f32 v[84:85], v[84:85], v[88:89] op_sel_hi:[1,0]\n"
    "  v_pk_mul_f32 v[86:87], v[86:87], v[88:89] op_sel_hi:[1,0]\n"
    "  v_cvt_pk_bf16_f32 v40, v80, v81\n"
    "  v_cvt_pk_bf16_f32 v41, v82, v83\n"
    "  v_cvt_pk_bf16_f32 v42, v84, v85\n"
    "  v_cvt_pk_bf16_f32 v43, v86, v87\n"
    "  ds_write_b128 v11, v[40:43] offset:6144\n"
    "  v_add_f32 v104, 0x358637bd, v104\n"
    "  v_rcp_f32 v104, v104\n"
    "  s_nop 0\n"
    "  v_pk_mul_f32 v[96:97], v[96:97], v[104:105] op_sel_hi:[1,0]\n"
    "  v_pk_mul_f32 v[98:99], v[98:99], v[104:105] op_sel_hi:[1,0]\n"
    "  v_pk_mul_f32 v[100:101], v[100:101], v[104:105] op_sel_hi:[1,0]\n"
    "  v_pk_mul_f32 v[102:103], v[102:103], v[104:105] op_sel_hi:[1,0]\n"
    "  v_cvt_pk_bf16_f32 v44, v96, v97\n"
    "  v_cvt_pk_bf16_f32 v45, v98, v99\n"
    "  v_cvt_pk_bf16_f32 v46, v100, v101\n"
    "  v_cvt_pk_bf16_f32 v47, v102, v103\n"
    "  ds_write_b128 v11, v[44:47] offset:7168\n"
    "  s_waitcnt lgkmcnt(0)\n"
    "  s_barrier\n"
    "  ds_read_b128 v[16:19], v13\n"
    "  ds_read_b128 v[20:23], v13 offset:1024\n"
    "  ds_read_b128 v[24:27], v13 offset:2048\n"
    "  ds_read_b128 v[28:31], v13 offset:3072\n"
    "  ds_read_b128 v[32:35], v13 offset:4096\n"
    "  ds_read_b128 v[36:39], v13 offset:5120\n"
    "  v_mfma_f32_32x32x16_bf16 v[112:127], v[152:155], v[40:43], v[112:127]\n"
    "  ds_read_b128 v[40:43], v13 offset:6144\n"
    "  v_mfma_f32_32x32x16_bf16 v[112:127], v[156:159], v[44:47], v[112:127]\n"
    "  ds_read_b128 v[44:47], v13 offset:7168\n"
    "  s_waitcnt lgkmcnt(6)\n"
    "  v_mfma_f32_32x32x16_bf16 v[112:127], v[160:163], v[16:19], v[112:127]\n"
    "  ds_read_b128 v[16:19], v14\n"
    "  s_lshl_b32 s67, s44, 2\n"
    "  s_lshl4_add_u32 s67, s43, s67\n"
    "  s_add_u32 s67, s67, s40\n"
    "  v_mfma_f32_32x32x16_bf16 v[112:127], v[164:167], v[20:23], v[112:127]\n"
    "  ds_read_b128 v[20:23], v14 offset:1024\n"
    "  s_lshl_b32 s67, s67, 8\n"
    "  s_add_u32 s51, s67, 0x80000\n"
    "  s_lshl_b32 s67, s43, 12\n"
    "  s_waitcnt lgkmcnt(6)\n"
    "  v_mfma_f32_32x32x16_bf16 v[112:127], v[168:171], v[24:27], v[112:127]\n"
    "  ds_read_b128 v[24:27], v14 offset:2048\n"
    "  s_add_u32 s33, s67, 0x80000\n"
    "  s_lshl_b32 s67, s44, 6\n"
    "  s_lshl4_add_u32 s67, s40, s67\n"
    "  v_mfma_f32_32x32x16_bf16 v[112:127], v[172:175], v[28:31], v[112:127]\n"
    "  ds_read_b128 v[28:31], v14 offset:3072\n"
    "  s_lshl_b32 s67, s67, 13\n"
    "  s_lshl_b32 s66, s43, 7\n"
    "  s_add_u32 s66, s67, s66\n"
    "  s_waitcnt lgkmcnt(6)\n"
    "  v_mfma_f32_32x32x16_bf16 v[112:127], v[176:179], v[32:35], v[112:127]\n"
    "  ds_read_b128 v[32:35], v14 offset:4096\n"
    "  s_add_u32 s32, s66, 0xa00000\n"
    "  v_mfma_f32_32x32x16_bf16 v[112:127], v[180:183], v[36:39], v[112:127]\n"
    "  ds_read_b128 v[36:39], v14 offset:5120\n"
    "  v_and_b32 v83, 7, v1\n"
    "  v_lshlrev_b32 v83, 2, v83\n"
    "  v_bfe_u32 v82, v1, 3, 2\n"
    "  s_waitcnt lgkmcnt(6)\n"
    "  v_mfma_f32_32x32x16_bf16 v[112:127], v[184:187], v[40:43], v[112:127]\n"
    "  ds_read_b128 v[40:43], v14 offset:6144\n"
    "  v_add_lshl_u32 v13, v83, v82, 3\n"
    "  v_lshrrev_b32 v83, 5, v1\n"
    "  v_lshl_add_u32 v15, v83, 11, v13\n"
    "  v_mfma_f32_32x32x16_bf16 v[112:127], v[188:191], v[44:47], v[112:127]\n"
    "  ds_read_b128 v[44:47], v14 offset:7168\n"
    "  s_waitcnt lgkmcnt(6)\n"
    "  v_mfma_f32_32x32x16_bf16 v[112:127], v[192:195], v[16:19], v[112:127]\n"
    "  ds_read_b128 v[16:19], v10\n"
    "  v_lshrrev_b32 v83, 3, v1\n"
    "  v_bfe_u32 v82, v83, 1, 1\n"
    "  v_lshlrev_b32 v82, 2, v82\n"
    "  v_mfma_f32_32x32x16_bf16 v[112:127], v[196:199], v[20:23], v[112:127]\n"
    "  ds_read_b128 v[20:23], v10 offset:1024\n"
    "  v_lshrrev_b32 v81, 5, v1\n"
    "  v_lshlrev_b32 v81, 1, v81\n"
    "  v_and_b32 v83, 3, v83\n"
    "  s_waitcnt lgkmcnt(6)\n"
    "  v_mfma_f32_32x32x16_bf16 v[112:127], v[200:203], v[24:27], v[112:127]\n"
    "  ds_read_b128 v[24:27], v10 offset:2048\n"
    "  v_and_b32 v83, 1, v83\n"
    "  v_add3_u32 v83, v82, v81, v83\n"
    "  v_and_b32 v82, 7, v1\n"
    "  v_mfma_f32_32x32x16_bf16 v[112:127], v[204:207], v[28:31], v[112:127]\n"
    "  ds_read_b128 v[28:31], v10 offset:3072\n"
    "  v_lshlrev_b32 v82, 4, v82\n"
    "  v_lshl_add_u32 v14, v83, 13, v82\n"
    "  s_waitcnt lgkmcnt(6)\n"
    "  v_mfma_f32_32x32x16_bf16 v[112:127], v[208:211], v[32:35], v[112:127]\n"
    "  ds_read_b128 v[32:35], v10 offset:4096\n"
    "  v_mfma_f32_32x32x16_bf16 v[112:127], v[212:215], v[36:39], v[112:127]\n"
    "  ds_read_b128 v[36:39], v10 offset:5120\n"
    "  s_waitcnt lgkmcnt(6)\n"
    "  v_mfma_f32_32x32x16_bf16 v[112:127], v[216:219], v[40:43], v[112:127]\n"
    "  ds_read_b128 v[40:43], v10 offset:6144\n"
    "  v_mfma_f32_32x32x16_bf16 v[112:127], v[220:223], v[44:47], v[112:127]\n"
    "  ds_read_b128 v[44:47], v10 offset:7168\n"
    "  s_waitcnt lgkmcnt(6)\n"
    "  v_mfma_f32_32x32x16_bf16 v[112:127], v[224:227], v[16:19], v[112:127]\n"
    "  v_mfma_f32_32x32x16_bf16 v[112:127], v[228:231], v[20:23], v[112:127]\n"
    "  s_waitcnt lgkmcnt(5)\n"
    "  v_mfma_f32_32x32x16_bf16 v[112:127], v[232:235], v[24:27], v[112:127]\n"
    "  s_waitcnt lgkmcnt(4)\n"
    "  v_mfma_f32_32x32x16_bf16 v[112:127], v[236:239], v[28:31], v[112:127]\n"
    "  s_waitcnt lgkmcnt(3)\n"
    "  v_mfma_f32_32x32x16_bf16 v[112:127], v[240:243], v[32:35], v[112:127]\n"
    "  s_waitcnt lgkmcnt(2)\n"
    "  v_mfma_f32_32x32x16_bf16 v[112:127], v[244:247], v[36:39], v[112:127]\n"
    "  s_waitcnt lgkmcnt(1)\n"
    "  v_mfma_f32_32x32x16_bf16 v[112:127], v[248:251], v[40:43], v[112:127]\n"
    "  s_waitcnt lgkmcnt(0)\n"
    "  v_mfma_f32_32x32x16_bf16 v[112:127], v[252:255], v[44:47], v[112:127]\n"
    "  s_nop 11\n"
    "  v_cvt_pk_bf16_f32 v16, v112, v113\n"
    "  v_cvt_pk_bf16_f32 v17, v114, v115\n"
    "  v_cvt_pk_bf16_f32 v18, v116, v117\n"
    "  v_cvt_pk_bf16_f32 v19, v118, v119\n"
    "  v_cvt_pk_bf16_f32 v20, v120, v121\n"
    "  v_cvt_pk_bf16_f32 v21, v122, v123\n"
    "  v_cvt_pk_bf16_f32 v22, v124, v125\n"
    "  v_cvt_pk_bf16_f32 v23, v126, v127\n"
    "  v_mov_b32 v28, 0\n"
    "  v_mov_b32 v29, 0\n"
    "  v_dot2c_f32_bf16 v28, 0x3f803f80, v16\n"
    "  v_dot2c_f32_bf16 v29, v16, v16\n"
    "  v_dot2c_f32_bf16 v28, 0x3f803f80, v17\n"
    "  v_dot2c_f32_bf16 v29, v17, v17\n"
    "  v_dot2c_f32_bf16 v28, 0x3f803f80, v18\n"
    "  v_dot2c_f32_bf16 v29, v18, v18\n"
    "  v_dot2c_f32_bf16 v28, 0x3f803f80, v19\n"
    "  v_dot2c_f32_bf16 v29, v19, v19\n"
    "  v_dot2c_f32_bf16 v28, 0x3f803f80, v20\n"
    "  v_dot2c_f32_bf16 v29, v20, v20\n"
    "  v_dot2c_f32_bf16 v28, 0x3f803f80, v21\n"
    "  v_dot2c_f32_bf16 v29, v21, v21\n"
    "  v_dot2c_f32_bf16 v28, 0x3f803f80, v22\n"
    "  v_dot2c_f32_bf16 v29, v22, v22\n"
    "  v_dot2c_f32_bf16 v28, 0x3f803f80, v23\n"
    "  v_dot2c_f32_bf16 v29, v23, v23\n"
    "  s_nop 1\n"
    "  v_mov_b32 v30, v28\n"
    "  v_mov_b32 v31, v29\n"
    "  s_nop 0\n"
    "  v_permlane32_swap_b32 v28, v30\n"
    "  v_permlane32_swap_b32 v29, v31\n"
    "  v_add_f32 v28, v28, v30\n"
    "  v_add_f32 v29, v29, v31\n"
    "  s_mov_b32 exec_lo, 0xffffffff\n"
    "  s_mov_b32 exec_hi, 0x00000000\n"
    "  buffer_store_dwordx2 v[28:29], v13, s[16:19], s51 offen sc1\n"
    "  s_mov_b64 exec, -1\n"
    "  s_add_u32 s53, s32, 0x00010000\n"
    "  s_nop 3\n"
    "  v_permlane16_swap_b32 v16, v18\n"
    "  v_permlane16_swap_b32 v17, v19\n"
    "  v_permlane16_swap_b32 v20, v22\n"
    "  v_permlane16_swap_b32 v21, v23\n"
    "  v_mov_b32 v24, v16\n"
    "  v_mov_b32 v25, v18\n"
    "  v_mov_b32 v26, v20\n"
    "  v_mov_b32 v27, v22\n"
    "  v_mov_b32_dpp v16, v17 row_ror:8 row_mask:0xf bank_mask:0xc\n"
    "  v_mov_b32_dpp v18, v19 row_ror:8 row_mask:0xf bank_mask:0xc\n"
    "  v_mov_b32_dpp v20, v21 row_ror:8 row_mask:0xf bank_mask:0xc\n"
    "  v_mov_b32_dpp v22, v23 row_ror:8 row_mask:0xf bank_mask:0xc\n"
    "  v_mov_b32_dpp v17, v24 row_ror:8 row_mask:0xf bank_mask:0x3\n"
    "  v_mov_b32_dpp v19, v25 row_ror:8 row_mask:0xf bank_mask:0x3\n"
    "  v_mov_b32_dpp v21, v26 row_ror:8 row_mask:0xf bank_mask:0x3\n"
    "  v_mov_b32_dpp v23, v27 row_ror:8 row_mask:0xf bank_mask:0x3\n"
    "  buffer_store_dwordx4 v[16:19], v14, s[16:19], s32 offen sc1\n"
    "  buffer_store_dwordx4 v[20:23], v14, s[16:19], s53 offen sc1\n"
    "  s_add_u32 s52, s32, 0x00400000\n"
    "  s_add_u32 s53, s32, 0x00410000\n"
    "  s_sleep 8\n"
    "  buffer_load_dwordx2 v[32:33], v15, s[16:19], s33 offen sc1\n"
    "  buffer_load_dwordx2 v[34:35], v15, s[16:19], s33 offen offset:256 sc1\n"
    "  buffer_load_dwordx2 v[36:37], v15, s[16:19], s33 offen offset:512 sc1\n"
    "  buffer_load_dwordx2 v[38:39], v15, s[16:19], s33 offen offset:768 sc1\n"
    "  buffer_load_dwordx2 v[40:41], v15, s[16:19], s33 offen offset:1024 sc1\n"
    "  buffer_load_dwordx2 v[42:43], v15, s[16:19], s33 offen offset:1280 sc1\n"
    "  buffer_load_dwordx2 v[44:45], v15, s[16:19], s33 offen offset:1536 sc1\n"
    "  buffer_load_dwordx2 v[46:47], v15, s[16:19], s33 offen offset:1792 sc1\n"
    "  s_waitcnt vmcnt(0)\n"
    "  s_mov_b32 s55, 0x00008000\n"
    "Lc_91%=:\n"
    "  v_max3_u32 v10, v33, v35, v37\n"
    "  v_max3_u32 v10, v10, v39, v41\n"
    "  v_max3_u32 v10, v10, v43, v45\n"
    "  v_max_u32 v10, v10, v47\n"
    "  v_cmp_eq_u32 vcc, -1, v10\n"
    "  s_cbranch_vccz Lo_92%=\n"
    "  s_sub_u32 s55, s55, 1\n"
    "  s_cbranch_scc1 Lo_92%=\n"
    "  s_sleep 2\n"
    "  buffer_load_dwordx2 v[32:33], v15, s[16:19], s33 offen sc1\n"
    "  buffer_load_dwordx2 v[34:35], v15, s[16:19], s33 offen offset:256 sc1\n"
    "  buffer_load_dwordx2 v[36:37], v15, s[16:19], s33 offen offset:512 sc1\n"
    "  buffer_load_dwordx2 v[38:39], v15, s[16:19], s33 offen offset:768 sc1\n"
    "  buffer_load_dwordx2 v[40:41], v15, s[16:19], s33 offen offset:1024 sc1\n"
    "  buffer_load_dwordx2 v[42:43], v15, s[16:19], s33 offen offset:1280 sc1\n"
    "  buffer_load_dwordx2 v[44:45], v15, s[16:19], s33 offen offset:1536 sc1\n"
    "  buffer_load_dwordx2 v[46:47], v15, s[16:19], s33 offen offset:1792 sc1\n"
    "  s_waitcnt vmcnt(0)\n"
    "  s_branch Lc_91%=\n"
    "Lo_92%=:\n"
    "  v_pk_add_f32 v[32:33], v[32:33], v[34:35]\n"
    "  v_pk_add_f32 v[36:37], v[36:37], v[38:39]\n"
    "  v_pk_add_f32 v[40:41], v[40:41], v[42:43]\n"
    "  v_pk_add_f32 v[44:45], v[44:45], v[46:47]\n"
    "  v_pk_add_f32 v[32:33], v[32:33], v[36:37]\n"
    "  v_pk_add_f32 v[40:41], v[40:41], v[44:45]\n"
    "  v_pk_add_f32 v[32:33], v[32:33], v[40:41]\n"
    "  v_mov_b32 v30, v32\n"
    "  v_mov_b32 v31, v33\n"
    "  s_nop 0\n"
    "  v_permlane32_swap_b32 v32, v30\n"
    "  v_permlane32_swap_b32 v33, v31\n"
    "  v_add_f32 v32, v32, v30\n"
    "  v_add_f32 v33, v33, v31\n"
    "  v_mul_f32 v32, 0x3b000000, v32\n"
    "  v_mul_f32 v33, 0x3b000000, v33\n"
    "  v_fma_f32 v33, -v32, v32, v33\n"
    "  v_max_f32 v33, 0, v33\n"
    "  v_cvt_pk_bf16_f32 v32, v32, v32\n"
    "  v_and_b32 v32, 0xffff0000, v32\n"
    "  v_cvt_pk_bf16_f32 v33, v33, v33\n"
    "  v_and_b32 v33, 0xffff0000, v33\n"
    "  v_add_f32 v33, 0x3727c5ac, v33\n"
    "  v_cvt_pk_bf16_f32 v33, v33, v33\n"
    "  v_and_b32 v33, 0xffff0000, v33\n"
    "  v_sqrt_f32 v33, v33\n"
    "  s_nop 0\n"
    "  v_cvt_pk_bf16_f32 v33, v33, v33\n"
    "  v_and_b32 v33, 0xffff0000, v33\n"
    "  v_rcp_f32 v33, v33\n"
    "  s_nop 0\n"
    "  v_mul_f32 v34, 0x3fb8aa3b, v33\n"
    "  v_mul_f32 v34, v32, v33\n"
    "  v_pk_fma_f32 v[112:113], v[112:113], v[32:33], v[34:35] op_sel:[0,1,0] op_sel_hi:[1,1,0] neg_lo:[0,0,1] neg_hi:[0,0,1]\n"
    "  v_pk_fma_f32 v[114:115], v[114:115], v[32:33], v[34:35] op_sel:[0,1,0] op_sel_hi:[1,1,0] neg_lo:[0,0,1] neg_hi:[0,0,1]\n"
    "  v_pk_fma_f32 v[116:117], v[116:117], v[32:33], v[34:35] op_sel:[0,1,0] op_sel_hi:[1,1,0] neg_lo:[0,0,1] neg_hi:[0,0,1]\n"
    "  v_pk_fma_f32 v[118:119], v[118:119], v[32:33], v[34:35] op_sel:[0,1,0] op_sel_hi:[1,1,0] neg_lo:[0,0,1] neg_hi:[0,0,1]\n"
    "  v_pk_fma_f32 v[120:121], v[120:121], v[32:33], v[34:35] op_sel:[0,1,0] op_sel_hi:[1,1,0] neg_lo:[0,0,1] neg_hi:[0,0,1]\n"
    "  v_pk_fma_f32 v[122:123], v[122:123], v[32:33], v[34:35] op_sel:[0,1,0] op_sel_hi:[1,1,0] neg_lo:[0,0,1] neg_hi:[0,0,1]\n"
    "  v_pk_fma_f32 v[124:125], v[124:125], v[32:33], v[34:35] op_sel:[0,1,0] op_sel_hi:[1,1,0] neg_lo:[0,0,1] neg_hi:[0,0,1]\n"
    "  v_pk_fma_f32 v[126:127], v[126:127], v[32:33], v[34:35] op_sel:[0,1,0] op_sel_hi:[1,1,0] neg_lo:[0,0,1] neg_hi:[0,0,1]\n"
    "  v_cvt_pk_bf16_f32 v16, v112, v113\n"
    "  v_cvt_pk_bf16_f32 v17, v114, v115\n"
    "  v_cvt_pk_bf16_f32 v18, v116, v117\n"
    "  v_cvt_pk_bf16_f32 v19, v118, v119\n"
    "  v_cvt_pk_bf16_f32 v20, v120, v121\n"
    "  v_cvt_pk_bf16_f32 v21, v122, v123\n"
    "  v_cvt_pk_bf16_f32 v22, v124, v125\n"
    "  v_cvt_pk_bf16_f32 v23, v126, v127\n"
    "  v_permlane16_swap_b32 v16, v18\n"
    "  v_permlane16_swap_b32 v17, v19\n"
    "  v_permlane16_swap_b32 v20, v22\n"
    "  v_permlane16_swap_b32 v21, v23\n"
    "  v_mov_b32 v24, v16\n"
    "  v_mov_b32 v25, v18\n"
    "  v_mov_b32 v26, v20\n"
    "  v_mov_b32 v27, v22\n"
    "  v_mov_b32_dpp v16, v17 row_ror:8 row_mask:0xf bank_mask:0xc\n"
    "  v_mov_b32_dpp v18, v19 row_ror:8 row_mask:0xf bank_mask:0xc\n"
    "  v_mov_b32_dpp v20, v21 row_ror:8 row_mask:0xf bank_mask:0xc\n"
    "  v_mov_b32_dpp v22, v23 row_ror:8 row_mask:0xf bank_mask:0xc\n"
    "  v_mov_b32_dpp v17, v24 row_ror:8 row_mask:0xf bank_mask:0x3\n"
    "  v_mov_b32_dpp v19, v25 row_ror:8 row_mask:0xf bank_mask:0x3\n"
    "  v_mov_b32_dpp v21, v26 row_ror:8 row_mask:0xf bank_mask:0x3\n"
    "  v_mov_b32_dpp v23, v27 row_ror:8 row_mask:0xf bank_mask:0x3\n"
    "  buffer_store_dwordx4 v[16:19], v14, s[16:19], s52 offen sc1\n"
    "  buffer_store_dwordx4 v[20:23], v14, s[16:19], s53 offen sc1\n"
    "  v_lshlrev_b32 v120, 4, v0\n"
    "  s_lshl_b32 s67, s2, 13\n"
    "  s_add_u32 s48, s67, 0xe00000\n"
    "  s_add_u32 s49, s48, 4096\n"
    "  s_sleep 18\n"
    "  buffer_load_dwordx4 v[112:115], v120, s[16:19], s48 offen sc1\n"
    "  buffer_load_dwordx4 v[116:119], v120, s[16:19], s49 offen sc1\n"
    "  v_mov_b32 v124, 0\n"
    "  v_mov_b32 v125, 0\n"
    "  v_mov_b32 v126, 0\n"
    "  v_mov_b32 v127, 0\n"
    "  v_and_b32 v122, 31, v1\n"
    "  v_lshrrev_b32 v121, 5, v1\n"
    "  v_mul_u32_u24 v121, 0x1200, v121\n"
    "  v_lshl_add_u32 v123, v122, 4, v121\n"
    "  s_cmp_eq_u32 s40, 0\n"
    "  s_cselect_b64 exec, -1, 0\n"
    "  ds_write_b128 v123, v[124:127]\n"
    "  s_mov_b64 exec, -1\n"
    "  v_and_b32 v51, 15, v1\n"
    "  v_lshrrev_b32 v50, 4, v1\n"
    "  v_lshlrev_b32 v50, 4, v50\n"
    "  v_lshl_add_u32 v14, v51, 6, v50\n"
    "  s_lshl_b32 s67, s2, 13\n"
    "  s_add_u32 s50, s67, 0xa00000\n"
    "  s_and_b32 s67, s40, 1\n"
    "  s_lshl_b32 s67, s67, 1\n"
    "  s_sub_u32 s67, 7, s67\n"
    "  s_lshl_b32 s67, s67, 10\n"
    "  s_add_u32 s34, s67, s50\n"
    "  s_and_b32 s67, s40, 1\n"
    "  s_lshl_b32 s67, s67, 1\n"
    "  s_sub_u32 s67, 6, s67\n"
    "  s_lshl_b32 s67, s67, 10\n"
    "  s_add_u32 s35, s67, s50\n"
    "  s_and_b32 s67, s40, 1\n"
    "  s_lshl1_add_u32 s67, s67, 1\n"
    "  s_lshl_b32 s67, s67, 10\n"
    "  s_add_u32 s36, s67, s50\n"
    "  s_and_b32 s67, s40, 1\n"
    "  s_lshl_b32 s67, s67, 1\n"
    "  s_lshl_b32 s67, s67, 10\n"
    "  s_add_u32 s37, s67, s50\n"
    "  v_and_b32 v51, 3, v1\n"
    "  v_bfe_u32 v50, v1, 2, 2\n"
    "  v_sub_u32 v49, 4, v50\n"
    "  v_and_b32 v49, 3, v49\n"
    "  v_mul_u32_u24 v48, 0x1040, v49\n"
    "  s_lshr_b32 s67, s40, 1\n"
    "  s_mul_i32 s67, s67, 0x4100\n"
    "  s_and_b32 s66, s40, 1\n"
    "  s_lshl_b32 s66, s66, 8\n"
    "  s_add_u32 s66, s67, s66\n"
    "  s_add_u32 s66, s66, 0x10058\n"
    "  v_add_u32 v48, s66, v48\n"
    "  v_lshl_add_u32 v48, v51, 3, v48\n"
    "  v_lshrrev_b32 v51, 4, v1\n"
    "  v_add_lshl_u32 v50, v50, v49, 1\n"
    "  v_lshl_add_u32 v51, v51, 4, v50\n"
    "  v_sub_u32 v10, v48, v51\n"
    "  v_and_b32 v51, 15, v1\n"
    "  v_lshlrev_b32 v51, 5, v51\n"
    "  v_lshrrev_b32 v50, 4, v1\n"
    "  v_lshlrev_b32 v50, 4, v50\n"
    "  s_lshr_b32 s67, s40, 1\n"
    "  s_mul_i32 s67, s67, 0x1200\n"
    "  s_and_b32 s66, s40, 1\n"
    "  s_sub_u32 s66, 1, s66\n"
    "  s_lshl_b32 s66, s66, 8\n"
    "  s_add_u32 s66, s67, s66\n"
    "  s_add_u32 s66, s66, 32\n"
    "  v_add3_u32 v11, v51, v50, s66\n"
    "  v_and_b32 v51, 15, v1\n"
    "  v_lshlrev_b32 v51, 5, v51\n"
    "  v_lshrrev_b32 v50, 4, v1\n"
    "  v_lshlrev_b32 v50, 3, v50\n"
    "  s_lshr_b32 s67, s40, 1\n"
    "  s_mul_i32 s67, s67, 0x1200\n"
    "  s_add_u32 s67, s67, 0x200\n"
    "  v_add3_u32 v12, v51, v50, s67\n"
    "  v_lshrrev_b32 v51, 4, v1\n"
    "  v_lshrrev_b32 v50, 1, v51\n"
    "  v_and_b32 v49, 15, v1\n"
    "  v_and_b32 v48, 3, v49\n"
    "  v_and_b32 v51, 1, v51\n"
    "  v_sub_u32 v51, 1, v51\n"
    "  v_lshrrev_b32 v49, 2, v49\n"
    "  v_lshlrev_b32 v49, 16, v49\n"
    "  v_lshl_add_u32 v49, v51, 18, v49\n"
    "  v_lshl_add_u32 v49, v48, 5, v49\n"
    "  v_lshl_add_u32 v13, v50, 4, v49\n"
    "  s_lshr_b32 s67, s40, 1\n"
    "  s_lshl_b32 s66, s67, 17\n"
    "  s_lshl_b32 s67, s67, 25\n"
    "  s_add_u32 s67, s66, s67\n"
    "  s_add_u32 s54, s67, 0x5040c0c\n"
    "  s_and_b32 s67, s40, 1\n"
    "  s_sub_u32 s67, 3, s67\n"
    "  s_lshl_b32 s67, s67, 19\n"
    "  s_lshr_b32 s66, s40, 1\n"
    "  s_lshl1_add_u32 s66, s2, s66\n"
    "  s_lshl_b32 s66, s66, 7\n"
    "  s_add_u32 s66, s67, s66\n"
    "  s_add_u32 s32, s66, 0x1200000\n"
    "  s_and_b32 s67, s40, 1\n"
    "  s_lshl_b32 s67, s67, 19\n"
    "  s_lshr_b32 s66, s40, 1\n"
    "  s_lshl1_add_u32 s66, s2, s66\n"
    "  s_lshl_b32 s66, s66, 7\n"
    "  s_add_u32 s66, s67, s66\n"
    "  s_add_u32 s33, s66, 0x1200000\n"
    "  s_mov_b32 s67, 0x200\n"
    "  v_lshl_add_u32 v15, v0, 3, s67\n"
    "  s_mov_b32 s52, 0x05040100\n"
    "  s_mov_b32 s53, 0x07060302\n"
    "  ds_read_b64_tr_b16 v[88:89], v10 offset:3592\n"
    "  ds_read_b64_tr_b16 v[90:91], v10 offset:3584\n"
    "  ds_read_b64_tr_b16 v[92:93], v10 offset:3080\n"
    "  ds_read_b64_tr_b16 v[94:95], v10 offset:3072\n"
    "  ds_read_b64_tr_b16 v[96:97], v10 offset:2568\n"
    "  ds_read_b64_tr_b16 v[98:99], v10 offset:2560\n"
    "  ds_read_b64_tr_b16 v[100:101], v10 offset:2056\n"
    "  ds_read_b64_tr_b16 v[102:103], v10 offset:2048\n"
    "  s_waitcnt vmcnt(0)\n"
    "  s_mov_b32 s55, 0x00008000\n"
    "Lc_93%=:\n"
    "  v_max_u32 v16, v112, v116\n"
    "  v_cmp_eq_u32 vcc, -1, v16\n"
    "  s_cbranch_vccz Lo_94%=\n"
    "  s_sub_u32 s55, s55, 1\n"
    "  s_cbranch_scc1 Lo_94%=\n"
    "  s_sleep 2\n"
    "  s_mov_b64 exec, vcc\n"
    "  buffer_load_dwordx4 v[112:115], v120, s[16:19], s48 offen sc1\n"
    "  buffer_load_dwordx4 v[116:119], v120, s[16:19], s49 offen sc1\n"
    "  s_mov_b64 exec, -1\n"
    "  s_waitcnt vmcnt(0)\n"
    "  s_branch Lc_93%=\n"
    "Lo_94%=:\n"
    "  v_perm_b32 v52, v113, v112, s52\n"
    "  v_perm_b32 v53, v115, v114, s52\n"
    "  v_perm_b32 v54, v113, v112, s53\n"
    "  v_perm_b32 v55, v115, v114, s53\n"
    "  ds_write_b64 v15, v[52:53]\n"
    "  ds_write_b64 v15, v[54:55] offset:4608\n"
    "  v_perm_b32 v56, v117, v116, s52\n"
    "  v_perm_b32 v57, v119, v118, s52\n"
    "  v_perm_b32 v58, v117, v116, s53\n"
    "  v_perm_b32 v59, v119, v118, s53\n"
    "  ds_write_b64 v15, v[56:57] offset:2048\n"
    "  ds_write_b64 v15, v[58:59] offset:6656\n"
    "  s_waitcnt lgkmcnt(0)\n"
    "  s_barrier\n"
    "  ds_read_b128 v[60:63], v11 offset:192\n"
    "  ds_read_b128 v[64:67], v11 offset:704\n"
    "  ds_read_b128 v[68:71], v11 offset:1216\n"
    "  ds_read_b128 v[72:75], v11 offset:1728\n"
    "  s_bitcmp1_b32 s40, 0\n"
    "  s_cbranch_scc1 Lb_95%=\n"
    "  s_waitcnt lgkmcnt(3)\n"
    "  v_mfma_f32_16x16x32_bf16 v[16:19], v[88:91], v[60:63], 0\n"
    "  ds_read_b64_tr_b16 v[48:49], v10 offset:1544\n"
    "  v_mfma_f32_16x16x32_bf16 v[20:23], v[92:95], v[60:63], 0\n"
    "  buffer_load_dwordx4 v[32:35], v14, s[16:19], s34 offen sc1\n"
    "  s_waitcnt lgkmcnt(3)\n"
    "  v_mfma_f32_16x16x32_bf16 v[16:19], v[92:95], v[64:67], v[16:19]\n"
    "  ds_read_b64_tr_b16 v[50:51], v10 offset:1536\n"
    "  v_mfma_f32_16x16x32_bf16 v[104:107], v[96:99], v[60:63], 0\n"
    "  ds_read_b128 v[52:55], v11 offset:2240\n"
    "  v_mfma_f32_16x16x32_bf16 v[20:23], v[96:99], v[64:67], v[20:23]\n"
    "  buffer_load_dwordx4 v[36:39], v14, s[16:19], s35 offen sc1\n"
    "  s_waitcnt lgkmcnt(4)\n"
    "  v_mfma_f32_16x16x32_bf16 v[16:19], v[96:99], v[68:71], v[16:19]\n"
    "  ds_read_b64_tr_b16 v[56:57], v10 offset:1032\n"
    "  v_mfma_f32_16x16x32_bf16 v[108:111], v[100:103], v[60:63], 0\n"
    "  ds_read_b64_tr_b16 v[58:59], v10 offset:1024\n"
    "  v_mfma_f32_16x16x32_bf16 v[104:107], v[100:103], v[64:67], v[104:107]\n"
    "  buffer_load_dwordx4 v[40:43], v14, s[16:19], s36 offen sc1\n"
    "  v_mfma_f32_16x16x32_bf16 v[20:23], v[100:103], v[68:71], v[20:23]\n"
    "  ds_read_b128 v[76:79], v11 offset:2752\n"
    "  s_waitcnt lgkmcnt(6)\n"
    "  v_mfma_f32_16x16x32_bf16 v[16:19], v[100:103], v[72:75], v[16:19]\n"
    "  ds_read_b64_tr_b16 v[80:81], v10 offset:520\n"
    "  s_waitcnt lgkmcnt(5)\n"
    "  v_mfma_f32_16x16x32_bf16 v[112:115], v[48:51], v[60:63], 0\n"
    "  buffer_load_dwordx4 v[44:47], v14, s[16:19], s37 offen sc1\n"
    "  v_mfma_f32_16x16x32_bf16 v[108:111], v[48:51], v[64:67], v[108:111]\n"
    "  ds_read_b64_tr_b16 v[82:83], v10 offset:512\n"
    "  s_lshl_b32 s67, s40, 12\n"
    "  v_mfma_f32_16x16x32_bf16 v[104:107], v[48:51], v[68:71], v[104:107]\n"
    "  ds_read_b128 v[84:87], v11 offset:3264\n"
    "  s_add_u32 s67, s67, 0x4000\n"
    "  v_mfma_f32_16x16x32_bf16 v[20:23], v[48:51], v[72:75], v[20:23]\n"
    "  ds_read_b64_tr_b16 v[88:89], v10 offset:8\n"
    "  v_add_u32 v14, s67, v2\n"
    "  s_waitcnt lgkmcnt(7)\n"
    "  v_mfma_f32_16x16x32_bf16 v[16:19], v[48:51], v[52:55], v[16:19]\n"
    "  ds_read_b64_tr_b16 v[90:91], v10\n"
    "  s_waitcnt lgkmcnt(6)\n"
    "  v_mfma_f32_16x16x32_bf16 v[116:119], v[56:59], v[60:63], 0\n"
    "  ds_read_b128 v[92:95], v11 offset:3776\n"
    "  v_mfma_f32_16x16x32_bf16 v[112:115], v[56:59], v[64:67], v[112:115]\n"
    "  ds_read_b64_tr_b16 v[96:97], v10 offset:3656\n"
    "  v_mfma_f32_16x16x32_bf16 v[108:111], v[56:59], v[68:71], v[108:111]\n"
    "  ds_read_b64_tr_b16 v[98:99], v10 offset:3648\n"
    "  v_mfma_f32_16x16x32_bf16 v[104:107], v[56:59], v[72:75], v[104:107]\n"
    "  ds_read_b128 v[100:103], v11 offset:128\n"
    "  v_mfma_f32_16x16x32_bf16 v[20:23], v[56:59], v[52:55], v[20:23]\n"
    "  ds_read_b64_tr_b16 v[48:49], v10 offset:3144\n"
    "  s_waitcnt lgkmcnt(8)\n"
    "  v_mfma_f32_16x16x32_bf16 v[16:19], v[56:59], v[76:79], v[16:19]\n"
    "  ds_read_b64_tr_b16 v[50:51], v10 offset:3136\n"
    "  v_mfma_f32_16x16x32_bf16 v[24:27], v[80:83], v[60:63], 0\n"
    "  ds_read_b128 v[56:59], v11 offset:640\n"
    "  v_mfma_f32_16x16x32_bf16 v[116:119], v[80:83], v[64:67], v[116:119]\n"
    "  v_mfma_f32_16x16x32_bf16 v[112:115], v[80:83], v[68:71], v[112:115]\n"
    "  v_mfma_f32_16x16x32_bf16 v[108:111], v[80:83], v[72:75], v[108:111]\n"
    "  v_mfma_f32_16x16x32_bf16 v[104:107], v[80:83], v[52:55], v[104:107]\n"
    "  v_mfma_f32_16x16x32_bf16 v[20:23], v[80:83], v[76:79], v[20:23]\n"
    "  s_waitcnt lgkmcnt(7)\n"
    "  v_mfma_f32_16x16x32_bf16 v[16:19], v[80:83], v[84:87], v[16:19]\n"
    "  ds_read_b64_tr_b16 v[80:81], v10 offset:2632\n"
    "  v_mfma_f32_16x16x32_bf16 v[28:31], v[88:91], v[60:63], 0\n"
    "  ds_read_b64_tr_b16 v[82:83], v10 offset:2624\n"
    "  v_mfma_f32_16x16x32_bf16 v[24:27], v[88:91], v[64:67], v[24:27]\n"
    "  ds_read_b128 v[60:63], v11 offset:1152\n"
    "  v_mfma_f32_16x16x32_bf16 v[116:119], v[88:91], v[68:71], v[116:119]\n"
    "  ds_read_b64_tr_b16 v[64:65], v10 offset:2120\n"
    "  v_mfma_f32_16x16x32_bf16 v[112:115], v[88:91], v[72:75], v[112:115]\n"
    "  ds_read_b64_tr_b16 v[66:67], v10 offset:2112\n"
    "  v_mfma_f32_16x16x32_bf16 v[108:111], v[88:91], v[52:55], v[108:111]\n"
    "  ds_read_b128 v[68:71], v11 offset:1664\n"
    "  v_mfma_f32_16x16x32_bf16 v[104:107], v[88:91], v[76:79], v[104:107]\n"
    "  ds_read_b64_tr_b16 v[72:73], v10 offset:1608\n"
    "  v_mfma_f32_16x16x32_bf16 v[20:23], v[88:91], v[84:87], v[20:23]\n"
    "  ds_read_b64_tr_b16 v[74:75], v10 offset:1600\n"
    "  s_waitcnt lgkmcnt(8)\n"
    "  v_mfma_f32_16x16x32_bf16 v[16:19], v[88:91], v[92:95], v[16:19]\n"
    "  ds_read_b128 v[52:55], v11 offset:2176\n"
    "  v_mfma_f32_16x16x32_bf16 v[16:19], v[96:99], v[100:103], v[16:19]\n"
    "  ds_read_b64_tr_b16 v[76:77], v10 offset:1096\n"
    "  v_mfma_f32_16x16x32_bf16 v[20:23], v[48:51], v[100:103], v[20:23]\n"
    "  ds_read_b64_tr_b16 v[78:79], v10 offset:1088\n"
    "  v_mfma_f32_16x16x32_bf16 v[16:19], v[48:51], v[56:59], v[16:19]\n"
    "  ds_read_b128 v[84:87], v11 offset:2688\n"
    "  s_waitcnt lgkmcnt(7)\n"
    "  v_mfma_f32_16x16x32_bf16 v[104:107], v[80:83], v[100:103], v[104:107]\n"
    "  ds_read_b64_tr_b16 v[88:89], v10 offset:584\n"
    "  v_mfma_f32_16x16x32_bf16 v[20:23], v[80:83], v[56:59], v[20:23]\n"
    "  ds_read_b64_tr_b16 v[90:91], v10 offset:576\n"
    "  v_mfma_f32_16x16x32_bf16 v[16:19], v[80:83], v[60:63], v[16:19]\n"
    "  v_mfma_f32_16x16x32_bf16 v[108:111], v[64:67], v[100:103], v[108:111]\n"
    "  v_mfma_f32_16x16x32_bf16 v[104:107], v[64:67], v[56:59], v[104:107]\n"
    "  ds_read_b128 v[92:95], v11 offset:3200\n"
    "  v_mfma_f32_16x16x32_bf16 v[20:23], v[64:67], v[60:63], v[20:23]\n"
    "  ds_read_b64_tr_b16 v[96:97], v10 offset:72\n"
    "  s_waitcnt lgkmcnt(7)\n"
    "  v_mfma_f32_16x16x32_bf16 v[16:19], v[64:67], v[68:71], v[16:19]\n"
    "  ds_read_b64_tr_b16 v[98:99], v10 offset:64\n"
    "  v_mfma_f32_16x16x32_bf16 v[112:115], v[72:75], v[100:103], v[112:115]\n"
    "  v_mfma_f32_16x16x32_bf16 v[108:111], v[72:75], v[56:59], v[108:111]\n"
    "  v_mfma_f32_16x16x32_bf16 v[104:107], v[72:75], v[60:63], v[104:107]\n"
    "  v_mfma_f32_16x16x32_bf16 v[20:23], v[72:75], v[68:71], v[20:23]\n"
    "  v_mfma_f32_16x16x32_bf16 v[16:19], v[72:75], v[52:55], v[16:19]\n"
    "  s_waitcnt lgkmcnt(6)\n"
    "  v_mfma_f32_16x16x32_bf16 v[116:119], v[76:79], v[100:103], v[116:119]\n"
    "  ds_read_b128 v[48:51], v11 offset:3712\n"
    "  v_mfma_f32_16x16x32_bf16 v[112:115], v[76:79], v[56:59], v[112:115]\n"
    "  ds_read_b64_tr_b16 v[80:81], v10 offset:3720\n"
    "  v_mfma_f32_16x16x32_bf16 v[108:111], v[76:79], v[60:63], v[108:111]\n"
    "  ds_read_b64_tr_b16 v[82:83], v10 offset:3712\n"
    "  v_mfma_f32_16x16x32_bf16 v[104:107], v[76:79], v[68:71], v[104:107]\n"
    "  ds_read_b128 v[64:67], v11 offset:64\n"
    "  v_mfma_f32_16x16x32_bf16 v[20:23], v[76:79], v[52:55], v[20:23]\n"
    "  ds_read_b64_tr_b16 v[72:73], v10 offset:3208\n"
    "  s_waitcnt lgkmcnt(8)\n"
    "  v_mfma_f32_16x16x32_bf16 v[16:19], v[76:79], v[84:87], v[16:19]\n"
    "  ds_read_b64_tr_b16 v[74:75], v10 offset:3200\n"
    "  v_mfma_f32_16x16x32_bf16 v[24:27], v[88:91], v[100:103], v[24:27]\n"
    "  ds_read_b128 v[76:79], v11 offset:576\n"
    "  v_mfma_f32_16x16x32_bf16 v[116:119], v[88:91], v[56:59], v[116:119]\n"
    "  v_mfma_f32_16x16x32_bf16 v[112:115], v[88:91], v[60:63], v[112:115]\n"
    "  v_mfma_f32_16x16x32_bf16 v[108:111], v[88:91], v[68:71], v[108:111]\n"
    "  v_mfma_f32_16x16x32_bf16 v[104:107], v[88:91], v[52:55], v[104:107]\n"
    "  v_mfma_f32_16x16x32_bf16 v[20:23], v[88:91], v[84:87], v[20:23]\n"
    "  s_waitcnt lgkmcnt(7)\n"
    "  v_mfma_f32_16x16x32_bf16 v[16:19], v[88:91], v[92:95], v[16:19]\n"
    "  ds_read_b64_tr_b16 v[88:89], v10 offset:2696\n"
    "  v_mfma_f32_16x16x32_bf16 v[28:31], v[96:99], v[100:103], v[28:31]\n"
    "  ds_read_b64_tr_b16 v[90:91], v10 offset:2688\n"
    "  v_mfma_f32_16x16x32_bf16 v[24:27], v[96:99], v[56:59], v[24:27]\n"
    "  ds_read_b128 v[100:103], v11 offset:1088\n"
    "  v_mfma_f32_16x16x32_bf16 v[116:119], v[96:99], v[60:63], v[116:119]\n"
    "  ds_read_b64_tr_b16 v[56:57], v10 offset:2184\n"
    "  v_mfma_f32_16x16x32_bf16 v[112:115], v[96:99], v[68:71], v[112:115]\n"
    "  ds_read_b64_tr_b16 v[58:59], v10 offset:2176\n"
    "  v_mfma_f32_16x16x32_bf16 v[108:111], v[96:99], v[52:55], v[108:111]\n"
    "  ds_read_b128 v[60:63], v11 offset:1600\n"
    "  v_mfma_f32_16x16x32_bf16 v[104:107], v[96:99], v[84:87], v[104:107]\n"
    "  ds_read_b64_tr_b16 v[68:69], v10 offset:1672\n"
    "  v_mfma_f32_16x16x32_bf16 v[20:23], v[96:99], v[92:95], v[20:23]\n"
    "  ds_read_b64_tr_b16 v[70:71], v10 offset:1664\n"
    "  s_waitcnt lgkmcnt(8)\n"
    "  v_mfma_f32_16x16x32_bf16 v[16:19], v[96:99], v[48:51], v[16:19]\n"
    "  ds_read_b128 v[52:55], v11 offset:2112\n"
    "  v_mfma_f32_16x16x32_bf16 v[16:19], v[80:83], v[64:67], v[16:19]\n"
    "  ds_read_b64_tr_b16 v[84:85], v10 offset:1160\n"
    "  v_mfma_f32_16x16x32_bf16 v[20:23], v[72:75], v[64:67], v[20:23]\n"
    "  ds_read_b64_tr_b16 v[86:87], v10 offset:1152\n"
    "  v_mfma_f32_16x16x32_bf16 v[16:19], v[72:75], v[76:79], v[16:19]\n"
    "  ds_read_b128 v[92:95], v11 offset:2624\n"
    "  s_waitcnt lgkmcnt(7)\n"
    "  v_mfma_f32_16x16x32_bf16 v[104:107], v[88:91], v[64:67], v[104:107]\n"
    "  ds_read_b64_tr_b16 v[96:97], v10 offset:648\n"
    "  v_mfma_f32_16x16x32_bf16 v[20:23], v[88:91], v[76:79], v[20:23]\n"
    "  ds_read_b64_tr_b16 v[98:99], v10 offset:640\n"
    "  v_mfma_f32_16x16x32_bf16 v[16:19], v[88:91], v[100:103], v[16:19]\n"
    "  ds_read_b64 v[124:125], v12 offset:512\n"
    "  v_mfma_f32_16x16x32_bf16 v[108:111], v[56:59], v[64:67], v[108:111]\n"
    "  ds_read_b64 v[126:127], v12\n"
    "  v_mfma_f32_16x16x32_bf16 v[104:107], v[56:59], v[76:79], v[104:107]\n"
    "  ds_read_b128 v[48:51], v11 offset:3136\n"
    "  v_mfma_f32_16x16x32_bf16 v[20:23], v[56:59], v[100:103], v[20:23]\n"
    "  ds_read_b64_tr_b16 v[80:81], v10 offset:136\n"
    "  s_waitcnt lgkmcnt(10)\n"
    "  v_mfma_f32_16x16x32_bf16 v[16:19], v[56:59], v[60:63], v[16:19]\n"
    "  ds_read_b64_tr_b16 v[82:83], v10 offset:128\n"
    "  v_mfma_f32_16x16x32_bf16 v[112:115], v[68:71], v[64:67], v[112:115]\n"
    "  ds_read_b64 v[120:121], v12 offset:3584\n"
    "  v_mfma_f32_16x16x32_bf16 v[108:111], v[68:71], v[76:79], v[108:111]\n"
    "  ds_read_b64 v[122:123], v12 offset:3072\n"
    "  s_waitcnt vmcnt(1)\n"
    "  v_perm_b32 v40, v40, v40, s54\n"
    "  v_mfma_f32_16x16x32_bf16 v[104:107], v[68:71], v[100:103], v[104:107]\n"
    "  v_perm_b32 v41, v41, v41, s54\n"
    "  v_perm_b32 v42, v42, v42, s54\n"
    "  v_mfma_f32_16x16x32_bf16 v[20:23], v[68:71], v[60:63], v[20:23]\n"
    "  v_perm_b32 v43, v43, v43, s54\n"
    "  s_waitcnt lgkmcnt(6)\n"
    "  v_lshlrev_b32 v15, 16, v124\n"
    "  v_mfma_f32_16x16x32_bf16 v[16:19], v[68:71], v[52:55], v[16:19]\n"
    "  v_fmac_f32 v40, v15, v3\n"
    "  v_and_b32 v15, 0xffff0000, v124\n"
    "  v_mfma_f32_16x16x32_bf16 v[116:119], v[84:87], v[64:67], v[116:119]\n"
    "  ds_read_b128 v[72:75], v11 offset:3648\n"
    "  v_fmac_f32 v41, v15, v3\n"
    "  v_mfma_f32_16x16x32_bf16 v[112:115], v[84:87], v[76:79], v[112:115]\n"
    "  ds_read_b64_tr_b16 v[88:89], v10 offset:2760\n"
    "  v_lshlrev_b32 v15, 16, v125\n"
    "  v_mfma_f32_16x16x32_bf16 v[108:111], v[84:87], v[100:103], v[108:111]\n"
    "  ds_read_b64_tr_b16 v[90:91], v10 offset:2752\n"
    "  v_fmac_f32 v42, v15, v3\n"
    "  v_mfma_f32_16x16x32_bf16 v[104:107], v[84:87], v[60:63], v[104:107]\n"
    "  ds_read_b128 v[56:59], v11\n"
    "  v_and_b32 v15, 0xffff0000, v125\n"
    "  v_mfma_f32_16x16x32_bf16 v[20:23], v[84:87], v[52:55], v[20:23]\n"
    "  ds_read_b64_tr_b16 v[68:69], v10 offset:2248\n"
    "  v_fmac_f32 v43, v15, v3\n"
    "  v_mfma_f32_16x16x32_bf16 v[16:19], v[84:87], v[92:95], v[16:19]\n"
    "  ds_read_b64_tr_b16 v[70:71], v10 offset:2240\n"
    "  s_waitcnt vmcnt(0)\n"
    "  v_perm_b32 v44, v44, v44, s54\n"
    "  v_mfma_f32_16x16x32_bf16 v[24:27], v[96:99], v[64:67], v[24:27]\n"
    "  ds_read_b128 v[84:87], v11 offset:512\n"
    "  v_perm_b32 v45, v45, v45, s54\n"
    "  v_mfma_f32_16x16x32_bf16 v[116:119], v[96:99], v[76:79], v[116:119]\n"
    "  v_perm_b32 v46, v46, v46, s54\n"
    "  v_perm_b32 v47, v47, v47, s54\n"
    "  v_mfma_f32_16x16x32_bf16 v[112:115], v[96:99], v[100:103], v[112:115]\n"
    "  s_waitcnt lgkmcnt(12)\n"
    "  v_lshlrev_b32 v15, 16, v126\n"
    "  v_fmac_f32 v44, v15, v3\n"
    "  v_mfma_f32_16x16x32_bf16 v[108:111], v[96:99], v[60:63], v[108:111]\n"
    "  v_and_b32 v15, 0xffff0000, v126\n"
    "  v_fmac_f32 v45, v15, v3\n"
    "  v_mfma_f32_16x16x32_bf16 v[104:107], v[96:99], v[52:55], v[104:107]\n"
    "  v_lshlrev_b32 v15, 16, v127\n"
    "  v_fmac_f32 v46, v15, v3\n"
    "  v_mfma_f32_16x16x32_bf16 v[20:23], v[96:99], v[92:95], v[20:23]\n"
    "  v_and_b32 v15, 0xffff0000, v127\n"
    "  v_fmac_f32 v47, v15, v3\n"
    "  s_waitcnt lgkmcnt(9)\n"
    "  v_mfma_f32_16x16x32_bf16 v[16:19], v[96:99], v[48:51], v[16:19]\n"
    "  ds_read_b64_tr_b16 v[96:97], v10 offset:1736\n"
    "  v_perm_b32 v32, v32, v32, s54\n"
    "  v_mfma_f32_16x16x32_bf16 v[28:31], v[80:83], v[64:67], v[28:31]\n"
    "  ds_read_b64_tr_b16 v[98:99], v10 offset:1728\n"
    "  v_perm_b32 v33, v33, v33, s54\n"
    "  v_mfma_f32_16x16x32_bf16 v[24:27], v[80:83], v[76:79], v[24:27]\n"
    "  ds_read_b128 v[64:67], v11 offset:1024\n"
    "  v_perm_b32 v34, v34, v34, s54\n"
    "  v_mfma_f32_16x16x32_bf16 v[116:119], v[80:83], v[100:103], v[116:119]\n"
    "  ds_read_b64_tr_b16 v[76:77], v10 offset:1224\n"
    "  v_perm_b32 v35, v35, v35, s54\n"
    "  v_mfma_f32_16x16x32_bf16 v[112:115], v[80:83], v[60:63], v[112:115]\n"
    "  ds_read_b64_tr_b16 v[78:79], v10 offset:1216\n"
    "  s_waitcnt lgkmcnt(13)\n"
    "  v_lshlrev_b32 v15, 16, v120\n"
    "  v_mfma_f32_16x16x32_bf16 v[108:111], v[80:83], v[52:55], v[108:111]\n"
    "  ds_read_b64_tr_b16 v[100:101], v10 offset:712\n"
    "  v_fmac_f32 v32, v15, v3\n"
    "  v_mfma_f32_16x16x32_bf16 v[104:107], v[80:83], v[92:95], v[104:107]\n"
    "  ds_read_b64_tr_b16 v[102:103], v10 offset:704\n"
    "  v_and_b32 v15, 0xffff0000, v120\n"
    "  v_mfma_f32_16x16x32_bf16 v[20:23], v[80:83], v[48:51], v[20:23]\n"
    "  ds_read_b64_tr_b16 v[60:61], v10 offset:200\n"
    "  v_fmac_f32 v33, v15, v3\n"
    "  s_waitcnt lgkmcnt(9)\n"
    "  v_mfma_f32_16x16x32_bf16 v[16:19], v[80:83], v[72:75], v[16:19]\n"
    "  ds_read_b64_tr_b16 v[62:63], v10 offset:192\n"
    "  v_lshlrev_b32 v15, 16, v121\n"
    "  v_mfma_f32_16x16x32_bf16 v[104:107], v[88:91], v[56:59], v[104:107]\n"
    "  ds_read_b128 v[52:55], v11 offset:1536\n"
    "  v_fmac_f32 v34, v15, v3\n"
    "  v_mfma_f32_16x16x32_bf16 v[108:111], v[68:71], v[56:59], v[108:111]\n"
    "  ds_read_b128 v[92:95], v11 offset:2048\n"
    "  v_and_b32 v15, 0xffff0000, v121\n"
    "  s_waitcnt lgkmcnt(9)\n"
    "  v_mfma_f32_16x16x32_bf16 v[104:107], v[68:71], v[84:87], v[104:107]\n"
    "  ds_read_b128 v[48:51], v11 offset:2560\n"
    "  v_fmac_f32 v35, v15, v3\n"
    "  v_mfma_f32_16x16x32_bf16 v[112:115], v[96:99], v[56:59], v[112:115]\n"
    "  ds_read_b64_tr_b16 v[80:81], v10 offset:3784\n"
    "  v_perm_b32 v36, v36, v36, s54\n"
    "  v_mfma_f32_16x16x32_bf16 v[108:111], v[96:99], v[84:87], v[108:111]\n"
    "  ds_read_b64_tr_b16 v[82:83], v10 offset:3776\n"
    "  v_perm_b32 v37, v37, v37, s54\n"
    "  s_waitcnt lgkmcnt(7)\n"
    "  v_mfma_f32_16x16x32_bf16 v[104:107], v[96:99], v[64:67], v[104:107]\n"
    "  ds_read_b64_tr_b16 v[72:73], v10 offset:3272\n"
    "  v_perm_b32 v38, v38, v38, s54\n"
    "  v_mfma_f32_16x16x32_bf16 v[116:119], v[76:79], v[56:59], v[116:119]\n"
    "  ds_read_b64_tr_b16 v[74:75], v10 offset:3264\n"
    "  v_perm_b32 v39, v39, v39, s54\n"
    "  v_mfma_f32_16x16x32_bf16 v[116:119], v[100:103], v[84:87], v[116:119]\n"
    "  v_lshlrev_b32 v15, 16, v122\n"
    "  v_fmac_f32 v36, v15, v3\n"
    "  s_waitcnt lgkmcnt(7)\n"
    "  v_mfma_f32_16x16x32_bf16 v[116:119], v[60:63], v[64:67], v[116:119]\n"
    "  v_and_b32 v15, 0xffff0000, v122\n"
    "  v_fmac_f32 v37, v15, v3\n"
    "  v_mfma_f32_16x16x32_bf16 v[112:115], v[76:79], v[84:87], v[112:115]\n"
    "  v_lshlrev_b32 v15, 16, v123\n"
    "  v_fmac_f32 v38, v15, v3\n"
    "  v_mfma_f32_16x16x32_bf16 v[112:115], v[100:103], v[64:67], v[112:115]\n"
    "  v_and_b32 v15, 0xffff0000, v123\n"
    "  v_fmac_f32 v39, v15, v3\n"
    "  s_waitcnt lgkmcnt(6)\n"
    "  v_mfma_f32_16x16x32_bf16 v[112:115], v[60:63], v[52:55], v[112:115]\n"
    "  v_mfma_f32_16x16x32_bf16 v[108:111], v[76:79], v[64:67], v[108:111]\n"
    "  ds_write_b128 v14, v[116:119] offset:3072\n"
    "  v_mfma_f32_16x16x32_bf16 v[108:111], v[100:103], v[52:55], v[108:111]\n"
    "  s_waitcnt lgkmcnt(6)\n"
    "  v_mfma_f32_16x16x32_bf16 v[108:111], v[60:63], v[92:95], v[108:111]\n"
    "  v_mfma_f32_16x16x32_bf16 v[104:107], v[76:79], v[52:55], v[104:107]\n"
    "  s_nop 1\n"
    "  ds_write_b128 v14, v[112:115] offset:2048\n"
    "  v_mfma_f32_16x16x32_bf16 v[104:107], v[100:103], v[92:95], v[104:107]\n"
    "  s_waitcnt lgkmcnt(6)\n"
    "  v_mfma_f32_16x16x32_bf16 v[104:107], v[60:63], v[48:51], v[104:107]\n"
    "  s_waitcnt lgkmcnt(4)\n"
    "  v_mfma_f32_16x16x32_bf16 v[16:19], v[80:83], v[56:59], v[16:19]\n"
    "  ds_write_b128 v14, v[108:111] offset:1024\n"
    "  s_waitcnt lgkmcnt(3)\n"
    "  v_mfma_f32_16x16x32_bf16 v[20:23], v[72:75], v[56:59], v[20:23]\n"
    "  ds_read_b128 v[80:83], v11 offset:3072\n"
    "  v_mfma_f32_16x16x32_bf16 v[16:19], v[72:75], v[84:87], v[16:19]\n"
    "  ds_read_b128 v[72:75], v11 offset:3584\n"
    "  v_mfma_f32_16x16x32_bf16 v[20:23], v[88:91], v[84:87], v[20:23]\n"
    "  ds_write_b128 v14, v[104:107]\n"
    "  v_mfma_f32_16x16x32_bf16 v[16:19], v[88:91], v[64:67], v[16:19]\n"
    "  v_mfma_f32_16x16x32_bf16 v[20:23], v[68:71], v[64:67], v[20:23]\n"
    "  v_mfma_f32_16x16x32_bf16 v[16:19], v[68:71], v[52:55], v[16:19]\n"
    "  v_xor_b32 v14, 4096, v14\n"
    "  s_waitcnt lgkmcnt(0)\n"
    "  s_barrier\n"
    "  ds_read_b128 v[104:107], v14\n"
    "  v_mfma_f32_16x16x32_bf16 v[20:23], v[96:99], v[52:55], v[20:23]\n"
    "  ds_read_b128 v[108:111], v14 offset:1024\n"
    "  v_mfma_f32_16x16x32_bf16 v[16:19], v[96:99], v[92:95], v[16:19]\n"
    "  ds_read_b128 v[112:115], v14 offset:2048\n"
    "  v_mfma_f32_16x16x32_bf16 v[20:23], v[76:79], v[92:95], v[20:23]\n"
    "  ds_read_b128 v[116:119], v14 offset:3072\n"
    "  v_mfma_f32_16x16x32_bf16 v[16:19], v[76:79], v[48:51], v[16:19]\n"
    "  v_mfma_f32_16x16x32_bf16 v[24:27], v[100:103], v[56:59], v[24:27]\n"
    "  s_waitcnt lgkmcnt(3)\n"
    "  v_add_f32 v32, v32, v104\n"
    "  v_add_f32 v33, v33, v105\n"
    "  v_mfma_f32_16x16x32_bf16 v[20:23], v[100:103], v[48:51], v[20:23]\n"
    "  v_add_f32 v34, v34, v106\n"
    "  v_add_f32 v35, v35, v107\n"
    "  v_mfma_f32_16x16x32_bf16 v[16:19], v[100:103], v[80:83], v[16:19]\n"
    "  s_waitcnt lgkmcnt(2)\n"
    "  v_add_f32 v36, v36, v108\n"
    "  v_add_f32 v37, v37, v109\n"
    "  v_mfma_f32_16x16x32_bf16 v[28:31], v[60:63], v[56:59], v[28:31]\n"
    "  v_add_f32 v38, v38, v110\n"
    "  v_add_f32 v39, v39, v111\n"
    "  v_mfma_f32_16x16x32_bf16 v[24:27], v[60:63], v[84:87], v[24:27]\n"
    "  s_waitcnt lgkmcnt(1)\n"
    "  v_add_f32 v40, v40, v112\n"
    "  v_add_f32 v41, v41, v113\n"
    "  v_mfma_f32_16x16x32_bf16 v[20:23], v[60:63], v[80:83], v[20:23]\n"
    "  v_add_f32 v42, v42, v114\n"
    "  v_add_f32 v43, v43, v115\n"
    "  v_mfma_f32_16x16x32_bf16 v[16:19], v[60:63], v[72:75], v[16:19]\n"
    "  s_waitcnt lgkmcnt(0)\n"
    "  v_add_f32 v44, v44, v116\n"
    "  v_add_f32 v45, v45, v117\n"
    "  v_add_f32 v46, v46, v118\n"
    "  v_add_f32 v47, v47, v119\n"
    "  s_branch Lbj_96%=\n"
    "Lb_95%=:\n"
    "  s_waitcnt lgkmcnt(3)\n"
    "  v_mfma_f32_16x16x32_bf16 v[104:107], v[88:91], v[60:63], 0\n"
    "  ds_read_b64_tr_b16 v[48:49], v10 offset:1544\n"
    "  v_mfma_f32_16x16x32_bf16 v[108:111], v[92:95], v[60:63], 0\n"
    "  buffer_load_dwordx4 v[32:35], v14, s[16:19], s34 offen sc1\n"
    "  s_waitcnt lgkmcnt(3)\n"
    "  v_mfma_f32_16x16x32_bf16 v[104:107], v[92:95], v[64:67], v[104:107]\n"
    "  ds_read_b64_tr_b16 v[50:51], v10 offset:1536\n"
    "  v_mfma_f32_16x16x32_bf16 v[16:19], v[96:99], v[60:63], 0\n"
    "  ds_read_b128 v[52:55], v11 offset:2240\n"
    "  v_mfma_f32_16x16x32_bf16 v[108:111], v[96:99], v[64:67], v[108:111]\n"
    "  buffer_load_dwordx4 v[36:39], v14, s[16:19], s35 offen sc1\n"
    "  s_waitcnt lgkmcnt(4)\n"
    "  v_mfma_f32_16x16x32_bf16 v[104:107], v[96:99], v[68:71], v[104:107]\n"
    "  ds_read_b64_tr_b16 v[56:57], v10 offset:1032\n"
    "  v_mfma_f32_16x16x32_bf16 v[20:23], v[100:103], v[60:63], 0\n"
    "  ds_read_b64_tr_b16 v[58:59], v10 offset:1024\n"
    "  v_mfma_f32_16x16x32_bf16 v[16:19], v[100:103], v[64:67], v[16:19]\n"
    "  buffer_load_dwordx4 v[40:43], v14, s[16:19], s36 offen sc1\n"
    "  v_mfma_f32_16x16x32_bf16 v[108:111], v[100:103], v[68:71], v[108:111]\n"
    "  ds_read_b128 v[76:79], v11 offset:2752\n"
    "  s_waitcnt lgkmcnt(6)\n"
    "  v_mfma_f32_16x16x32_bf16 v[104:107], v[100:103], v[72:75], v[104:107]\n"
    "  ds_read_b64_tr_b16 v[80:81], v10 offset:520\n"
    "  s_waitcnt lgkmcnt(5)\n"
    "  v_mfma_f32_16x16x32_bf16 v[24:27], v[48:51], v[60:63], 0\n"
    "  buffer_load_dwordx4 v[44:47], v14, s[16:19], s37 offen sc1\n"
    "  v_mfma_f32_16x16x32_bf16 v[20:23], v[48:51], v[64:67], v[20:23]\n"
    "  ds_read_b64_tr_b16 v[82:83], v10 offset:512\n"
    "  s_lshl_b32 s67, s40, 12\n"
    "  v_mfma_f32_16x16x32_bf16 v[16:19], v[48:51], v[68:71], v[16:19]\n"
    "  ds_read_b128 v[84:87], v11 offset:3264\n"
    "  s_add_u32 s67, s67, 0x4000\n"
    "  v_mfma_f32_16x16x32_bf16 v[108:111], v[48:51], v[72:75], v[108:111]\n"
    "  ds_read_b64_tr_b16 v[88:89], v10 offset:8\n"
    "  v_add_u32 v14, s67, v2\n"
    "  s_waitcnt lgkmcnt(7)\n"
    "  v_mfma_f32_16x16x32_bf16 v[104:107], v[48:51], v[52:55], v[104:107]\n"
    "  ds_read_b64_tr_b16 v[90:91], v10\n"
    "  s_waitcnt lgkmcnt(6)\n"
    "  v_mfma_f32_16x16x32_bf16 v[28:31], v[56:59], v[60:63], 0\n"
    "  ds_read_b128 v[92:95], v11 offset:3776\n"
    "  v_mfma_f32_16x16x32_bf16 v[24:27], v[56:59], v[64:67], v[24:27]\n"
    "  ds_read_b64_tr_b16 v[96:97], v10 offset:3656\n"
    "  v_mfma_f32_16x16x32_bf16 v[20:23], v[56:59], v[68:71], v[20:23]\n"
    "  ds_read_b64_tr_b16 v[98:99], v10 offset:3648\n"
    "  v_mfma_f32_16x16x32_bf16 v[16:19], v[56:59], v[72:75], v[16:19]\n"
    "  ds_read_b128 v[100:103], v11 offset:128\n"
    "  v_mfma_f32_16x16x32_bf16 v[108:111], v[56:59], v[52:55], v[108:111]\n"
    "  ds_read_b64_tr_b16 v[48:49], v10 offset:3144\n"
    "  s_waitcnt lgkmcnt(8)\n"
    "  v_mfma_f32_16x16x32_bf16 v[104:107], v[56:59], v[76:79], v[104:107]\n"
    "  ds_read_b64_tr_b16 v[50:51], v10 offset:3136\n"
    "  v_mfma_f32_16x16x32_bf16 v[112:115], v[80:83], v[60:63], 0\n"
    "  ds_read_b128 v[56:59], v11 offset:640\n"
    "  v_mfma_f32_16x16x32_bf16 v[28:31], v[80:83], v[64:67], v[28:31]\n"
    "  v_mfma_f32_16x16x32_bf16 v[24:27], v[80:83], v[68:71], v[24:27]\n"
    "  v_mfma_f32_16x16x32_bf16 v[20:23], v[80:83], v[72:75], v[20:23]\n"
    "  v_mfma_f32_16x16x32_bf16 v[16:19], v[80:83], v[52:55], v[16:19]\n"
    "  v_mfma_f32_16x16x32_bf16 v[108:111], v[80:83], v[76:79], v[108:111]\n"
    "  s_waitcnt lgkmcnt(7)\n"
    "  v_mfma_f32_16x16x32_bf16 v[104:107], v[80:83], v[84:87], v[104:107]\n"
    "  ds_read_b64_tr_b16 v[80:81], v10 offset:2632\n"
    "  v_mfma_f32_16x16x32_bf16 v[116:119], v[88:91], v[60:63], 0\n"
    "  ds_read_b64_tr_b16 v[82:83], v10 offset:2624\n"
    "  v_mfma_f32_16x16x32_bf16 v[112:115], v[88:91], v[64:67], v[112:115]\n"
    "  ds_read_b128 v[60:63], v11 offset:1152\n"
    "  v_mfma_f32_16x16x32_bf16 v[28:31], v[88:91], v[68:71], v[28:31]\n"
    "  ds_read_b64_tr_b16 v[64:65], v10 offset:2120\n"
    "  v_mfma_f32_16x16x32_bf16 v[24:27], v[88:91], v[72:75], v[24:27]\n"
    "  ds_read_b64_tr_b16 v[66:67], v10 offset:2112\n"
    "  v_mfma_f32_16x16x32_bf16 v[20:23], v[88:91], v[52:55], v[20:23]\n"
    "  ds_read_b128 v[68:71], v11 offset:1664\n"
    "  v_mfma_f32_16x16x32_bf16 v[16:19], v[88:91], v[76:79], v[16:19]\n"
    "  ds_read_b64_tr_b16 v[72:73], v10 offset:1608\n"
    "  v_mfma_f32_16x16x32_bf16 v[108:111], v[88:91], v[84:87], v[108:111]\n"
    "  ds_read_b64_tr_b16 v[74:75], v10 offset:1600\n"
    "  s_waitcnt lgkmcnt(8)\n"
    "  v_mfma_f32_16x16x32_bf16 v[104:107], v[88:91], v[92:95], v[104:107]\n"
    "  ds_read_b128 v[52:55], v11 offset:2176\n"
    "  v_mfma_f32_16x16x32_bf16 v[104:107], v[96:99], v[100:103], v[104:107]\n"
    "  ds_read_b64_tr_b16 v[76:77], v10 offset:1096\n"
    "  v_mfma_f32_16x16x32_bf16 v[108:111], v[48:51], v[100:103], v[108:111]\n"
    "  ds_read_b64_tr_b16 v[78:79], v10 offset:1088\n"
    "  v_mfma_f32_16x16x32_bf16 v[104:107], v[48:51], v[56:59], v[104:107]\n"
    "  ds_read_b128 v[84:87], v11 offset:2688\n"
    "  s_waitcnt lgkmcnt(7)\n"
    "  v_mfma_f32_16x16x32_bf16 v[16:19], v[80:83], v[100:103], v[16:19]\n"
    "  ds_read_b64_tr_b16 v[88:89], v10 offset:584\n"
    "  v_mfma_f32_16x16x32_bf16 v[108:111], v[80:83], v[56:59], v[108:111]\n"
    "  ds_read_b64_tr_b16 v[90:91], v10 offset:576\n"
    "  v_mfma_f32_16x16x32_bf16 v[104:107], v[80:83], v[60:63], v[104:107]\n"
    "  v_mfma_f32_16x16x32_bf16 v[20:23], v[64:67], v[100:103], v[20:23]\n"
    "  v_mfma_f32_16x16x32_bf16 v[16:19], v[64:67], v[56:59], v[16:19]\n"
    "  ds_read_b128 v[92:95], v11 offset:3200\n"
    "  v_mfma_f32_16x16x32_bf16 v[108:111], v[64:67], v[60:63], v[108:111]\n"
    "  ds_read_b64_tr_b16 v[96:97], v10 offset:72\n"
    "  s_waitcnt lgkmcnt(7)\n"
    "  v_mfma_f32_16x16x32_bf16 v[104:107], v[64:67], v[68:71], v[104:107]\n"
    "  ds_read_b64_tr_b16 v[98:99], v10 offset:64\n"
    "  v_mfma_f32_16x16x32_bf16 v[24:27], v[72:75], v[100:103], v[24:27]\n"
    "  v_mfma_f32_16x16x32_bf16 v[20:23], v[72:75], v[56:59], v[20:23]\n"
    "  v_mfma_f32_16x16x32_bf16 v[16:19], v[72:75], v[60:63], v[16:19]\n"
    "  v_mfma_f32_16x16x32_bf16 v[108:111], v[72:75], v[68:71], v[108:111]\n"
    "  v_mfma_f32_16x16x32_bf16 v[104:107], v[72:75], v[52:55], v[104:107]\n"
    "  s_waitcnt lgkmcnt(6)\n"
    "  v_mfma_f32_16x16x32_bf16 v[28:31], v[76:79], v[100:103], v[28:31]\n"
    "  ds_read_b128 v[48:51], v11 offset:3712\n"
    "  v_mfma_f32_16x16x32_bf16 v[24:27], v[76:79], v[56:59], v[24:27]\n"
    "  ds_read_b64_tr_b16 v[80:81], v10 offset:3720\n"
    "  v_mfma_f32_16x16x32_bf16 v[20:23], v[76:79], v[60:63], v[20:23]\n"
    "  ds_read_b64_tr_b16 v[82:83], v10 offset:3712\n"
    "  v_mfma_f32_16x16x32_bf16 v[16:19], v[76:79], v[68:71], v[16:19]\n"
    "  ds_read_b128 v[64:67], v11 offset:64\n"
    "  v_mfma_f32_16x16x32_bf16 v[108:111], v[76:79], v[52:55], v[108:111]\n"
    "  ds_read_b64_tr_b16 v[72:73], v10 offset:3208\n"
    "  s_waitcnt lgkmcnt(8)\n"
    "  v_mfma_f32_16x16x32_bf16 v[104:107], v[76:79], v[84:87], v[104:107]\n"
    "  ds_read_b64_tr_b16 v[74:75], v10 offset:3200\n"
    "  v_mfma_f32_16x16x32_bf16 v[112:115], v[88:91], v[100:103], v[112:115]\n"
    "  ds_read_b128 v[76:79], v11 offset:576\n"
    "  v_mfma_f32_16x16x32_bf16 v[28:31], v[88:91], v[56:59], v[28:31]\n"
    "  v_mfma_f32_16x16x32_bf16 v[24:27], v[88:91], v[60:63], v[24:27]\n"
    "  v_mfma_f32_16x16x32_bf16 v[20:23], v[88:91], v[68:71], v[20:23]\n"
    "  v_mfma_f32_16x16x32_bf16 v[16:19], v[88:91], v[52:55], v[16:19]\n"
    "  v_mfma_f32_16x16x32_bf16 v[108:111], v[88:91], v[84:87], v[108:111]\n"
    "  s_waitcnt lgkmcnt(7)\n"
    "  v_mfma_f32_16x16x32_bf16 v[104:107], v[88:91], v[92:95], v[104:107]\n"
    "  ds_read_b64_tr_b16 v[88:89], v10 offset:2696\n"
    "  v_mfma_f32_16x16x32_bf16 v[116:119], v[96:99], v[100:103], v[116:119]\n"
    "  ds_read_b64_tr_b16 v[90:91], v10 offset:2688\n"
    "  v_mfma_f32_16x16x32_bf16 v[112:115], v[96:99], v[56:59], v[112:115]\n"
    "  ds_read_b128 v[100:103], v11 offset:1088\n"
    "  v_mfma_f32_16x16x32_bf16 v[28:31], v[96:99], v[60:63], v[28:31]\n"
    "  ds_read_b64_tr_b16 v[56:57], v10 offset:2184\n"
    "  v_mfma_f32_16x16x32_bf16 v[24:27], v[96:99], v[68:71], v[24:27]\n"
    "  ds_read_b64_tr_b16 v[58:59], v10 offset:2176\n"
    "  v_mfma_f32_16x16x32_bf16 v[20:23], v[96:99], v[52:55], v[20:23]\n"
    "  ds_read_b128 v[60:63], v11 offset:1600\n"
    "  v_mfma_f32_16x16x32_bf16 v[16:19], v[96:99], v[84:87], v[16:19]\n"
    "  ds_read_b64_tr_b16 v[68:69], v10 offset:1672\n"
    "  v_mfma_f32_16x16x32_bf16 v[108:111], v[96:99], v[92:95], v[108:111]\n"
    "  ds_read_b64_tr_b16 v[70:71], v10 offset:1664\n"
    "  s_waitcnt lgkmcnt(8)\n"
    "  v_mfma_f32_16x16x32_bf16 v[104:107], v[96:99], v[48:51], v[104:107]\n"
    "  ds_read_b128 v[52:55], v11 offset:2112\n"
    "  v_mfma_f32_16x16x32_bf16 v[104:107], v[80:83], v[64:67], v[104:107]\n"
    "  ds_read_b64_tr_b16 v[84:85], v10 offset:1160\n"
    "  v_mfma_f32_16x16x32_bf16 v[108:111], v[72:75], v[64:67], v[108:111]\n"
    "  ds_read_b64_tr_b16 v[86:87], v10 offset:1152\n"
    "  v_mfma_f32_16x16x32_bf16 v[104:107], v[72:75], v[76:79], v[104:107]\n"
    "  ds_read_b128 v[92:95], v11 offset:2624\n"
    "  s_waitcnt lgkmcnt(7)\n"
    "  v_mfma_f32_16x16x32_bf16 v[16:19], v[88:91], v[64:67], v[16:19]\n"
    "  ds_read_b64_tr_b16 v[96:97], v10 offset:648\n"
    "  v_mfma_f32_16x16x32_bf16 v[108:111], v[88:91], v[76:79], v[108:111]\n"
    "  ds_read_b64_tr_b16 v[98:99], v10 offset:640\n"
    "  v_mfma_f32_16x16x32_bf16 v[104:107], v[88:91], v[100:103], v[104:107]\n"
    "  ds_read_b64 v[124:125], v12 offset:1536\n"
    "  v_mfma_f32_16x16x32_bf16 v[20:23], v[56:59], v[64:67], v[20:23]\n"
    "  ds_read_b64 v[126:127], v12 offset:1024\n"
    "  v_mfma_f32_16x16x32_bf16 v[16:19], v[56:59], v[76:79], v[16:19]\n"
    "  ds_read_b128 v[48:51], v11 offset:3136\n"
    "  v_mfma_f32_16x16x32_bf16 v[108:111], v[56:59], v[100:103], v[108:111]\n"
    "  ds_read_b64_tr_b16 v[80:81], v10 offset:136\n"
    "  s_waitcnt lgkmcnt(10)\n"
    "  v_mfma_f32_16x16x32_bf16 v[104:107], v[56:59], v[60:63], v[104:107]\n"
    "  ds_read_b64_tr_b16 v[82:83], v10 offset:128\n"
    "  v_mfma_f32_16x16x32_bf16 v[24:27], v[68:71], v[64:67], v[24:27]\n"
    "  ds_read_b64 v[120:121], v12 offset:2560\n"
    "  v_mfma_f32_16x16x32_bf16 v[20:23], v[68:71], v[76:79], v[20:23]\n"
    "  ds_read_b64 v[122:123], v12 offset:2048\n"
    "  s_waitcnt vmcnt(1)\n"
    "  v_perm_b32 v40, v40, v40, s54\n"
    "  v_mfma_f32_16x16x32_bf16 v[16:19], v[68:71], v[100:103], v[16:19]\n"
    "  v_perm_b32 v41, v41, v41, s54\n"
    "  v_perm_b32 v42, v42, v42, s54\n"
    "  v_mfma_f32_16x16x32_bf16 v[108:111], v[68:71], v[60:63], v[108:111]\n"
    "  v_perm_b32 v43, v43, v43, s54\n"
    "  s_waitcnt lgkmcnt(6)\n"
    "  v_lshlrev_b32 v15, 16, v124\n"
    "  v_mfma_f32_16x16x32_bf16 v[104:107], v[68:71], v[52:55], v[104:107]\n"
    "  v_fmac_f32 v40, v15, v3\n"
    "  v_and_b32 v15, 0xffff0000, v124\n"
    "  v_mfma_f32_16x16x32_bf16 v[28:31], v[84:87], v[64:67], v[28:31]\n"
    "  ds_read_b128 v[72:75], v11 offset:3648\n"
    "  v_fmac_f32 v41, v15, v3\n"
    "  v_mfma_f32_16x16x32_bf16 v[24:27], v[84:87], v[76:79], v[24:27]\n"
    "  ds_read_b64_tr_b16 v[88:89], v10 offset:3784\n"
    "  v_lshlrev_b32 v15, 16, v125\n"
    "  v_mfma_f32_16x16x32_bf16 v[20:23], v[84:87], v[100:103], v[20:23]\n"
    "  ds_read_b64_tr_b16 v[90:91], v10 offset:3776\n"
    "  v_fmac_f32 v42, v15, v3\n"
    "  v_mfma_f32_16x16x32_bf16 v[16:19], v[84:87], v[60:63], v[16:19]\n"
    "  ds_read_b128 v[56:59], v11\n"
    "  v_and_b32 v15, 0xffff0000, v125\n"
    "  v_mfma_f32_16x16x32_bf16 v[108:111], v[84:87], v[52:55], v[108:111]\n"
    "  ds_read_b64_tr_b16 v[68:69], v10 offset:3272\n"
    "  v_fmac_f32 v43, v15, v3\n"
    "  v_mfma_f32_16x16x32_bf16 v[104:107], v[84:87], v[92:95], v[104:107]\n"
    "  ds_read_b64_tr_b16 v[70:71], v10 offset:3264\n"
    "  s_waitcnt vmcnt(0)\n"
    "  v_perm_b32 v44, v44, v44, s54\n"
    "  v_mfma_f32_16x16x32_bf16 v[112:115], v[96:99], v[64:67], v[112:115]\n"
    "  ds_read_b128 v[84:87], v11 offset:512\n"
    "  v_perm_b32 v45, v45, v45, s54\n"
    "  v_mfma_f32_16x16x32_bf16 v[28:31], v[96:99], v[76:79], v[28:31]\n"
    "  v_perm_b32 v46, v46, v46, s54\n"
    "  v_perm_b32 v47, v47, v47, s54\n"
    "  v_mfma_f32_16x16x32_bf16 v[24:27], v[96:99], v[100:103], v[24:27]\n"
    "  s_waitcnt lgkmcnt(12)\n"
    "  v_lshlrev_b32 v15, 16, v126\n"
    "  v_fmac_f32 v44, v15, v3\n"
    "  v_mfma_f32_16x16x32_bf16 v[20:23], v[96:99], v[60:63], v[20:23]\n"
    "  v_and_b32 v15, 0xffff0000, v126\n"
    "  v_fmac_f32 v45, v15, v3\n"
    "  v_mfma_f32_16x16x32_bf16 v[16:19], v[96:99], v[52:55], v[16:19]\n"
    "  v_lshlrev_b32 v15, 16, v127\n"
    "  v_fmac_f32 v46, v15, v3\n"
    "  v_mfma_f32_16x16x32_bf16 v[108:111], v[96:99], v[92:95], v[108:111]\n"
    "  v_and_b32 v15, 0xffff0000, v127\n"
    "  v_fmac_f32 v47, v15, v3\n"
    "  s_waitcnt lgkmcnt(9)\n"
    "  v_mfma_f32_16x16x32_bf16 v[104:107], v[96:99], v[48:51], v[104:107]\n"
    "  ds_read_b64_tr_b16 v[96:97], v10 offset:2760\n"
    "  v_perm_b32 v32, v32, v32, s54\n"
    "  v_mfma_f32_16x16x32_bf16 v[116:119], v[80:83], v[64:67], v[116:119]\n"
    "  ds_read_b64_tr_b16 v[98:99], v10 offset:2752\n"
    "  v_perm_b32 v33, v33, v33, s54\n"
    "  v_mfma_f32_16x16x32_bf16 v[112:115], v[80:83], v[76:79], v[112:115]\n"
    "  ds_read_b128 v[64:67], v11 offset:1024\n"
    "  v_perm_b32 v34, v34, v34, s54\n"
    "  v_mfma_f32_16x16x32_bf16 v[28:31], v[80:83], v[100:103], v[28:31]\n"
    "  ds_read_b64_tr_b16 v[76:77], v10 offset:2248\n"
    "  v_perm_b32 v35, v35, v35, s54\n"
    "  v_mfma_f32_16x16x32_bf16 v[24:27], v[80:83], v[60:63], v[24:27]\n"
    "  ds_read_b64_tr_b16 v[78:79], v10 offset:2240\n"
    "  s_waitcnt lgkmcnt(13)\n"
    "  v_lshlrev_b32 v15, 16, v120\n"
    "  v_mfma_f32_16x16x32_bf16 v[20:23], v[80:83], v[52:55], v[20:23]\n"
    "  ds_read_b128 v[100:103], v11 offset:1536\n"
    "  v_fmac_f32 v32, v15, v3\n"
    "  v_mfma_f32_16x16x32_bf16 v[16:19], v[80:83], v[92:95], v[16:19]\n"
    "  ds_read_b64_tr_b16 v[60:61], v10 offset:1736\n"
    "  v_and_b32 v15, 0xffff0000, v120\n"
    "  v_mfma_f32_16x16x32_bf16 v[108:111], v[80:83], v[48:51], v[108:111]\n"
    "  ds_read_b64_tr_b16 v[62:63], v10 offset:1728\n"
    "  v_fmac_f32 v33, v15, v3\n"
    "  s_waitcnt lgkmcnt(9)\n"
    "  v_mfma_f32_16x16x32_bf16 v[104:107], v[80:83], v[72:75], v[104:107]\n"
    "  ds_read_b128 v[52:55], v11 offset:2048\n"
    "  v_lshlrev_b32 v15, 16, v121\n"
    "  v_mfma_f32_16x16x32_bf16 v[104:107], v[88:91], v[56:59], v[104:107]\n"
    "  ds_read_b64_tr_b16 v[92:93], v10 offset:200\n"
    "  v_fmac_f32 v34, v15, v3\n"
    "  v_mfma_f32_16x16x32_bf16 v[108:111], v[68:71], v[56:59], v[108:111]\n"
    "  ds_read_b64_tr_b16 v[94:95], v10 offset:192\n"
    "  v_and_b32 v15, 0xffff0000, v121\n"
    "  s_waitcnt lgkmcnt(8)\n"
    "  v_mfma_f32_16x16x32_bf16 v[104:107], v[68:71], v[84:87], v[104:107]\n"
    "  ds_read_b64_tr_b16 v[48:49], v10 offset:712\n"
    "  v_fmac_f32 v35, v15, v3\n"
    "  v_mfma_f32_16x16x32_bf16 v[108:111], v[96:99], v[84:87], v[108:111]\n"
    "  ds_read_b64_tr_b16 v[50:51], v10 offset:704\n"
    "  v_perm_b32 v36, v36, v36, s54\n"
    "  v_mfma_f32_16x16x32_bf16 v[104:107], v[96:99], v[64:67], v[104:107]\n"
    "  ds_read_b64_tr_b16 v[80:81], v10 offset:1224\n"
    "  v_perm_b32 v37, v37, v37, s54\n"
    "  s_waitcnt lgkmcnt(6)\n"
    "  v_mfma_f32_16x16x32_bf16 v[108:111], v[76:79], v[64:67], v[108:111]\n"
    "  ds_read_b64_tr_b16 v[82:83], v10 offset:1216\n"
    "  v_perm_b32 v38, v38, v38, s54\n"
    "  v_mfma_f32_16x16x32_bf16 v[104:107], v[76:79], v[100:103], v[104:107]\n"
    "  ds_read_b128 v[72:75], v11 offset:2560\n"
    "  v_perm_b32 v39, v39, v39, s54\n"
    "  v_mfma_f32_16x16x32_bf16 v[108:111], v[60:63], v[100:103], v[108:111]\n"
    "  ds_read_b128 v[88:91], v11 offset:3072\n"
    "  v_lshlrev_b32 v15, 16, v122\n"
    "  s_waitcnt lgkmcnt(6)\n"
    "  v_mfma_f32_16x16x32_bf16 v[104:107], v[60:63], v[52:55], v[104:107]\n"
    "  ds_read_b128 v[68:71], v11 offset:3584\n"
    "  v_fmac_f32 v36, v15, v3\n"
    "  v_mfma_f32_16x16x32_bf16 v[116:119], v[92:95], v[56:59], v[116:119]\n"
    "  v_and_b32 v15, 0xffff0000, v122\n"
    "  v_fmac_f32 v37, v15, v3\n"
    "  s_waitcnt lgkmcnt(5)\n"
    "  v_mfma_f32_16x16x32_bf16 v[112:115], v[48:51], v[56:59], v[112:115]\n"
    "  v_lshlrev_b32 v15, 16, v123\n"
    "  v_fmac_f32 v38, v15, v3\n"
    "  v_mfma_f32_16x16x32_bf16 v[112:115], v[92:95], v[84:87], v[112:115]\n"
    "  v_and_b32 v15, 0xffff0000, v123\n"
    "  v_fmac_f32 v39, v15, v3\n"
    "  s_waitcnt lgkmcnt(3)\n"
    "  v_mfma_f32_16x16x32_bf16 v[108:111], v[80:83], v[52:55], v[108:111]\n"
    "  s_waitcnt lgkmcnt(2)\n"
    "  v_mfma_f32_16x16x32_bf16 v[108:111], v[48:51], v[72:75], v[108:111]\n"
    "  ds_write_b128 v14, v[116:119] offset:3072\n"
    "  s_waitcnt lgkmcnt(2)\n"
    "  v_mfma_f32_16x16x32_bf16 v[108:111], v[92:95], v[88:91], v[108:111]\n"
    "  v_mfma_f32_16x16x32_bf16 v[104:107], v[80:83], v[72:75], v[104:107]\n"
    "  ds_write_b128 v14, v[112:115] offset:2048\n"
    "  v_mfma_f32_16x16x32_bf16 v[104:107], v[48:51], v[88:91], v[104:107]\n"
    "  s_waitcnt lgkmcnt(2)\n"
    "  v_mfma_f32_16x16x32_bf16 v[104:107], v[92:95], v[68:71], v[104:107]\n"
    "  v_mfma_f32_16x16x32_bf16 v[16:19], v[96:99], v[56:59], v[16:19]\n"
    "  s_nop 1\n"
    "  ds_write_b128 v14, v[108:111] offset:1024\n"
    "  v_mfma_f32_16x16x32_bf16 v[20:23], v[76:79], v[56:59], v[20:23]\n"
    "  v_mfma_f32_16x16x32_bf16 v[16:19], v[76:79], v[84:87], v[16:19]\n"
    "  v_mfma_f32_16x16x32_bf16 v[24:27], v[60:63], v[56:59], v[24:27]\n"
    "  s_nop 0\n"
    "  ds_write_b128 v14, v[104:107]\n"
    "  v_mfma_f32_16x16x32_bf16 v[20:23], v[60:63], v[84:87], v[20:23]\n"
    "  v_mfma_f32_16x16x32_bf16 v[16:19], v[60:63], v[64:67], v[16:19]\n"
    "  v_mfma_f32_16x16x32_bf16 v[28:31], v[80:83], v[56:59], v[28:31]\n"
    "  v_xor_b32 v14, 4096, v14\n"
    "  s_waitcnt lgkmcnt(0)\n"
    "  s_barrier\n"
    "  ds_read_b128 v[104:107], v14\n"
    "  v_mfma_f32_16x16x32_bf16 v[24:27], v[80:83], v[84:87], v[24:27]\n"
    "  ds_read_b128 v[108:111], v14 offset:1024\n"
    "  v_mfma_f32_16x16x32_bf16 v[20:23], v[80:83], v[64:67], v[20:23]\n"
    "  ds_read_b128 v[112:115], v14 offset:2048\n"
    "  v_mfma_f32_16x16x32_bf16 v[16:19], v[80:83], v[100:103], v[16:19]\n"
    "  ds_read_b128 v[116:119], v14 offset:3072\n"
    "  v_mfma_f32_16x16x32_bf16 v[28:31], v[48:51], v[84:87], v[28:31]\n"
    "  v_mfma_f32_16x16x32_bf16 v[24:27], v[48:51], v[64:67], v[24:27]\n"
    "  s_waitcnt lgkmcnt(3)\n"
    "  v_add_f32 v32, v32, v104\n"
    "  v_add_f32 v33, v33, v105\n"
    "  v_mfma_f32_16x16x32_bf16 v[20:23], v[48:51], v[100:103], v[20:23]\n"
    "  v_add_f32 v34, v34, v106\n"
    "  v_add_f32 v35, v35, v107\n"
    "  v_mfma_f32_16x16x32_bf16 v[16:19], v[48:51], v[52:55], v[16:19]\n"
    "  s_waitcnt lgkmcnt(2)\n"
    "  v_add_f32 v36, v36, v108\n"
    "  v_add_f32 v37, v37, v109\n"
    "  v_mfma_f32_16x16x32_bf16 v[28:31], v[92:95], v[64:67], v[28:31]\n"
    "  v_add_f32 v38, v38, v110\n"
    "  v_add_f32 v39, v39, v111\n"
    "  v_mfma_f32_16x16x32_bf16 v[24:27], v[92:95], v[100:103], v[24:27]\n"
    "  s_waitcnt lgkmcnt(1)\n"
    "  v_add_f32 v40, v40, v112\n"
    "  v_add_f32 v41, v41, v113\n"
    "  v_mfma_f32_16x16x32_bf16 v[20:23], v[92:95], v[52:55], v[20:23]\n"
    "  v_add_f32 v42, v42, v114\n"
    "  v_add_f32 v43, v43, v115\n"
    "  v_mfma_f32_16x16x32_bf16 v[16:19], v[92:95], v[72:75], v[16:19]\n"
    "  s_waitcnt lgkmcnt(0)\n"
    "  v_add_f32 v44, v44, v116\n"
    "  v_add_f32 v45, v45, v117\n"
    "  v_add_f32 v46, v46, v118\n"
    "  v_add_f32 v47, v47, v119\n"
    "Lbj_96%=:\n"
    "  v_pk_add_f32 v[24:25], v[24:25], v[40:41]\n"
    "  v_pk_add_f32 v[26:27], v[26:27], v[42:43]\n"
    "  v_pk_add_f32 v[28:29], v[28:29], v[44:45]\n"
    "  v_pk_add_f32 v[30:31], v[30:31], v[46:47]\n"
    "  v_cvt_pk_bf16_f32 v52, v24, v25\n"
    "  v_cvt_pk_bf16_f32 v53, v26, v27\n"
    "  v_cvt_pk_bf16_f32 v54, v28, v29\n"
    "  v_cvt_pk_bf16_f32 v55, v30, v31\n"
    "  s_nop 0\n"
    "  v_permlane16_swap_b32 v52, v54\n"
    "  v_permlane16_swap_b32 v53, v55\n"
    "  buffer_store_dwordx4 v[52:55], v13, s[16:19], s33 offen sc1\n"
    "  v_pk_add_f32 v[16:17], v[16:17], v[32:33]\n"
    "  v_pk_add_f32 v[18:19], v[18:19], v[34:35]\n"
    "  v_pk_add_f32 v[20:21], v[20:21], v[36:37]\n"
    "  v_pk_add_f32 v[22:23], v[22:23], v[38:39]\n"
    "  v_cvt_pk_bf16_f32 v48, v16, v17\n"
    "  v_cvt_pk_bf16_f32 v49, v18, v19\n"
    "  v_cvt_pk_bf16_f32 v50, v20, v21\n"
    "  v_cvt_pk_bf16_f32 v51, v22, v23\n"
    "  s_nop 0\n"
    "  v_permlane16_swap_b32 v48, v50\n"
    "  v_permlane16_swap_b32 v49, v51\n"
    "  buffer_store_dwordx4 v[48:51], v13, s[16:19], s32 offen sc1\n"
    "  v_lshlrev_b32 v10, 4, v0\n"
    "  s_lshl_b32 s67, s41, 16\n"
    "  s_lshl_b32 s66, s42, 13\n"
    "  s_add_u32 s66, s67, s66\n"
    "  s_add_u32 s48, s66, 0x1200000\n"
    "  s_add_u32 s49, s48, 4096\n"
    "  s_sleep 14\n"
    "  buffer_load_dwordx4 v[16:19], v10, s[16:19], s48 offen sc1\n"
    "  buffer_load_dwordx4 v[20:23], v10, s[16:19], s49 offen sc1\n"
    "  v_lshrrev_b32 v14, 3, v0\n"
    "  v_and_b32 v13, 7, v0\n"
    "  v_lshlrev_b32 v13, 4, v13\n"
    "  v_bfe_u32 v12, v14, 1, 1\n"
    "  v_lshlrev_b32 v12, 1, v12\n"
    "  v_bfe_u32 v11, v14, 3, 1\n"
    "  v_add_lshl_u32 v12, v12, v11, 5\n"
    "  v_xor_b32 v13, v13, v12\n"
    "  v_lshl_add_u32 v15, v14, 7, v13\n"
    "  v_and_b32 v14, 3, v1\n"
    "  v_lshrrev_b32 v13, 4, v1\n"
    "  v_bfe_u32 v12, v1, 2, 2\n"
    "  v_lshl_add_u32 v11, v13, 3, v12\n"
    "  v_lshrrev_b32 v12, 1, v12\n"
    "  v_and_b32 v13, 1, v13\n"
    "  v_lshl_add_u32 v12, v12, 1, v13\n"
    "  v_xor_b32 v12, s40, v12\n"
    "  v_lshlrev_b32 v12, 5, v12\n"
    "  v_lshl_add_u32 v11, v11, 7, v12\n"
    "  v_lshl_add_u32 v24, v14, 3, v11\n"
    "  v_lshrrev_b32 v14, 4, v1\n"
    "  v_and_b32 v13, 15, v1\n"
    "  s_lshl_b32 s67, s41, 6\n"
    "  s_lshl4_add_u32 s67, s40, s67\n"
    "  s_lshl_b32 s67, s67, 10\n"
    "  s_lshl_b32 s66, s42, 7\n"
    "  s_add_u32 s67, s67, s66\n"
    "  v_lshl_add_u32 v13, v13, 10, s67\n"
    "  v_lshl_add_u32 v25, v14, 4, v13\n"
    "  s_waitcnt lgkmcnt(0)\n"
    "  s_barrier\n"
    "  s_waitcnt vmcnt(0)\n"
    "  s_mov_b32 s55, 0x00008000\n"
    "Lc_97%=:\n"
    "  v_max_u32 v26, v16, v20\n"
    "  v_cmp_eq_u32 vcc, -1, v26\n"
    "  s_cbranch_vccz Lo_98%=\n"
    "  s_sub_u32 s55, s55, 1\n"
    "  s_cbranch_scc1 Lo_98%=\n"
    "  s_sleep 2\n"
    "  s_mov_b64 exec, vcc\n"
    "  buffer_load_dwordx4 v[16:19], v10, s[16:19], s48 offen sc1\n"
    "  buffer_load_dwordx4 v[20:23], v10, s[16:19], s49 offen sc1\n"
    "  s_mov_b64 exec, -1\n"
    "  s_waitcnt vmcnt(0)\n"
    "  s_branch Lc_97%=\n"
    "Lo_98%=:\n"
    "  ds_write_b128 v15, v[16:19]\n"
    "  ds_write_b128 v15, v[20:23] offset:4096\n"
    "  s_waitcnt lgkmcnt(0)\n"
    "  s_barrier\n"
    "  ds_read_b64_tr_b16 v[28:29], v24\n"
    "  ds_read_b64_tr_b16 v[30:31], v24 offset:512\n"
    "  ds_read_b64_tr_b16 v[32:33], v24 offset:4096\n"
    "  ds_read_b64_tr_b16 v[34:35], v24 offset:4608\n"
    "  s_waitcnt lgkmcnt(2)\n"
    "  buffer_store_dwordx4 v[28:31], v25, s[20:23], 0 offen sc0 sc1 nt\n"
    "  s_waitcnt lgkmcnt(0)\n"
    "  buffer_store_dwordx4 v[32:35], v25, s[20:23], 0 offen offset:64 sc0 sc1 nt\n"
    "Lend%=:\n"
    "  s_or_b32 s52, s2, s40\n"
    "  s_cmp_eq_u32 s52, 0\n"
    "  s_cselect_b64 exec, 1, 0\n"
    "  v_mov_b32 v10, s46\n"
    "  buffer_store_dword v10, off, s[16:19], 0 sc1\n"
    "  s_waitcnt vmcnt(0)\n"
    "Lfin%=:\n"
        :
        : "{s[0:1]}"(__builtin_amdgcn_kernarg_segment_ptr()), "{s2}"(blockIdx.x), "{s3}"(ldsbase), "{v0}"(threadIdx.x)
        :
          "v1", "v2", "v3", "v4", "v5", "v6", "v7", "v8", "v9", "v10", "v11", "v12", "v13", "v14", "v15", "v16", "v17", "v18", "v19", "v20", "v21", "v22", "v23", "v24",
          "v25", "v26", "v27", "v28", "v29", "v30", "v31", "v32", "v33", "v34", "v35", "v36", "v37", "v38", "v39", "v40", "v41", "v42", "v43", "v44", "v45", "v46", "v47", "v48",
          "v49", "v50", "v51", "v52", "v53", "v54", "v55", "v56", "v57", "v58", "v59", "v60", "v61", "v62", "v63", "v64", "v65", "v66", "v67", "v68", "v69", "v70", "v71", "v72",
          "v73", "v74", "v75", "v76", "v77", "v78", "v79", "v80", "v81", "v82", "v83", "v84", "v85", "v86", "v87", "v88", "v89", "v90", "v91", "v92", "v93", "v94", "v95", "v96",
          "v97", "v98", "v99", "v100", "v101", "v102", "v103", "v104", "v105", "v106", "v107", "v108", "v109", "v110", "v111", "v112", "v113", "v114", "v115", "v116", "v117", "v118", "v119", "v120",
          "v121", "v122", "v123", "v124", "v125", "v126", "v127", "v128", "v129", "v130", "v131", "v132", "v133", "v134", "v135", "v136", "v137", "v138", "v139", "v140", "v141", "v142", "v143", "v144",
          "v145", "v146", "v147", "v148", "v149", "v150", "v151", "v152", "v153", "v154", "v155", "v156", "v157", "v158", "v159", "v160", "v161", "v162", "v163", "v164", "v165", "v166", "v167", "v168",
          "v169", "v170", "v171", "v172", "v173", "v174", "v175", "v176", "v177", "v178", "v179", "v180", "v181", "v182", "v183", "v184", "v185", "v186", "v187", "v188", "v189", "v190", "v191", "v192",
          "v193", "v194", "v195", "v196", "v197", "v198", "v199", "v200", "v201", "v202", "v203", "v204", "v205", "v206", "v207", "v208", "v209", "v210", "v211", "v212", "v213", "v214", "v215", "v216",
          "v217", "v218", "v219", "v220", "v221", "v222", "v223", "v224", "v225", "v226", "v227", "v228", "v229", "v230", "v231", "v232", "v233", "v234", "v235", "v236", "v237", "v238", "v239", "v240",
          "v241", "v242", "v243", "v244", "v245", "v246", "v247", "v248", "v249", "v250", "v251", "v252", "v253", "v254", "v255", "a0", "a1", "a2", "a3", "a4", "a5", "a6", "a7", "a8",
          "a9", "a10", "a11", "a12", "a13", "a14", "a15", "a16", "a17", "a18", "a19", "a20", "a21", "a22", "a23", "a24", "a25", "a26", "a27", "a28", "a29", "a30", "a31", "a32",
          "a33", "a34", "a35", "a36", "a37", "a38", "a39", "a40", "a41", "a42", "a43", "a44", "a45", "a46", "a47", "a48", "a49", "a50", "a51", "a52", "a53", "a54", "a55", "a56",
          "a57", "a58", "a59", "a60", "a61", "a62", "a63", "a64", "a65", "a66", "a67", "a68", "a69", "a70", "a71", "a72", "a73", "a74", "a75", "a76", "a77", "a78", "a79", "a80",
          "a81", "a82", "a83", "a84", "a85", "a86", "a87", "a88", "a89", "a90", "a91", "a92", "a93", "a94", "a95", "a96", "a97", "a98", "a99", "a100", "a101", "a102", "a103", "a104",
          "a105", "a106", "a107", "a108", "a109", "a110", "a111", "a112", "a113", "a114", "a115", "a116", "a117", "a118", "a119", "a120", "a121", "a122", "a123", "a124", "a125", "a126", "a127", "a128",
          "a129", "a130", "a131", "a132", "a133", "a134", "a135", "a136", "a137", "a138", "a139", "a140", "a141", "a142", "a143", "a144", "a145", "a146", "a147", "a148", "a149", "a150", "a151", "a152",
          "a153", "a154", "a155", "a156", "a157", "a158", "a159", "a160", "a161", "a162", "a163", "a164", "a165", "a166", "a167", "a168", "a169", "a170", "a171", "a172", "a173", "a174", "a175", "a176",
          "a177", "a178", "a179", "a180", "a181", "a182", "a183", "a184", "a185", "a186", "a187", "a188", "a189", "a190", "a191", "a192", "a193", "a194", "a195", "a196", "a197", "a198", "a199", "a200",
          "a201", "a202", "a203", "a204", "a205", "a206", "a207", "a208", "a209", "a210", "a211", "a212", "a213", "a214", "a215", "a216", "a217", "a218", "a219", "a220", "a221", "a222", "a223", "a224",
          "a225", "a226", "a227", "a228", "a229", "a230", "a231", "a232", "a233", "a234", "a235", "a236", "a237", "a238", "a239", "a240", "a241", "a242", "a243", "a244", "a245", "a246", "a247", "a248",
          "a249", "a250", "a251", "a252", "a253", "a254", "a255", "s4", "s5", "s6", "s7", "s8", "s9", "s10", "s11", "s12", "s13", "s14", "s15", "s16", "s17", "s18", "s19", "s20",
          "s21", "s22", "s23", "s24", "s25", "s26", "s27", "s28", "s29", "s30", "s31", "s32", "s33", "s34", "s35", "s36", "s37", "s38", "s39", "s40", "s41", "s42", "s43", "s44",
          "s45", "s46", "s47", "s48", "s49", "s50", "s51", "s52", "s53", "s54", "s55", "s56", "s57", "s58", "s59", "s60", "s61", "s62", "s63", "s64", "s65", "s66", "s67", "s68",
          "s69", "s70", "s71", "s72", "s73", "s74", "s75", "s76", "s77", "s78", "s79", "s80", "s81", "s82", "s83", "s84", "s85", "s86", "s87", "s88", "s89", "s90", "s91", "s92",
          "s93", "s94", "s95", "s96", "s97", "s98", "s99", "s100", "s101", "vcc", "m0", "scc", "memory"
    );
#endif
}

extern "C" void kernel_launch(void* const* d_in, const int* in_sizes, int n_in, void* d_out, int out_size, void* d_ws, size_t ws_size,
                              hipStream_t stream) {
    fused_kernel<<<256, 256, 0, stream>>>((const uint16_t*)d_in[0], (const uint16_t*)d_in[1], (const uint16_t*)d_in[2],
                                          (const uint16_t*)d_in[4], (const uint16_t*)d_in[5], (const uint16_t*)d_in[6],
                                          (const uint16_t*)d_in[7], (const uint16_t*)d_in[8], (const uint16_t*)d_in[9],
                                          (const uint16_t*)d_in[10], (const uint16_t*)d_in[11], (uint16_t*)d_out,
                                          (unsigned char*)d_ws);
}
```
